```python
import torch

def _softmax(x, axis=-1):
    return torch.softmax(x, dim=axis)
def _gelu(x):
    return torch.nn.functional.gelu(x, approximate="tanh")
def _silu(x):
    return torch.nn.functional.silu(x)
def _sigmoid(x):
    return torch.sigmoid(x)
def _layernorm(x, axis=-1, eps=1e-5):
    m = x.mean(dim=axis, keepdim=True)
    v = x.var(dim=axis, keepdim=True, unbiased=False)
    return (x - m) / torch.sqrt(v + eps)
def _scatter_add(tgt, idx, src, axis=0):
    return tgt.clone().index_add_(axis, idx, src)
def _segment_sum(src, seg, n):
    out = torch.zeros((n,) + src.shape[1:], dtype=src.dtype)
    return out.index_add_(0, seg, src)
def _pad(x, pw):
    flat = tuple(v for pair in reversed(pw) for v in pair)
    return torch.nn.functional.pad(x, flat)

kernel_name = 'interleave_layers__softmax_full_mha__mixer_token_mlp__prompt'

def setup_inputs():

    _w = (lambda a: (lambda t: t.to(torch.bfloat16) if t.is_floating_point() else t)(torch.as_tensor(a)))
    def _A_setup():
        B, S, D = 1, 2048, 512
        return {
            "x": _w((lambda *s: torch.randn(*s, dtype=torch.bfloat16) * 0.05)(B, S, D)),
            "wqkv": _w((lambda *s: torch.randn(*s, dtype=torch.bfloat16) * 0.05)(D, 3 * D)),
            "wo": _w((lambda *s: torch.randn(*s, dtype=torch.bfloat16) * 0.05)(D, D)),
        }
    _A_in = _A_setup()

    def _B_setup():
        B, S, D, F = 1, 2048, 512, 1792
        return {
            "x": _w((lambda *s: torch.randn(*s, dtype=torch.bfloat16) * 0.05)(B, S, D)),
            "w1": _w((lambda *s: torch.randn(*s, dtype=torch.bfloat16) * 0.8)(S, F) * (S ** -0.5)),
            "b1": _w((lambda *s: torch.randn(*s, dtype=torch.bfloat16) * 12.8)(F) * 0.02),
            "w2": _w((lambda *s: torch.randn(*s, dtype=torch.bfloat16) * 0.2)(F, S) * (F ** -0.5)),
            "b2": _w((lambda *s: torch.randn(*s, dtype=torch.bfloat16) * 3.2)(S) * 0.02),
        }
    _B_in = _B_setup()

    return {
        "A_x": _A_in["x"],
        "A_wqkv": _A_in["wqkv"],
        "A_wo": _A_in["wo"],
        "B_x": _B_in["x"],
        "B_w1": _B_in["w1"],
        "B_b1": _B_in["b1"],
        "B_w2": _B_in["w2"],
        "B_b2": _B_in["b2"],
    }

def reference(A_x, A_wqkv, A_wo, B_x, B_w1, B_b1, B_w2, B_b2):
    def _A_ref(x, wqkv, wo):
        B, S, D = x.shape
        H = 32; Dh = D // H
        qkv = (lambda x, *d: x.permute(*d))((x @ wqkv).reshape(B, S, 3, H, Dh), 2, 0, 3, 1, 4)
        q, k, v = qkv[0], qkv[1], qkv[2]
        scale = Dh ** -0.5
        scores = torch.einsum("bhqd,bhkd->bhqk", q, k) * scale
        p = _softmax(scores, axis=-1)
        o = torch.einsum("bhqk,bhkd->bhqd", p, v)
        return (lambda x, *d: x.permute(*d))(o, 0, 2, 1, 3).reshape(B, S, D) @ wo

    def _B_ref(x, w1, b1, w2, b2):
        B, S, D = x.shape
        h = _layernorm(x, axis=-1)
        u = torch.einsum("bsd,sf->bdf", h, w1) + b1
        u = _gelu(u)
        y = torch.einsum("bdf,fs->bds", u, w2) + b2
        return (lambda x, *d: x.permute(*d))(y, 0, 2, 1)

    x = A_x
    x = x + _A_ref(_layernorm(x, axis=-1), A_wqkv, A_wo)
    x = x + _B_ref(_layernorm(x, axis=-1), B_w1, B_b1, B_w2, B_b2)
    x = x + _A_ref(_layernorm(x, axis=-1), A_wqkv, A_wo)
    x = x + _B_ref(_layernorm(x, axis=-1), B_w1, B_b1, B_w2, B_b2)
    return x
```

```cpp
#include <hip/hip_runtime.h>
#include <hip/hip_bf16.h>
#include <stdint.h>

typedef unsigned short u16;
typedef short bf16x8 __attribute__((ext_vector_type(8)));
typedef float f32x16 __attribute__((ext_vector_type(16)));
constexpr int S = 2048, D = 512, H = 32, DH = 16, F = 1792;

__device__ __forceinline__ float bf2f(u16 v) { return __uint_as_float((unsigned)v << 16); }
__device__ __forceinline__ u16 f2bf(float f) { __hip_bfloat16 b = __float2bfloat16(f); return *(u16*)&b; }

__global__ void ln_kernel(const u16* x, u16* y, int twice) {
    int row = blockIdx.x, lane = threadIdx.x;
    float v[8]; float s = 0.f;
    for (int i = 0; i < 8; ++i) { v[i] = bf2f(x[row * D + lane * 8 + i]); s += v[i]; }
    for (int o = 32; o; o >>= 1) s += __shfl_xor(s, o);
    float m = s / D, q = 0.f;
    for (int i = 0; i < 8; ++i) { v[i] -= m; q += v[i] * v[i]; }
    for (int o = 32; o; o >>= 1) q += __shfl_xor(q, o);
    float var = q / D, r = rsqrtf(var + 1e-5f);
    if (twice) r *= rsqrtf(var * r * r + 1e-5f);
    for (int i = 0; i < 8; ++i) y[row * D + lane * 8 + i] = f2bf(v[i] * r);
}

template <int MODE>
__global__ void gemm_kernel(const u16* A, int am, int ak, const u16* B, int bk, int bn, u16* C, int ldc, int K,
                            const u16* res, const u16* bias) {
    int lane = threadIdx.x & 63, w = threadIdx.x >> 6, r = lane & 31, h = lane >> 5;
    int m0 = blockIdx.y * 64 + (w >> 1) * 32, n0 = blockIdx.x * 64 + (w & 1) * 32;
    f32x16 acc = {};
    for (int k0 = 0; k0 < K; k0 += 16) {
        bf16x8 a, b;
        for (int j = 0; j < 8; ++j) { a[j] = A[(size_t)(m0 + r) * am + (size_t)(k0 + 8 * h + j) * ak]; b[j] = B[(size_t)(k0 + 8 * h + j) * bk + (size_t)(n0 + r) * bn]; }
        acc = __builtin_amdgcn_mfma_f32_32x32x16_bf16(a, b, acc, 0, 0, 0);
    }
    for (int i = 0; i < 16; ++i) {
        int m = m0 + (i & 3) + 8 * (i >> 2) + 4 * h, n = n0 + r;
        float v = acc[i];
        if (MODE == 1) v += bf2f(res[(size_t)m * ldc + n]);
        if (MODE == 2) { v += bf2f(bias[n]); float t = 0.7978845608f * (v + 0.044715f * v * v * v); v = 0.5f * v * (1.f + tanhf(t)); }
        if (MODE == 3) v += bf2f(res[(size_t)m * ldc + n]) + bf2f(bias[m]);
        C[(size_t)m * ldc + n] = f2bf(v);
    }
}

__global__ void attn_kernel(const u16* qkv, u16* o) {
    int h = blockIdx.y, q = blockIdx.x * 256 + threadIdx.x;
    __shared__ float kv[256][32];
    float qq[16], acc[16]; float m = -1e30f, l = 0.f;
    for (int d = 0; d < 16; ++d) { qq[d] = bf2f(qkv[(size_t)q * 1536 + h * 16 + d]) * 0.25f; acc[d] = 0.f; }
    for (int k0 = 0; k0 < S; k0 += 256) {
        __syncthreads();
        for (int d = 0; d < 16; ++d) { kv[threadIdx.x][d] = bf2f(qkv[(size_t)(k0 + threadIdx.x) * 1536 + 512 + h * 16 + d]); kv[threadIdx.x][16 + d] = bf2f(qkv[(size_t)(k0 + threadIdx.x) * 1536 + 1024 + h * 16 + d]); }
        __syncthreads();
        for (int k = 0; k < 256; ++k) {
            float s = 0.f;
            for (int d = 0; d < 16; ++d) s += qq[d] * kv[k][d];
            float mn = fmaxf(m, s), a = __expf(m - mn), p = __expf(s - mn);
            l = l * a + p; m = mn;
            for (int d = 0; d < 16; ++d) acc[d] = acc[d] * a + p * kv[k][16 + d];
        }
    }
    for (int d = 0; d < 16; ++d) o[(size_t)q * D + h * 16 + d] = f2bf(acc[d] / l);
}

extern "C" void kernel_launch(void* const* d_in, const int* in_sizes, int n_in, void* d_out, int out_size, void* d_ws, size_t ws_size, hipStream_t stream) {
    const u16* x0 = (const u16*)d_in[0]; const u16* wqkv = (const u16*)d_in[1]; const u16* wo = (const u16*)d_in[2];
    const u16* w1 = (const u16*)d_in[4]; const u16* b1 = (const u16*)d_in[5]; const u16* w2 = (const u16*)d_in[6]; const u16* b2 = (const u16*)d_in[7];
    u16* ws = (u16*)d_ws; u16* xn = ws; u16* qkv = xn + S * D; u16* o = qkv + S * 3 * D; u16* x1 = o + S * D; u16* u = x1 + S * D; u16* x2 = u + D * F;
    const u16* x = x0;
    for (int l = 0; l < 2; ++l) {
        ln_kernel<<<S, 64, 0, stream>>>(x, xn, 0);
        gemm_kernel<0><<<dim3(1536 / 64, S / 64), 256, 0, stream>>>(xn, D, 1, wqkv, 1536, 1, qkv, 1536, D, nullptr, nullptr);
        attn_kernel<<<dim3(S / 256, H), 256, 0, stream>>>(qkv, o);
        gemm_kernel<1><<<dim3(D / 64, S / 64), 256, 0, stream>>>(o, D, 1, wo, D, 1, x1, D, D, x, nullptr);
        ln_kernel<<<S, 64, 0, stream>>>(x1, xn, 1);
        gemm_kernel<2><<<dim3(F / 64, D / 64), 256, 0, stream>>>(xn, 1, D, w1, F, 1, u, F, S, nullptr, b1);
        u16* xo = l ? (u16*)d_out : x2;
        gemm_kernel<3><<<dim3(D / 64, S / 64), 256, 0, stream>>>(w2, 1, S, u, 1, F, xo, D, F, x1, b2);
        x = x2;
    }
}
```

```cpp
#include <hip/hip_runtime.h>
#include <stdint.h>

typedef unsigned short u16;
typedef unsigned u32;
typedef unsigned long long u64;
typedef __bf16 bf2 __attribute__((ext_vector_type(2)));
typedef __bf16 bf8 __attribute__((ext_vector_type(8)));
typedef __bf16 bf16v __attribute__((ext_vector_type(16)));
typedef short s4 __attribute__((ext_vector_type(4)));
typedef float f2 __attribute__((ext_vector_type(2)));
typedef float f4 __attribute__((ext_vector_type(4)));
typedef float f8 __attribute__((ext_vector_type(8)));
typedef float f16v __attribute__((ext_vector_type(16)));
typedef u32 u2 __attribute__((ext_vector_type(2)));
typedef u32 u4 __attribute__((ext_vector_type(4)));
#define LAS __attribute__((address_space(3)))
#define GAS __attribute__((address_space(1)))
#define DI __device__ __forceinline__
typedef LAS char* lptr;
typedef __amdgpu_buffer_rsrc_t rsrc_t;

constexpr int S = 2048, D = 512, F = 1792;
constexpr u32 MB = 1u << 20;
constexpr u32 WS_FLAGS = 0x1000, WS_STATS = 0xB0000, WS_L0 = 2 * MB, WS_LSTR = 12 * MB;
constexpr u32 O_Q = 0, O_K = 2 * MB, O_V = 4 * MB, O_O = 6 * MB, O_H = 8 * MB, O_U = 10 * MB, WS_X = 26 * MB, WS_END = 28 * MB;
constexpr int LDS_BYTES = 160 * 1024;
constexpr int SC1 = 16;
constexpr float EPS = 1e-5f;
constexpr u32 SPIN_CAP = 1u << 16;

struct Params { const u16 *x, *wqkv, *wo, *w1, *b1, *w2, *b2; u16* out; char* ws; };

#define RLX_AGENT __ATOMIC_RELAXED, __HIP_MEMORY_SCOPE_AGENT
#define MFMA(a, b, c) __builtin_amdgcn_mfma_f32_32x32x16_bf16(a, b, c, 0, 0, 0)
#define BAR() asm volatile("s_waitcnt lgkmcnt(0)\n\ts_barrier" ::: "memory")
#define VM_BAR(n) asm volatile("s_waitcnt vmcnt(" #n ") lgkmcnt(0)\n\ts_barrier" ::: "memory")

#define STAMP(i) do {} while (0)

DI u4 mk_srd(const void* p, u32 bytes) { const u64 a = (u64)p; return u4{(u32)a, (u32)(a >> 32) & 0xffffu, bytes, 0x00020000u}; }
DI rsrc_t mk_rsrc(const void* p, u32 bytes) { return __builtin_amdgcn_make_buffer_rsrc((void*)p, 0, bytes, 0x00020000); }
DI void dma16(u4 srd, u32 voff, u32 soff, u32 ldsaddr) {
    asm volatile("s_mov_b32 m0, %0\n\ts_nop 0\n\tbuffer_load_dwordx4 %1, %2, %3 offen lds" :: "s"(ldsaddr), "v"(voff), "s"(srd), "s"(soff) : "memory");
}
DI void dma16_sc1(u4 srd, u32 voff, u32 soff, u32 ldsaddr) {
    asm volatile("s_mov_b32 m0, %0\n\ts_nop 0\n\tbuffer_load_dwordx4 %1, %2, %3 offen sc1 lds" :: "s"(ldsaddr), "v"(voff), "s"(srd), "s"(soff) : "memory");
}
DI void store_wt(u4 v, rsrc_t rs, u32 off) { __builtin_amdgcn_raw_buffer_store_b128(v, rs, off, 0, SC1); }
DI bf8 as_bf8(u4 v) { return __builtin_bit_cast(bf8, v); }
DI bf8 tr_frag(lptr p0, lptr p1) {
    const s4 a = __builtin_amdgcn_ds_read_tr16_b64_v4i16((LAS s4*)p0), b = __builtin_amdgcn_ds_read_tr16_b64_v4i16((LAS s4*)p1);
    return __builtin_bit_cast(bf8, __builtin_shufflevector(a, b, 0, 1, 2, 3, 4, 5, 6, 7));
}
DI u32 pk2(float lo, float hi) { bf2 t = {(__bf16)lo, (__bf16)hi}; return __builtin_bit_cast(u32, t); }
DI u4 pk8(const float* v) { return u4{pk2(v[0], v[1]), pk2(v[2], v[3]), pk2(v[4], v[5]), pk2(v[6], v[7])}; }
DI float bflo(u32 v) { return __uint_as_float(v << 16); }
DI float bfhi(u32 v) { return __uint_as_float(v & 0xffff0000u); }
DI float halves_sum(float x) { auto t = __builtin_amdgcn_permlane32_swap(__float_as_uint(x), __float_as_uint(x), false, false); return __uint_as_float(t[0]) + __uint_as_float(t[1]); }
DI float halves_max(float x) { auto t = __builtin_amdgcn_permlane32_swap(__float_as_uint(x), __float_as_uint(x), false, false); return fmaxf(__uint_as_float(t[0]), __uint_as_float(t[1])); }
DI float dot2(u32 a, u32 b, float c) { return __builtin_amdgcn_fdot2_f32_bf16(__builtin_bit_cast(bf2, a), __builtin_bit_cast(bf2, b), c, false); }
DI f4 lds_f4(lptr p) { return *(LAS f4*)p; }
DI u4 lds_u4(lptr p) { return *(LAS u4*)p; }
DI u64 mix64(u64 z) { z += 0x9E3779B97F4A7C15ull; z = (z ^ (z >> 30)) * 0xBF58476D1CE4E5B9ull; z = (z ^ (z >> 27)) * 0x94D049BB133111EBull; return z ^ (z >> 31); }

struct Ctx {
    lptr lds; u32 lds0; int lane, w, r, hh; u64 tag; bool dead;
    DI void refresh() { int t = threadIdx.x; asm volatile("" : "+v"(t)); lane = t & 63; r = lane & 31; hh = lane >> 5; asm volatile("" : "+s"(w)); }
};
DI int opaque(int v) { asm volatile("" : "+s"(v)); return v; }

DI void publish(const Params& P, Ctx& c, int phase, int idx) {
    VM_BAR(0);
    if (threadIdx.x == 0) __hip_atomic_store((GAS u64*)(P.ws + WS_FLAGS + (phase * 256 + idx) * 256), c.tag, RLX_AGENT);
}
DI void await(const Params& P, Ctx& c, int phase, int first, int n) {
    if (c.w == 0 && !c.dead) {
        GAS u64* f = (GAS u64*)(P.ws + WS_FLAGS + (phase * 256 + first + (c.lane < n ? c.lane : 0)) * 256);
        u32 spins = 0;
        while (!__all(__hip_atomic_load(f, RLX_AGENT) == c.tag)) {
            if (++spins > SPIN_CAP) { c.dead = true; break; }
        }
    }
    VM_BAR(0);
}

constexpr int L_XCH = 98304, L_ST = 147456, L_CS = L_ST + 2048, L_ST2 = L_CS + 1024;
DI void prefetch_wqkv(const Params& P, Ctx& c, int p) {
    const u4 srd = mk_srd(P.wqkv, D * 3 * D * 2);
    const u32 voff = ((c.lane >> 2) * 1536 + (c.lane & 3) * 8 + p * 32) * 2;
#pragma unroll
    for (int g = 0; g < 4; ++g)
#pragma unroll
        for (int b = 0; b < 3; ++b) { const int ks = c.w * 4 + g; dma16(srd, voff, (ks * 16 * 1536 + b * 512) * 2, c.lds0 + ks * 3072 + b * 1024); }
}
DI void qkv_phase(const Params& P, Ctx& c, int layer, int rt, int p) {
    const int lane = c.lane, r = c.r, hh = c.hh, sb = c.w & 3, kh = c.w >> 2, sblk = rt * 4 + sb;
    const rsrc_t rx = mk_rsrc(layer ? (const void*)(P.ws + WS_X) : (const void*)P.x, 2 * MB);
    const u32 xoff = layer ? (sblk * 32 + kh * 16) * 1024 + lane * 16 : (sblk * 32 + r) * 1024 + kh * 512 + hh * 16;
    const u32 xstep = layer ? 1024 : 32;
    u4 xf[16];
#pragma unroll
    for (int ks = 0; ks < 16; ++ks) xf[ks] = __builtin_amdgcn_raw_buffer_load_b128(rx, xoff, ks * xstep, SC1);
    if (!layer) VM_BAR(16);
    const int q = (lane & 15) >> 2, cc = (lane >> 4) & 1, pp = lane & 3;
    const int k0 = layer ? 4 * hh : 8 * hh, k1 = layer ? 8 + 4 * hh : 8 * hh + 4;
    lptr w0 = c.lds + kh * 16 * 3072 + (k0 + q) * 64 + cc * 32 + pp * 8, w1 = c.lds + kh * 16 * 3072 + (k1 + q) * 64 + cc * 32 + pp * 8;
    const int cb = sb == 3 ? 0 : sb;
    lptr c0 = w0 + cb * 1024, c1 = w1 + cb * 1024;
    f16v aq = {}, ak = {}, av = {};
    float ssum = 0.f, ssq = 0.f, cs = 0.f;
#pragma unroll
    for (int ks = 0; ks < 16; ++ks) {
        const bf8 wq = tr_frag(w0 + ks * 3072, w1 + ks * 3072), wk = tr_frag(w0 + ks * 3072 + 1024, w1 + ks * 3072 + 1024), wv = tr_frag(w0 + ks * 3072 + 2048, w1 + ks * 3072 + 2048);
        const u4 wc = __builtin_bit_cast(u4, tr_frag(c0 + ks * 3072, c1 + ks * 3072));
        const bf8 x = as_bf8(xf[ks]);
        aq = MFMA(wq, x, aq); ak = MFMA(wk, x, ak); av = MFMA(x, wv, av);
#pragma unroll
        for (int j = 0; j < 4; ++j) { ssum = dot2(xf[ks][j], 0x3f803f80u, ssum); ssq = dot2(xf[ks][j], xf[ks][j], ssq); cs = dot2(wc[j], 0x3f803f80u, cs); }
    }
    ssum = halves_sum(ssum); ssq = halves_sum(ssq); cs = halves_sum(cs);
    float kq[8], kk[8], kv[8];
    lptr xo = c.lds + L_XCH + c.w * 6144 + lane * 16;
#define SEND(acc, b, o) do { *(LAS f4*)(xo + (b) * 2048) = f4{acc[o], acc[o + 1], acc[o + 2], acc[o + 3]}; *(LAS f4*)(xo + (b) * 2048 + 1024) = f4{acc[o + 4], acc[o + 5], acc[o + 6], acc[o + 7]}; } while (0)
    if (kh) { SEND(aq, 0, 0); SEND(ak, 1, 0); SEND(av, 2, 0);
#pragma unroll
        for (int j = 0; j < 8; ++j) { kq[j] = aq[8 + j]; kk[j] = ak[8 + j]; kv[j] = av[8 + j]; }
    } else { SEND(aq, 0, 8); SEND(ak, 1, 8); SEND(av, 2, 8);
#pragma unroll
        for (int j = 0; j < 8; ++j) { kq[j] = aq[j]; kk[j] = ak[j]; kv[j] = av[j]; }
    }
#undef SEND
    if (hh == 0) {
        *(LAS f2*)(c.lds + L_ST + c.w * 256 + r * 8) = f2{ssum, ssq};
        if (sb < 3) *(LAS float*)(c.lds + L_CS + (kh * 3 + sb) * 128 + r * 4) = cs;
    }
    BAR();
    const f2 ps = *(LAS f2*)(c.lds + L_ST + (c.w ^ 4) * 256 + r * 8);
    const float mean = (ssum + ps[0]) * (1.f / D), var = fmaxf((ssq + ps[1]) * (1.f / D) - mean * mean, 0.f);
    const float rs = rsqrtf(var + EPS), mr = mean * rs;
    if (hh == 0) *(LAS f2*)(c.lds + L_ST2 + c.w * 256 + r * 8) = f2{rs, mr};
    lptr xi = c.lds + L_XCH + (c.w ^ 4) * 6144 + lane * 16;
    lptr csp = c.lds + L_CS + (16 * kh + 4 * hh) * 4;
    const rsrc_t rw = mk_rsrc(P.ws + WS_L0 + layer * WS_LSTR, 6 * MB);
    constexpr float QS = 0.25f * 1.4426950408889634f;
    float t[8];
#pragma unroll
    for (int b = 0; b < 2; ++b) {
        const f4 p0 = lds_f4(xi + b * 2048), p1 = lds_f4(xi + b * 2048 + 1024);
        const f4 ca = lds_f4(csp + b * 128) + lds_f4(csp + (3 + b) * 128), cb2 = lds_f4(csp + b * 128 + 32) + lds_f4(csp + (3 + b) * 128 + 32);
        const float* keep = b ? kk : kq; const float sc = b ? 1.f : QS;
#pragma unroll
        for (int j = 0; j < 4; ++j) { t[j] = ((keep[j] + p0[j]) * rs - mr * ca[j]) * sc; t[4 + j] = ((keep[4 + j] + p1[j]) * rs - mr * cb2[j]) * sc; }
        store_wt(pk8(t), rw, ((2 * p + kh) * 64 + sblk) * 1024 + lane * 16 + (b ? O_K : O_Q));
    }
    {
        const f4 p0 = lds_f4(xi + 4096), p1 = lds_f4(xi + 4096 + 1024);
        const float cv = *(LAS float*)(c.lds + L_CS + 2 * 128 + r * 4) + *(LAS float*)(c.lds + L_CS + 5 * 128 + r * 4);
        lptr sp = c.lds + L_ST2 + c.w * 256 + (16 * kh + 4 * hh) * 8;
        const f4 s0 = lds_f4(sp), s1 = lds_f4(sp + 16), s2 = lds_f4(sp + 64), s3 = lds_f4(sp + 80);
        t[0] = (kv[0] + p0[0]) * s0[0] - s0[1] * cv; t[1] = (kv[1] + p0[1]) * s0[2] - s0[3] * cv;
        t[2] = (kv[2] + p0[2]) * s1[0] - s1[1] * cv; t[3] = (kv[3] + p0[3]) * s1[2] - s1[3] * cv;
        t[4] = (kv[4] + p1[0]) * s2[0] - s2[1] * cv; t[5] = (kv[5] + p1[1]) * s2[2] - s2[3] * cv;
        t[6] = (kv[6] + p1[2]) * s3[0] - s3[1] * cv; t[7] = (kv[7] + p1[3]) * s3[2] - s3[3] * cv;
        u4 v = pk8(t);
        const auto a = __builtin_amdgcn_permlane32_swap(v[0], v[2], false, false), b = __builtin_amdgcn_permlane32_swap(v[1], v[3], false, false);
        v = u4{a[0], b[0], a[1], b[1]};
        store_wt(v, rw, ((2 * p + (r >> 4)) * 128 + sblk * 2 + kh) * 512 + hh * 256 + (r & 15) * 16 + (O_V));
    }
}

constexpr int L_V = 65536, VSTR = 1040;
DI f16v pv_acc(bf8 v0, bf8 v1, f16v s, f16v o) {
    const f8 lo = __builtin_shufflevector(s, s, 0, 1, 2, 3, 4, 5, 6, 7), hi = __builtin_shufflevector(s, s, 8, 9, 10, 11, 12, 13, 14, 15);
    o = __builtin_amdgcn_smfmac_f32_32x32x32_bf16(v0, __builtin_bit_cast(bf16v, lo), o, 0xDDDD, 0, 0);
    return __builtin_amdgcn_smfmac_f32_32x32x32_bf16(v1, __builtin_bit_cast(bf16v, hi), o, 0xDDDD, 0, 0);
}
DI void attn_phase(const Params& P, Ctx& c, int layer, int h, int t) {
    const int lane = c.lane, r = c.r, hh = c.hh, w = c.w;
    const u4 srd = mk_srd(P.ws + WS_L0 + layer * WS_LSTR, 8 * MB);
    u4 qv;
    { const u32 qo = O_Q + (h * 64 + t * 8 + w) * 1024 + lane * 16;
      asm volatile("buffer_load_dwordx4 %0, %1, %2, 0 offen sc1" : "=v"(qv) : "v"(qo), "s"(srd) : "memory"); }
    const u32 voff = h * 65536 + w * 2048 + lane * 16;
#pragma unroll
    for (int g = 0; g < 4; ++g)
#pragma unroll
        for (int e = 0; e < 2; ++e) {
            dma16_sc1(srd, voff, O_K + (g * 16 + e) * 1024, c.lds0 + (g * 16 + w * 2 + e) * 1024);
            dma16_sc1(srd, voff, O_V + (g * 16 + e) * 1024, c.lds0 + L_V + (g * 16 + w * 2 + e) * VSTR);
        }
    if (w == 0) *(LAS u4*)(c.lds + L_V + lane * VSTR + 1024) = u4{0x3f803f80u, 0x3f803f80u, 0x3f803f80u, 0x3f803f80u};
    lptr kp = c.lds + lane * 16;
    lptr va = c.lds + L_V + (r < 16 ? hh * 256 + r * 16 : 1024), vb = c.lds + L_V + (r < 16 ? 512 + hh * 256 + r * 16 : 1024);
    asm volatile("s_waitcnt vmcnt(12) lgkmcnt(0)\n\ts_barrier" : "+v"(qv) :: "memory");
    const bf8 q = as_bf8(qv);
    f16v o = {};
    const f16v zero = {};
#pragma unroll
    for (int g = 0; g < 4; ++g) {
        if (g == 1) VM_BAR(8);
        if (g == 2) VM_BAR(4);
        if (g == 3) VM_BAR(0);
#pragma unroll 4
        for (int tt = 0; tt < 16; ++tt) {
            const int kt = g * 16 + tt;
            f16v s = MFMA(as_bf8(lds_u4(kp + kt * 1024)), q, zero);
#pragma unroll
            for (int j = 0; j < 16; ++j) s[j] = __builtin_amdgcn_exp2f(s[j]);
            o = pv_acc(as_bf8(lds_u4(va + kt * VSTR)), as_bf8(lds_u4(vb + kt * VSTR)), s, o);
        }
    }
    if (!__all(o[8] >= 0x1p-40f && o[8] <= 0x1p40f)) {
        float m = -INFINITY;
#pragma unroll 1
        for (int kt = 0; kt < 64; ++kt) {
            const f16v s = MFMA(as_bf8(lds_u4(kp + kt * 1024)), q, zero);
#pragma unroll
            for (int j = 0; j < 16; ++j) m = fmaxf(m, s[j]);
        }
        m = halves_max(m);
        o = zero;
#pragma unroll 1
        for (int kt = 0; kt < 64; ++kt) {
            f16v s = MFMA(as_bf8(lds_u4(kp + kt * 1024)), q, zero);
#pragma unroll
            for (int j = 0; j < 16; ++j) s[j] = __builtin_amdgcn_exp2f(s[j] - m);
            o = pv_acc(as_bf8(lds_u4(va + kt * VSTR)), as_bf8(lds_u4(vb + kt * VSTR)), s, o);
        }
    }
    const float inv = 1.f / o[8];
    float tv[8];
#pragma unroll
    for (int j = 0; j < 8; ++j) tv[j] = o[j] * inv;
    const rsrc_t rw = mk_rsrc(P.ws + WS_L0 + layer * WS_LSTR, 8 * MB);
    store_wt(pk8(tv), rw, ((t * 8 + w) * 32 + h) * 1024 + lane * 16 + (O_O));
}

DI u32 wtile_voff(int lane, int ld) { const int row = lane >> 3, ch = (lane & 7) ^ (((row >> 1) & 1) << 2); return (row * ld + ch * 8) * 2; }
DI lptr wtile_rd(Ctx& c, int nb, int k0) { const int q = (c.lane & 15) >> 2; return c.lds + (k0 + q) * 128 + ((nb ^ (q >> 1)) * 64) + ((c.lane >> 4) & 1) * 32 + (c.lane & 3) * 8; }
DI void reduce4_send(const f16v& a0, const f16v& a1, int own, lptr dst, float* keep) {
#define UNIT(u, acc, o) do { if (own == u) { _Pragma("unroll") for (int j = 0; j < 8; ++j) keep[j] = acc[o + j]; } \
        else { *(LAS f4*)(dst + (u) * 2048) = f4{acc[o], acc[o + 1], acc[o + 2], acc[o + 3]}; *(LAS f4*)(dst + (u) * 2048 + 1024) = f4{acc[o + 4], acc[o + 5], acc[o + 6], acc[o + 7]}; } } while (0)
    UNIT(0, a0, 0); UNIT(1, a0, 8); UNIT(2, a1, 0); UNIT(3, a1, 8);
#undef UNIT
}
DI void reduce4_recv(lptr base, int kq, int kstride, float* keep) {
#pragma unroll
    for (int d = 1; d < 4; ++d) {
        lptr p = base + ((kq + d) & 3) * kstride;
        const f4 a = lds_f4(p), b = lds_f4(p + 1024);
#pragma unroll
        for (int j = 0; j < 4; ++j) { keep[j] += a[j]; keep[4 + j] += b[j]; }
    }
}

constexpr int L_RED = 65536, L_T = 131072, L_PART = L_T + 8192;
DI void prefetch_wo(const Params& P, Ctx& c, int jc) {
    const u4 srd = mk_srd(P.wo, D * D * 2);
    const u32 voff = wtile_voff(c.lane, D) + jc * 128;
#pragma unroll
    for (int g = 0; g < 8; ++g) dma16(srd, voff, (c.w * 8 + g) * 8 * D * 2, c.lds0 + (c.w * 8 + g) * 1024);
}
DI void oproj_phase(const Params& P, Ctx& c, int layer, int i, int jc, float* res) {
    const int lane = c.lane, r = c.r, hh = c.hh, sb = c.w >> 2, kq = c.w & 3;
    const rsrc_t rw = mk_rsrc(P.ws + WS_L0 + layer * WS_LSTR, 12 * MB);
    u4 of[8];
#pragma unroll
    for (int ks = 0; ks < 8; ++ks) of[ks] = __builtin_amdgcn_raw_buffer_load_b128(rw, ((2 * i + sb) * 32 + kq * 8) * 1024 + lane * 16, O_O + ks * 1024, SC1);
    lptr b0 = wtile_rd(c, 0, 4 * hh) + kq * 16384, b1 = wtile_rd(c, 1, 4 * hh) + kq * 16384;
    f16v a0 = {}, a1 = {};
#pragma unroll
    for (int ks = 0; ks < 8; ++ks) {
        a0 = MFMA(tr_frag(b0 + ks * 2048, b0 + ks * 2048 + 1024), as_bf8(of[ks]), a0);
        a1 = MFMA(tr_frag(b1 + ks * 2048, b1 + ks * 2048 + 1024), as_bf8(of[ks]), a1);
    }
    float v[8];
    reduce4_send(a0, a1, kq, c.lds + L_RED + kq * 16384 + sb * 8192 + lane * 16, v);
    BAR();
    reduce4_recv(c.lds + L_RED + sb * 8192 + kq * 2048 + lane * 16, kq, 16384, v);
    float s = 0.f;
#pragma unroll
    for (int j = 0; j < 8; ++j) { res[j] += v[j]; s += res[j]; }
    const float mu = halves_sum(s) * (1.f / 16);
    float m2 = 0.f;
#pragma unroll
    for (int j = 0; j < 8; ++j) { const float d = res[j] - mu; m2 += d * d; }
    m2 = halves_sum(m2);
    if (hh == 0) *(LAS f2*)(c.lds + L_PART + (sb * 32 + r) * 32 + kq * 8) = f2{mu, m2};
    BAR();
    GAS char* st = (GAS char*)(P.ws + WS_STATS + (layer * 32 + i) * 4096);
    if (c.w == 0) {
        const f4 a = lds_f4(c.lds + L_PART + lane * 32), b = lds_f4(c.lds + L_PART + lane * 32 + 16);
        const float mt = (a[0] + a[2] + b[0] + b[2]) * 0.25f;
        const float d0 = a[0] - mt, d1 = a[2] - mt, d2 = b[0] - mt, d3 = b[2] - mt;
        const float qt = a[1] + a[3] + b[1] + b[3] + 16.f * (d0 * d0 + d1 * d1 + d2 * d2 + d3 * d3);
        const u32 lo = min(__float_as_uint(mt), 0xfffffffeu), hi = min(__float_as_uint(qt), 0xfffffffeu);
        __hip_atomic_store((GAS u64*)(st + jc * 512 + lane * 8), ((u64)hi << 32) | lo, RLX_AGENT);
    }
    float pm[4], pq[4];
    {
        GAS u64* g = (GAS u64*)(st + hh * 2048 + (sb * 32 + r) * 8);
        u32 spins = 0;
        for (;;) {
            bool ok = true;
#pragma unroll
            for (int j = 0; j < 4; ++j) { const u64 x = __hip_atomic_load(g + j * 64, RLX_AGENT); pm[j] = __uint_as_float((u32)x); pq[j] = __uint_as_float((u32)(x >> 32)); ok &= ((u32)x != 0xffffffffu) & ((u32)(x >> 32) != 0xffffffffu); }
            if (__all(ok) || ++spins > SPIN_CAP) break;
        }
    }
    const float mean = halves_sum(pm[0] + pm[1] + pm[2] + pm[3]) * 0.125f;
    float qs = pq[0] + pq[1] + pq[2] + pq[3];
#pragma unroll
    for (int j = 0; j < 4; ++j) { const float d = pm[j] - mean; qs += 64.f * d * d; }
    const float var = halves_sum(qs) * (1.f / D);
    const float r1 = rsqrtf(var + EPS), rr = r1 * rsqrtf(var * r1 * r1 + EPS);
    const int srow = sb * 32 + r;
    lptr tp = c.lds + L_T + srow * 128 + (((kq >> 1) ^ ((srow >> 1) & 1)) * 64) + ((kq & 1) * 16 + 4 * hh) * 2;
    *(LAS u2*)tp = u2{pk2((res[0] - mean) * rr, (res[1] - mean) * rr), pk2((res[2] - mean) * rr, (res[3] - mean) * rr)};
    *(LAS u2*)(tp + 16) = u2{pk2((res[4] - mean) * rr, (res[5] - mean) * rr), pk2((res[6] - mean) * rr, (res[7] - mean) * rr)};
    BAR();
    const int db = c.w & 1, ks = c.w >> 1;
    lptr tr = wtile_rd(c, db, 8 * hh) + L_T + ks * 2048;
    store_wt(__builtin_bit_cast(u4, tr_frag(tr, tr + 512)), rw, ((2 * jc + db) * 128 + 4 * i + ks) * 1024 + lane * 16 + (O_H));
}

template <int NK, int LD> DI void wring_issue(u4 srd, u32 voff, Ctx& c, int kq, int ab, int stage) {
#pragma unroll
    for (int e = 0; e < 4; ++e) { const int ks = stage * 4 + e; dma16(srd, voff, ((kq * NK + ks) * 16 + ab * 8) * LD * 2, c.lds0 + kq * 32768 + (ks & 15) * 2048 + ab * 1024); }
}
template <int NK, int LD> DI void prefetch_w(const u16* w, Ctx& c, int kq, int ab, int col0) {
    const u4 srd = mk_srd(w, NK * 64 * LD * 2);
    const u32 voff = wtile_voff(c.lane, LD) + col0 * 2;
#pragma unroll
    for (int s = 0; s < 4; ++s) wring_issue<NK, LD>(srd, voff, c, kq, ab, s);
}
template <int NK, int LD, bool W_IS_A> DI void mix_gemm(const u16* w, int col0, rsrc_t ra, u32 aoff, Ctx& c, int kq, int ab, int own, float* v) {
    const u4 srd = mk_srd(w, NK * 64 * LD * 2);
    const u32 voff = wtile_voff(c.lane, LD) + col0 * 2;
    u4 af[NK];
#pragma unroll
    for (int ks = 0; ks < NK; ++ks) af[ks] = __builtin_amdgcn_raw_buffer_load_b128(ra, aoff, ks * 1024, SC1);
    constexpr int K1 = W_IS_A ? 512 : 1024;
    const int k0 = W_IS_A ? 8 * c.hh : 4 * c.hh;
    lptr b0 = wtile_rd(c, 0, k0) + kq * 32768, b1 = wtile_rd(c, 1, k0) + kq * 32768;
    f16v a0 = {}, a1 = {};
    constexpr int NS = NK / 4;
#pragma unroll
    for (int s = 0; s < NS; ++s) {
        if (s >= 1) {
            if (s < 4) BAR();
            else if (NS - 1 - s >= 2) VM_BAR(8);
            else if (NS - 1 - s == 1) VM_BAR(4);
            else VM_BAR(0);
            if (s + 3 < NS) wring_issue<NK, LD>(srd, voff, c, kq, ab, s + 3);
        }
#pragma unroll
        for (int e = 0; e < 4; ++e) {
            const int ks = s * 4 + e, o = (ks & 15) * 2048;
            const bf8 w0 = tr_frag(b0 + o, b0 + o + K1), w1 = tr_frag(b1 + o, b1 + o + K1), x = as_bf8(af[ks]);
            if (W_IS_A) { a0 = MFMA(w0, x, a0); a1 = MFMA(w1, x, a1); } else { a0 = MFMA(x, w0, a0); a1 = MFMA(x, w1, a1); }
        }
    }
    reduce4_send(a0, a1, own, c.lds + kq * 32768 + ab * 8192 + c.lane * 16, v);
    BAR();
    reduce4_recv(c.lds + ab * 8192 + own * 2048 + c.lane * 16, kq, 32768, v);
}

__global__ __launch_bounds__(512, 2) void fused_kernel(Params P) {
    extern __shared__ __attribute__((aligned(16))) char smem[];
    Ctx c;
    c.lds = (lptr)smem; c.lds0 = (u32)(uintptr_t)c.lds;
    c.lane = threadIdx.x & 63; c.w = __builtin_amdgcn_readfirstlane(threadIdx.x >> 6); c.r = c.lane & 31; c.hh = c.lane >> 5; c.dead = false;
    int wg = blockIdx.x;
#define xcd (wg & 7)
#define j5 (wg >> 3)
#define rt (2 * xcd + (j5 >> 4))
#define p (j5 & 15)
#define head (4 * xcd + (j5 >> 3))
#define qt (j5 & 7)
#define ti (8 * (xcd >> 1) + (j5 >> 2))
#define tj (4 * (xcd & 1) + (j5 & 3))
#define ft (7 * (xcd >> 1) + (j5 >> 2))
#define g1_on (j5 < 28)
#define wa (c.w >> 2)
#define wb ((c.w >> 1) & 1)
#define wc (c.w & 1)
#define FRESH() do { c.refresh(); wg = opaque(wg); } while (0)
    STAMP(0);
    prefetch_wqkv(P, c, p);
    const u64 epoch = __hip_atomic_load((GAS u64*)P.ws, RLX_AGENT);
    if (threadIdx.x < 128) __hip_atomic_store((GAS u64*)(P.ws + WS_STATS + ((threadIdx.x >> 6) * 32 + ti) * 4096 + tj * 512 + c.lane * 8), ~0ull, RLX_AGENT);
    float res[8];
    {
        const u16* xr = P.x + (ti * 64 + wa * 32 + c.r) * D + tj * 64 + wb * 32 + wc * 16 + 4 * c.hh;
        const u2 a = *(const u2*)xr, b = *(const u2*)(xr + 8);
        res[0] = bflo(a[0]); res[1] = bfhi(a[0]); res[2] = bflo(a[1]); res[3] = bfhi(a[1]);
        res[4] = bflo(b[0]); res[5] = bfhi(b[0]); res[6] = bflo(b[1]); res[7] = bfhi(b[1]);
    }
    c.tag = mix64(epoch);
#pragma unroll 1
    for (int layer = 0; layer < 2; ++layer) {
        const int ph = layer * 5, so = layer * 16;
        FRESH();
        if (layer) await(P, c, 4, rt * 16, 16);
        STAMP(so + 1);
        qkv_phase(P, c, layer, rt, p);
        STAMP(so + 2);
        publish(P, c, ph + 0, p * 16 + rt);
        STAMP(so + 3);
        FRESH();
        await(P, c, ph + 0, (head >> 1) * 16, 16);
        STAMP(so + 4);
        attn_phase(P, c, layer, head, qt);
        STAMP(so + 5);
        publish(P, c, ph + 1, qt * 32 + head);
        FRESH();
        prefetch_wo(P, c, tj);
        STAMP(so + 6);
        await(P, c, ph + 1, (ti >> 2) * 32, 32);
        STAMP(so + 7);
        oproj_phase(P, c, layer, ti, tj, res);
        STAMP(so + 8);
        publish(P, c, ph + 2, tj * 32 + ti);
        FRESH();
        const rsrc_t rw = mk_rsrc(P.ws + WS_L0 + layer * WS_LSTR, 12 * MB);
        STAMP(so + 9);
        if (g1_on) {
            const int kq = c.w & 3, db = wa;
            prefetch_w<32, F>(P.w1, c, kq, db, ft * 64);
            const u16* bp = P.b1 + ft * 64 + (kq >> 1) * 32 + (kq & 1) * 16 + 4 * c.hh;
            const u2 ba = *(const u2*)bp, bb = *(const u2*)(bp + 8);
            await(P, c, ph + 2, tj * 32, 32);
            STAMP(so + 10);
            float v[8];
            mix_gemm<32, F, true>(P.w1, ft * 64, rw, O_H + ((2 * tj + db) * 128 + kq * 32) * 1024 + c.lane * 16, c, kq, db, kq, v);
            const float bias[8] = {bflo(ba[0]), bfhi(ba[0]), bflo(ba[1]), bfhi(ba[1]), bflo(bb[0]), bfhi(bb[0]), bflo(bb[1]), bfhi(bb[1])};
#pragma unroll
            for (int e = 0; e < 8; ++e) {
                const float z = v[e] + bias[e];
                const float a = z * (-2.f * 0.7978845608028654f * 1.4426950408889634f) * (1.f + 0.044715f * z * z);
                v[e] = z * __builtin_amdgcn_rcpf(1.f + __builtin_amdgcn_exp2f(a));
            }
            store_wt(pk8(v), rw, ((2 * tj + db) * 112 + ft * 4 + kq) * 1024 + c.lane * 16 + (O_U));
            STAMP(so + 11);
            publish(P, c, ph + 3, tj * 32 + ft);
        }
        FRESH();
        {
            const int kq = wa * 2 + wc, db = wb;
            prefetch_w<28, S>(P.w2, c, kq, db, ti * 64);
            const float bias = bflo(P.b2[ti * 64 + wa * 32 + c.r]);
            STAMP(so + 12);
            await(P, c, ph + 3, tj * 32, 28);
            STAMP(so + 13);
            float v[8];
            mix_gemm<28, S, false>(P.w2, ti * 64, rw, O_U + ((2 * tj + db) * 112 + kq * 28) * 1024 + c.lane * 16, c, kq, db, kq, v);
#pragma unroll
            for (int e = 0; e < 8; ++e) res[e] += v[e] + bias;
            u4 o = pk8(res);
            if (layer == 0) {
                const rsrc_t rx = mk_rsrc(P.ws + WS_X, 2 * MB);
                store_wt(o, rx, ((2 * ti + wa) * 32 + tj * 4 + wb * 2 + wc) * 1024 + c.lane * 16);
                STAMP(so + 14);
                publish(P, c, 4, (ti >> 1) * 16 + (ti & 1) * 8 + tj);
                FRESH();
                prefetch_wqkv(P, c, p);
            } else {
                const auto a = __builtin_amdgcn_permlane32_swap(o[0], o[2], false, false), b = __builtin_amdgcn_permlane32_swap(o[1], o[3], false, false);
                *(u4*)(P.out + (ti * 64 + wa * 32 + c.r) * D + tj * 64 + wb * 32 + wc * 16 + 8 * c.hh) = u4{a[0], b[0], a[1], b[1]};
                STAMP(so + 14);
            }
        }
        STAMP(so + 15);
    }
    if (wg == 0 && threadIdx.x == 0) __hip_atomic_store((GAS u64*)P.ws, c.tag, RLX_AGENT);
}

extern "C" void kernel_launch(void* const* d_in, const int* in_sizes, int n_in, void* d_out, int out_size, void* d_ws, size_t ws_size, hipStream_t stream) {
    Params P;
    P.x = (const u16*)d_in[0]; P.wqkv = (const u16*)d_in[1]; P.wo = (const u16*)d_in[2];
    P.w1 = (const u16*)d_in[4]; P.b1 = (const u16*)d_in[5]; P.w2 = (const u16*)d_in[6]; P.b2 = (const u16*)d_in[7];
    P.out = (u16*)d_out; P.ws = (char*)d_ws;
    (void)hipFuncSetAttribute((const void*)fused_kernel, hipFuncAttributeMaxDynamicSharedMemorySize, LDS_BYTES);
    fused_kernel<<<256, 512, LDS_BYTES, stream>>>(P);
}
```

```cpp
#include <hip/hip_runtime.h>
#include <stdint.h>

typedef unsigned short u16;
typedef unsigned u32;
typedef unsigned long long u64;
typedef u32 u2 __attribute__((ext_vector_type(2)));
typedef u32 u4 __attribute__((ext_vector_type(4)));
typedef u32 u16v __attribute__((ext_vector_type(16)));
#define DI __device__ __forceinline__

constexpr int S = 2048, D = 512, F = 1792;
constexpr u32 MB = 1u << 20;
constexpr u32 WS_FLAGS = 0x1000, WS_STATS = 0xB0000, WS_XS = 0xF0000, WS_CS = 0x130000, WS_L0 = 2 * MB, WS_X = 26 * MB, WS_WP = 30 * MB, WS_END = 32 * MB;
constexpr u32 O_V = 4 * MB, O_O = 6 * MB, O_H = 8 * MB, O_U = 10 * MB;
constexpr int LDS_BYTES = 160 * 1024;

struct Params { const u16 *x, *wqkv, *wo, *w1, *b1, *w2, *b2; u16* out; char* ws; };

DI u4 srd(const void* p, u32 bytes) { const u64 a = (u64)p; return u4{(u32)a, (u32)(a >> 32) & 0xffffu, bytes, 0x00020000u}; }
DI u16v cat(u4 a, u4 b, u4 c, u4 d) { return u16v{a[0], a[1], a[2], a[3], b[0], b[1], b[2], b[3], c[0], c[1], c[2], c[3], d[0], d[1], d[2], d[3]}; }
DI u32 wtile_voff(u32 lane, u32 ld) { const u32 row = lane >> 3, ch = (lane & 7) ^ (((row >> 1) & 3) << 1); return (row * ld + ch * 8) * 2; }
DI u32 wtile_rd(u32 lane, u32 row) { return row * 128 + ((row >> 1) & 3) * 32 + (lane & 3) * 8; }

__global__ __launch_bounds__(512, 2) void fused_kernel(Params P) {
    const u32 lane = threadIdx.x & 63, w = __builtin_amdgcn_readfirstlane(threadIdx.x >> 6), r = lane & 31, hh = lane >> 5, c = lane & 15, g = lane >> 4, q = c >> 2;
    const u32 wg = blockIdx.x, xcd = wg & 7, j5 = wg >> 3;
    const u32 rt = 8 * (xcd >> 2) + (j5 >> 2), p = 4 * (xcd & 3) + (j5 & 3);
    const u32 head = 4 * xcd + (j5 >> 3), qt = j5 & 7;
    const u32 ti = 4 * xcd + (j5 >> 3), tj = j5 & 7;
    const u32 jd = 4 * (xcd & 1) + (j5 & 3), ft = 7 * (xcd >> 1) + (j5 >> 2), g1 = (j5 - 28) >> 31;
    const u32 kh = w >> 2, kq1 = w & 3, kq2 = kh * 2 + (w & 1), wb = (w >> 1) & 1, sblk = rt * 4 + kq1;
    const u32 w1 = (w + 2 * (j5 & 3)) & 7;
    const u4 sws = srd(P.ws, WS_END), swq = srd(P.wqkv, D * 3 * D * 2);
    const u32 wqv = ((lane >> 2) * 1536 + ((lane & 3) ^ ((g & 1) << 1)) * 8 + p * 32) * 2, wqso = w * 4 * 49152;
    u4 xin; u2 epoch; u2 sink;
    asm volatile("global_load_dwordx2 v[0:1], %3, off\n\tglobal_load_dwordx2 v[2:3], %3, off offset:32\n\tbuffer_load_dwordx2 v[4:5], off, %4, 0 sc1\n\t"
                 "s_mov_b32 m0, %7\n\ts_add_u32 s0, %8, 2048\n\tbuffer_load_dwordx4 %5, %6, s0 offen lds\n\t"
                 "s_add_u32 m0, m0, 1024\n\ts_add_u32 s0, s0, 49152\n\tbuffer_load_dwordx4 %5, %6, s0 offen lds\n\t"
                 "s_add_u32 m0, m0, 1024\n\ts_add_u32 s0, s0, 49152\n\tbuffer_load_dwordx4 %5, %6, s0 offen lds\n\t"
                 "s_add_u32 m0, m0, 1024\n\ts_add_u32 s0, s0, 49152\n\tbuffer_load_dwordx4 %5, %6, s0 offen lds\n\t"
                 "buffer_load_dword v26, %9, %4, 0 offen\n\t"
                 "buffer_load_dword v27, %5, %6, %10 offen"
        : "=&{v[0:3]}"(xin), "=&{v[4:5]}"(epoch), "=&{v[26:27]}"(sink)
        : "v"(P.x + (ti * 64 + kq2 * 16 + c) * D + tj * 64 + wb * 32 + 4 * g), "s"(sws), "v"(wqv), "s"(swq), "s"(131072 + w * 4096), "s"(wqso), "v"((lane & 15) * 2 * MB), "s"((((rt * 4 + kq1) & 31) * 16 * 1536 + (rt >> 3) * 512) * 2)
        : "s0", "scc", "memory");
    const u32 oH = O_H + ((tj * 4 + kq1) * 64 + ti * 2 + kh) * 1024;
    const u16v sA = cat(sws, srd(P.w1, S * F * 2), srd(P.w2, F * S * 2), srd(P.wo, D * D * 2));
    u16v sB = cat(swq, srd(P.b1, F * 2), srd(P.b2, S * 2), srd(P.out, S * D * 2));
    const u16v sC = {w, w * 1024, ((2 * p + kh) * 64 + sblk) * 1024, 0,
                     O_O + ((qt * 8 + w) * 32 + (head >> 1)) * 1024 + (head & 1) * 512, oH, oH + g1 * (O_U + ((jd * 4 + kh * 2 + (w & 1)) * 56 + ft * 2 + wb) * 1024 - oH),
                     WS_X + ((ti * 4 + kq2) * 16 + tj * 2 + wb) * 1024,
                     WS_X + (sblk * 32 + kh * 8) * 1024, (head * 64 + qt * 8 + w) * 1024, head * 65536 + w * 2048, O_O + ((2 * ti + kh) * 32 + kq1 * 4) * 1024,
                     O_H + ((jd * 4 + kh * 2) * 64 + kq1 * 16) * 1024, O_U + ((tj * 4 + wb * 2) * 56 + kq2 * 14) * 1024, ((w1 >> 1) * 512 + (w1 & 1) * 8) * F * 2, ((w >> 1) * 448 + (w & 1) * 8) * S * 2};
    const u16v sD = {((w >> 1) * 128 + (w & 1) * 8) * D * 2, wqso, WS_WP + p * 98304 + w * 4096, WS_WP + p * 98304 + (6 * rt + w) * 1024,
                     WS_FLAGS + ((ti >> 1) * 16 + (ti & 1) * 8 + tj) * 256, WS_FLAGS + rt * 16 * 256, WS_FLAGS + (256 + p * 16 + rt) * 256, WS_FLAGS + (256 + (head >> 1) * 16) * 256,
                     WS_STATS + ti * 4096 + tj * 512, WS_STATS + ti * 4096, WS_XS + ti * 4096 + tj * 512, WS_CS + wg * 1024,
                     (jd * 256 + w * 32) * F * 2, (tj * 224 + w * 28) * S * 2, g1, kq1};
    u4 sE = {kq2, kh, WS_L0, 0};
    const u2 sF = {(ti * 64 + kq2 * 16) * 1024 + tj * 128 + wb * 64, ((wg | w) - 1) >> 31};
    u32 sb16 = 0;
    const u32 b1so = (ft * 64 + wb * 32) * 2, b2so = (ti * 64 + kq2 * 16) * 2;
    const u4 vA = {lane * 16, wtile_voff(lane, F) + ft * 128, wtile_voff(lane, S) + ti * 128, wtile_voff(lane, D) + tj * 128};
    const u4 vB = {wqv, wtile_rd(lane, 4 * g + q), wtile_rd(lane, 4 * (g & 1) + q) + (g >> 1) * 8192, 65536 + kh * 16384 + (4 * g + q) * 64 + (g & 1) * 32 + (lane & 3) * 8};
    const u4 vC = {(sblk >> 1) * 4096 + hh * 2048 + ((sblk & 1) * 32 + r) * 8, (g & 1) * 16384 + (g >> 1) * 256 + c * 16, c, g};
    const u32 vD = (g & 1) * 512 + (g >> 1) * 256 + c * 16;
    asm volatile(
    "Lcbeg_6_%=:\n"
    "s_waitcnt vmcnt(6)\n"
    "v_lshlrev_b32 v116, 16, v0\n"
    "v_and_b32 v117, 0xffff0000, v0\n"
    "v_lshlrev_b32 v118, 16, v1\n"
    "v_and_b32 v119, 0xffff0000, v1\n"
    "v_lshlrev_b32 v120, 16, v2\n"
    "v_and_b32 v121, 0xffff0000, v2\n"
    "v_lshlrev_b32 v122, 16, v3\n"
    "v_and_b32 v123, 0xffff0000, v3\n"
    "v_readfirstlane_b32 s72, v4\n"
    "v_readfirstlane_b32 s73, v5\n"
    "s_lshl_b32 s2, s71, 21\n"
    "s_add_u32 s3, s2, s43\n"
    "v_cvt_pk_bf16_f32 v0, v116, v117\n"
    "v_cvt_pk_bf16_f32 v1, v118, v119\n"
    "v_cvt_pk_bf16_f32 v2, v120, v121\n"
    "v_cvt_pk_bf16_f32 v3, v122, v123\n"
    "v_min_u32 v0, -2, v0\n"
    "v_min_u32 v1, -2, v1\n"
    "v_min_u32 v2, -2, v2\n"
    "v_min_u32 v3, -2, v3\n"
    "buffer_store_dwordx4 v[0:3], v104, s[4:7], s3 offen sc1\n"
    "s_add_u32 s2, s2, s44\n"
    "v_add_u32 v82, s2, v104\n"
    "v_add_u32 v83, 16384, v82\n"
    "s_lshl_b32 s0, s68, 8\n"
    "s_bfe_u32 s1, s36, 0x10001\n"
    "s_lshl_b32 s1, s1, 3\n"
    "s_add_u32 s0, s0, s1\n"
    "s_add_u32 s0, s0, 0\n"
    "v_pk_add_f32 v[4:5], v[116:117], v[118:119]\n"
    "v_pk_add_f32 v[6:7], v[120:121], v[122:123]\n"
    "v_pk_add_f32 v[4:5], v[4:5], v[6:7]\n"
    "v_add_f32 v4, v4, v5\n"
    "v_lshl_add_u32 v16, v114, 4, s0\n"
    "v_mov_b32 v5, v4\n"
    "s_nop 1\n"
    "v_permlane16_swap_b32 v4, v5\n"
    "v_add_f32 v4, v4, v5\n"
    "v_mov_b32 v5, v4\n"
    "s_nop 1\n"
    "v_permlane32_swap_b32 v4, v5\n"
    "v_add_f32 v4, v4, v5\n"
    "v_mul_f32 v4, 0x3d000000, v4\n"
    "v_pk_add_f32 v[8:9], v[116:117], v[4:5] op_sel_hi:[1,0] neg_lo:[0,1] neg_hi:[0,1]\n"
    "v_pk_add_f32 v[10:11], v[118:119], v[4:5] op_sel_hi:[1,0] neg_lo:[0,1] neg_hi:[0,1]\n"
    "v_pk_add_f32 v[12:13], v[120:121], v[4:5] op_sel_hi:[1,0] neg_lo:[0,1] neg_hi:[0,1]\n"
    "v_pk_add_f32 v[14:15], v[122:123], v[4:5] op_sel_hi:[1,0] neg_lo:[0,1] neg_hi:[0,1]\n"
    "v_pk_mul_f32 v[8:9], v[8:9], v[8:9]\n"
    "v_pk_mul_f32 v[12:13], v[12:13], v[12:13]\n"
    "v_pk_fma_f32 v[8:9], v[10:11], v[10:11], v[8:9]\n"
    "v_pk_fma_f32 v[12:13], v[14:15], v[14:15], v[12:13]\n"
    "v_pk_add_f32 v[8:9], v[8:9], v[12:13]\n"
    "v_add_f32 v5, v8, v9\n"
    "v_mov_b32 v6, v5\n"
    "s_nop 1\n"
    "v_permlane16_swap_b32 v5, v6\n"
    "v_add_f32 v5, v5, v6\n"
    "v_mov_b32 v6, v5\n"
    "s_nop 1\n"
    "v_permlane32_swap_b32 v5, v6\n"
    "v_add_f32 v5, v5, v6\n"
    "ds_write_b64 v16, v[4:5]\n"
    "s_cmp_eq_u32 s36, 0\n"
    "s_cbranch_scc1 Lw_17_%=\n"
    "s_lshl_b32 s1, s37, 2\n"
    "s_add_u32 m0, s1, 98304\n"
    "s_add_u32 s0, s53, 1024\n"
    "buffer_load_dwordx4 v108, s[20:23], s0 offen lds\n"
    "s_lshl_b32 s1, s37, 2\n"
    "s_add_u32 m0, s1, 99328\n"
    "s_add_u32 s0, s53, 50176\n"
    "buffer_load_dwordx4 v108, s[20:23], s0 offen lds\n"
    "s_lshl_b32 s1, s37, 2\n"
    "s_add_u32 m0, s1, 100352\n"
    "s_add_u32 s0, s53, 99328\n"
    "buffer_load_dwordx4 v108, s[20:23], s0 offen lds\n"
    "s_lshl_b32 s1, s37, 2\n"
    "s_add_u32 m0, s1, 101376\n"
    "s_add_u32 s0, s53, 148480\n"
    "buffer_load_dwordx4 v108, s[20:23], s0 offen lds\n"
    "Lw_17_%=:\n"
    "s_add_u32 s72, s72, 0x7f4a7c15\n"
    "s_addc_u32 s73, s73, 0x9e3779b9\n"
    "s_lshr_b64 s[0:1], s[72:73], 30\n"
    "s_xor_b64 s[72:73], s[72:73], s[0:1]\n"
    "s_mul_i32 s0, s72, 0xbf58476d\n"
    "s_mul_hi_u32 s1, s72, 0x1ce4e5b9\n"
    "s_add_u32 s0, s0, s1\n"
    "s_mul_i32 s1, s73, 0x1ce4e5b9\n"
    "s_add_u32 s73, s0, s1\n"
    "s_mul_i32 s72, s72, 0x1ce4e5b9\n"
    "s_lshr_b64 s[0:1], s[72:73], 27\n"
    "s_xor_b64 s[72:73], s[72:73], s[0:1]\n"
    "s_mul_i32 s0, s72, 0x94d049bb\n"
    "s_mul_hi_u32 s1, s72, 0x133111eb\n"
    "s_add_u32 s0, s0, s1\n"
    "s_mul_i32 s1, s73, 0x133111eb\n"
    "s_add_u32 s73, s0, s1\n"
    "s_mul_i32 s72, s72, 0x133111eb\n"
    "s_lshr_b64 s[0:1], s[72:73], 31\n"
    "s_xor_b64 s[72:73], s[72:73], s[0:1]\n"
    "s_waitcnt lgkmcnt(0)\n"
    "s_barrier\n"
    "s_cmp_eq_u32 s36, 0\n"
    "s_cbranch_scc0 Lw_19_%=\n"
    "v_add_u32 v16, 0, v104\n"
    "ds_read_b128 v[4:7], v16\n"
    "v_lshrrev_b32 v17, 1, v104\n"
    "s_waitcnt lgkmcnt(0)\n"
    "v_pk_add_f32 v[12:13], v[4:5], v[6:7]\n"
    "v_sub_f32 v4, v4, v6\n"
    "v_mul_f32 v12, 0.5, v12\n"
    "v_mul_f32 v4, v4, v4\n"
    "v_fmac_f32 v13, 0x41800000, v4\n"
    "v_min_u32 v12, -2, v12\n"
    "v_min_u32 v13, -2, v13\n"
    "buffer_store_dwordx2 v[12:13], v17, s[4:7], s62 offen sc1\n"
    "s_lshl_b32 s1, s37, 2\n"
    "s_add_u32 m0, s1, 98304\n"
    "s_add_u32 s0, s53, 1024\n"
    "buffer_load_dwordx4 v108, s[20:23], s0 offen lds\n"
    "s_lshl_b32 s1, s37, 2\n"
    "s_add_u32 m0, s1, 99328\n"
    "s_add_u32 s0, s53, 50176\n"
    "buffer_load_dwordx4 v108, s[20:23], s0 offen lds\n"
    "s_lshl_b32 s1, s37, 2\n"
    "s_add_u32 m0, s1, 100352\n"
    "s_add_u32 s0, s53, 99328\n"
    "buffer_load_dwordx4 v108, s[20:23], s0 offen lds\n"
    "s_lshl_b32 s1, s37, 2\n"
    "s_add_u32 m0, s1, 101376\n"
    "s_add_u32 s0, s53, 148480\n"
    "buffer_load_dwordx4 v108, s[20:23], s0 offen lds\n"
    "Lw_19_%=:\n"
    "s_waitcnt vmcnt(4) lgkmcnt(0)\n"
    "s_barrier\n"
    "s_cmp_eq_u32 s36, 0\n"
    "s_cbranch_scc0 Lw_22_%=\n"
    "v_mov_b32 v68, s72\n"
    "v_mov_b32 v69, s73\n"
    "s_mov_b64 exec, 1\n"
    "buffer_store_dwordx2 v[68:69], off, s[4:7], s56 sc1\n"
    "s_mov_b64 exec, -1\n"
    "v_and_b32 v70, 0xf0, v104\n"
    "v_lshlrev_b32 v70, 4, v70\n"
    "s_mov_b32 s2, 0\n"
    "Lfp_20_%=:\n"
    "buffer_load_dwordx2 v[68:69], v70, s[4:7], s57 offen sc1\n"
    "s_add_u32 s2, s2, 1\n"
    "s_waitcnt vmcnt(0)\n"
    "v_cmp_ne_u64 vcc, s[72:73], v[68:69]\n"
    "s_cbranch_vccz Lfq_21_%=\n"
    "s_cmp_lt_u32 s2, 65536\n"
    "s_cbranch_scc1 Lfp_20_%=\n"
    "Lfq_21_%=:\n"
    "Lw_22_%=:\n"
    "s_cmp_eq_u32 s36, 0\n"
    "s_cbranch_scc1 Lw_23_%=\n"
    "s_sub_u32 s2, s36, 1\n"
    "Lwq_18_%=:\n"
    "s_lshl_b32 s0, s2, 10\n"
    "s_add_u32 m0, s0, 65536\n"
    "s_lshr_b32 s0, s2, 5\n"
    "s_lshl_b32 s0, s0, 10\n"
    "s_and_b32 s1, s2, 31\n"
    "s_mul_i32 s1, s1, 49152\n"
    "s_add_u32 s0, s0, s1\n"
    "buffer_load_dwordx4 v108, s[20:23], s0 offen lds\n"
    "s_add_u32 s2, s2, 7\n"
    "s_cmp_lt_u32 s2, 32\n"
    "s_cbranch_scc1 Lwq_18_%=\n"
    "Lw_23_%=:\n"
    "s_waitcnt vmcnt(0) lgkmcnt(0)\n"
    "s_barrier\n"
    "s_mov_b32 s84, 0\n"
    "buffer_load_dwordx4 v[128:131], v82, s[4:7], s84 offen offset:0 sc1\n"
    "buffer_load_dwordx4 v[132:135], v82, s[4:7], s84 offen offset:1024 sc1\n"
    "buffer_load_dwordx4 v[136:139], v83, s[4:7], s84 offen offset:0 sc1\n"
    "buffer_load_dwordx4 v[140:143], v83, s[4:7], s84 offen offset:1024 sc1\n"
    "s_mov_b32 s85, 2048\n"
    "buffer_load_dwordx4 v[144:147], v82, s[4:7], s85 offen offset:0 sc1\n"
    "buffer_load_dwordx4 v[148:151], v82, s[4:7], s85 offen offset:1024 sc1\n"
    "buffer_load_dwordx4 v[152:155], v83, s[4:7], s85 offen offset:0 sc1\n"
    "buffer_load_dwordx4 v[156:159], v83, s[4:7], s85 offen offset:1024 sc1\n"
    "s_mov_b32 s86, 0\n"
    "s_mov_b32 s96, 0\n"
    "s_cmp_eq_u32 s67, 0\n"
    "s_cbranch_scc1 Lc_26_%=\n"
    "s_sub_u32 s2, s67, 1\n"
    "s_lshl_b32 s0, s2, 15\n"
    "v_add_u32 v16, s0, v111\n"
    "v_xor_b32 v17, 32, v16\n"
    "v_mov_b32 v28, 0x3f803f80\n"
    "v_mov_b32 v29, 0x3f803f80\n"
    "v_mov_b32 v30, 0x3f803f80\n"
    "v_mov_b32 v31, 0x3f803f80\n"
    "ds_read_b64_tr_b16 v[48:49], v16 offset:0\n"
    "ds_read_b64_tr_b16 v[50:51], v16 offset:1024\n"
    "ds_read_b64_tr_b16 v[52:53], v17 offset:0\n"
    "ds_read_b64_tr_b16 v[54:55], v17 offset:1024\n"
    "ds_read_b64_tr_b16 v[56:57], v16 offset:2048\n"
    "ds_read_b64_tr_b16 v[58:59], v16 offset:3072\n"
    "ds_read_b64_tr_b16 v[60:61], v17 offset:2048\n"
    "ds_read_b64_tr_b16 v[62:63], v17 offset:3072\n"
    "s_waitcnt lgkmcnt(4)\n"
    "v_mfma_f32_16x16x32_bf16 v[32:35], v[28:31], v[48:51], 0\n"
    "v_mfma_f32_16x16x32_bf16 v[36:39], v[28:31], v[52:55], 0\n"
    "ds_read_b64_tr_b16 v[48:49], v16 offset:4096\n"
    "ds_read_b64_tr_b16 v[50:51], v16 offset:5120\n"
    "ds_read_b64_tr_b16 v[52:53], v17 offset:4096\n"
    "ds_read_b64_tr_b16 v[54:55], v17 offset:5120\n"
    "s_waitcnt lgkmcnt(4)\n"
    "v_mfma_f32_16x16x32_bf16 v[32:35], v[28:31], v[56:59], v[32:35]\n"
    "v_mfma_f32_16x16x32_bf16 v[36:39], v[28:31], v[60:63], v[36:39]\n"
    "ds_read_b64_tr_b16 v[56:57], v16 offset:6144\n"
    "ds_read_b64_tr_b16 v[58:59], v16 offset:7168\n"
    "ds_read_b64_tr_b16 v[60:61], v17 offset:6144\n"
    "ds_read_b64_tr_b16 v[62:63], v17 offset:7168\n"
    "s_waitcnt lgkmcnt(4)\n"
    "v_mfma_f32_16x16x32_bf16 v[32:35], v[28:31], v[48:51], v[32:35]\n"
    "v_mfma_f32_16x16x32_bf16 v[36:39], v[28:31], v[52:55], v[36:39]\n"
    "ds_read_b64_tr_b16 v[48:49], v16 offset:8192\n"
    "ds_read_b64_tr_b16 v[50:51], v16 offset:9216\n"
    "ds_read_b64_tr_b16 v[52:53], v17 offset:8192\n"
    "ds_read_b64_tr_b16 v[54:55], v17 offset:9216\n"
    "s_waitcnt lgkmcnt(4)\n"
    "v_mfma_f32_16x16x32_bf16 v[32:35], v[28:31], v[56:59], v[32:35]\n"
    "v_mfma_f32_16x16x32_bf16 v[36:39], v[28:31], v[60:63], v[36:39]\n"
    "ds_read_b64_tr_b16 v[56:57], v16 offset:10240\n"
    "ds_read_b64_tr_b16 v[58:59], v16 offset:11264\n"
    "ds_read_b64_tr_b16 v[60:61], v17 offset:10240\n"
    "ds_read_b64_tr_b16 v[62:63], v17 offset:11264\n"
    "s_waitcnt lgkmcnt(4)\n"
    "v_mfma_f32_16x16x32_bf16 v[32:35], v[28:31], v[48:51], v[32:35]\n"
    "v_mfma_f32_16x16x32_bf16 v[36:39], v[28:31], v[52:55], v[36:39]\n"
    "ds_read_b64_tr_b16 v[48:49], v16 offset:12288\n"
    "ds_read_b64_tr_b16 v[50:51], v16 offset:13312\n"
    "ds_read_b64_tr_b16 v[52:53], v17 offset:12288\n"
    "ds_read_b64_tr_b16 v[54:55], v17 offset:13312\n"
    "s_waitcnt lgkmcnt(4)\n"
    "v_mfma_f32_16x16x32_bf16 v[32:35], v[28:31], v[56:59], v[32:35]\n"
    "v_mfma_f32_16x16x32_bf16 v[36:39], v[28:31], v[60:63], v[36:39]\n"
    "ds_read_b64_tr_b16 v[56:57], v16 offset:14336\n"
    "ds_read_b64_tr_b16 v[58:59], v16 offset:15360\n"
    "ds_read_b64_tr_b16 v[60:61], v17 offset:14336\n"
    "ds_read_b64_tr_b16 v[62:63], v17 offset:15360\n"
    "s_waitcnt lgkmcnt(4)\n"
    "v_mfma_f32_16x16x32_bf16 v[32:35], v[28:31], v[48:51], v[32:35]\n"
    "v_mfma_f32_16x16x32_bf16 v[36:39], v[28:31], v[52:55], v[36:39]\n"
    "s_waitcnt lgkmcnt(0)\n"
    "v_mfma_f32_16x16x32_bf16 v[32:35], v[28:31], v[56:59], v[32:35]\n"
    "v_mfma_f32_16x16x32_bf16 v[36:39], v[28:31], v[60:63], v[36:39]\n"
    "s_mul_i32 s0, s69, 3\n"
    "s_add_u32 s0, s0, s2\n"
    "s_lshl_b32 s0, s0, 7\n"
    "v_lshl_add_u32 v16, v114, 2, s0\n"
    "s_nop 2\n"
    "ds_write_b32 v16, v32 offset:55296\n"
    "ds_write_b32 v16, v36 offset:55360\n"
    "Lc_26_%=:\n"
    "s_waitcnt lgkmcnt(0)\n"
    "s_barrier\n"
    "s_branch Lqkv_10_%=\n"
    "Lloop_8_%=:\n"
    "s_lshl_b32 s2, s71, 21\n"
    "s_add_u32 s3, s2, s43\n"
    "v_cvt_pk_bf16_f32 v0, v116, v117\n"
    "v_cvt_pk_bf16_f32 v1, v118, v119\n"
    "v_cvt_pk_bf16_f32 v2, v120, v121\n"
    "v_cvt_pk_bf16_f32 v3, v122, v123\n"
    "v_min_u32 v0, -2, v0\n"
    "v_min_u32 v1, -2, v1\n"
    "v_min_u32 v2, -2, v2\n"
    "v_min_u32 v3, -2, v3\n"
    "buffer_store_dwordx4 v[0:3], v104, s[4:7], s3 offen sc1\n"
    "s_add_u32 s2, s2, s44\n"
    "v_add_u32 v82, s2, v104\n"
    "v_add_u32 v83, 16384, v82\n"
    "s_lshl_b32 s1, s37, 2\n"
    "s_add_u32 m0, s1, 65536\n"
    "s_add_u32 s0, s54, 0\n"
    "buffer_load_dwordx4 v104, s[4:7], s0 offen offset:0 sc1 lds\n"
    "buffer_load_dwordx4 v104, s[4:7], s0 offen offset:1024 sc1 lds\n"
    "buffer_load_dwordx4 v104, s[4:7], s0 offen offset:2048 sc1 lds\n"
    "buffer_load_dwordx4 v104, s[4:7], s0 offen offset:3072 sc1 lds\n"
    "s_lshl_b32 s0, s68, 8\n"
    "s_bfe_u32 s1, s36, 0x10001\n"
    "s_lshl_b32 s1, s1, 3\n"
    "s_add_u32 s0, s0, s1\n"
    "s_add_u32 s0, s0, 0\n"
    "v_pk_add_f32 v[4:5], v[116:117], v[118:119]\n"
    "v_pk_add_f32 v[6:7], v[120:121], v[122:123]\n"
    "v_pk_add_f32 v[4:5], v[4:5], v[6:7]\n"
    "v_add_f32 v4, v4, v5\n"
    "v_lshl_add_u32 v16, v114, 4, s0\n"
    "v_mov_b32 v5, v4\n"
    "s_nop 1\n"
    "v_permlane16_swap_b32 v4, v5\n"
    "v_add_f32 v4, v4, v5\n"
    "v_mov_b32 v5, v4\n"
    "s_nop 1\n"
    "v_permlane32_swap_b32 v4, v5\n"
    "v_add_f32 v4, v4, v5\n"
    "v_mul_f32 v4, 0x3d000000, v4\n"
    "v_pk_add_f32 v[8:9], v[116:117], v[4:5] op_sel_hi:[1,0] neg_lo:[0,1] neg_hi:[0,1]\n"
    "v_pk_add_f32 v[10:11], v[118:119], v[4:5] op_sel_hi:[1,0] neg_lo:[0,1] neg_hi:[0,1]\n"
    "v_pk_add_f32 v[12:13], v[120:121], v[4:5] op_sel_hi:[1,0] neg_lo:[0,1] neg_hi:[0,1]\n"
    "v_pk_add_f32 v[14:15], v[122:123], v[4:5] op_sel_hi:[1,0] neg_lo:[0,1] neg_hi:[0,1]\n"
    "v_pk_mul_f32 v[8:9], v[8:9], v[8:9]\n"
    "v_pk_mul_f32 v[12:13], v[12:13], v[12:13]\n"
    "v_pk_fma_f32 v[8:9], v[10:11], v[10:11], v[8:9]\n"
    "v_pk_fma_f32 v[12:13], v[14:15], v[14:15], v[12:13]\n"
    "v_pk_add_f32 v[8:9], v[8:9], v[12:13]\n"
    "v_add_f32 v5, v8, v9\n"
    "v_mov_b32 v6, v5\n"
    "s_nop 1\n"
    "v_permlane16_swap_b32 v5, v6\n"
    "v_add_f32 v5, v5, v6\n"
    "v_mov_b32 v6, v5\n"
    "s_nop 1\n"
    "v_permlane32_swap_b32 v5, v6\n"
    "v_add_f32 v5, v5, v6\n"
    "ds_write_b64 v16, v[4:5]\n"
    "s_waitcnt vmcnt(4) lgkmcnt(0)\n"
    "s_barrier\n"
    "s_cmp_eq_u32 s36, 0\n"
    "s_cbranch_scc0 Lw_27_%=\n"
    "s_add_u32 s3, s62, 131072\n"
    "v_add_u32 v16, 0, v104\n"
    "ds_read_b128 v[4:7], v16\n"
    "v_lshrrev_b32 v17, 1, v104\n"
    "s_waitcnt lgkmcnt(0)\n"
    "v_pk_add_f32 v[12:13], v[4:5], v[6:7]\n"
    "v_sub_f32 v4, v4, v6\n"
    "v_mul_f32 v12, 0.5, v12\n"
    "v_mul_f32 v4, v4, v4\n"
    "v_fmac_f32 v13, 0x41800000, v4\n"
    "v_min_u32 v12, -2, v12\n"
    "v_min_u32 v13, -2, v13\n"
    "buffer_store_dwordx2 v[12:13], v17, s[4:7], s3 offen sc1\n"
    "Lw_27_%=:\n"
    "s_sleep 6\n"
    "s_mov_b32 s84, 0\n"
    "buffer_load_dwordx4 v[128:131], v82, s[4:7], s84 offen offset:0 sc1\n"
    "buffer_load_dwordx4 v[132:135], v82, s[4:7], s84 offen offset:1024 sc1\n"
    "buffer_load_dwordx4 v[136:139], v83, s[4:7], s84 offen offset:0 sc1\n"
    "buffer_load_dwordx4 v[140:143], v83, s[4:7], s84 offen offset:1024 sc1\n"
    "s_waitcnt vmcnt(4) lgkmcnt(0)\n"
    "s_barrier\n"
    "s_mov_b32 s85, 2048\n"
    "buffer_load_dwordx4 v[144:147], v82, s[4:7], s85 offen offset:0 sc1\n"
    "buffer_load_dwordx4 v[148:151], v82, s[4:7], s85 offen offset:1024 sc1\n"
    "buffer_load_dwordx4 v[152:155], v83, s[4:7], s85 offen offset:0 sc1\n"
    "buffer_load_dwordx4 v[156:159], v83, s[4:7], s85 offen offset:1024 sc1\n"
    "s_mov_b32 s86, 0\n"
    "s_mov_b32 s96, 0\n"
    "Lqkv_10_%=:\n"
    "v_xor_b32 v64, 32, v111\n"
    "v_add_u32 v65, 0x10000, v111\n"
    "v_add_u32 v66, 0x10000, v64\n"
    "ds_read_b64_tr_b16 v[48:49], v111 offset:0\n"
    "ds_read_b64_tr_b16 v[50:51], v111 offset:1024\n"
    "ds_read_b64_tr_b16 v[52:53], v111 offset:2048\n"
    "ds_read_b64_tr_b16 v[54:55], v111 offset:3072\n"
    "ds_read_b64_tr_b16 v[56:57], v64 offset:0\n"
    "ds_read_b64_tr_b16 v[58:59], v64 offset:1024\n"
    "ds_read_b64_tr_b16 v[60:61], v64 offset:2048\n"
    "ds_read_b64_tr_b16 v[62:63], v64 offset:3072\n"
    "s_waitcnt vmcnt(4)\n"
    "v_max_u32 v124, v128, v129\n"
    "v_max3_u32 v124, v124, v130, v131\n"
    "v_max3_u32 v124, v124, v132, v133\n"
    "v_max3_u32 v124, v124, v134, v135\n"
    "v_max3_u32 v124, v124, v136, v137\n"
    "v_max3_u32 v124, v124, v138, v139\n"
    "v_max3_u32 v124, v124, v140, v141\n"
    "v_max3_u32 v124, v124, v142, v143\n"
    "v_cmp_eq_u32 vcc, -1, v124\n"
    "s_cbranch_vccnz Lno_30_%=\n"
    "Lok_31_%=:\n"
    "ds_read_b64_tr_b16 v[248:249], v111 offset:32768\n"
    "ds_read_b64_tr_b16 v[250:251], v111 offset:33792\n"
    "ds_read_b64_tr_b16 v[252:253], v111 offset:34816\n"
    "ds_read_b64_tr_b16 v[254:255], v111 offset:35840\n"
    "s_waitcnt lgkmcnt(8)\n"
    "v_mfma_f32_16x16x32_bf16 v[0:3], v[48:51], v[128:131], 0\n"
    "v_mfma_f32_16x16x32_bf16 v[0:3], v[52:55], v[132:135], v[0:3]\n"
    "s_lshl_b32 s3, s71, 17\n"
    "s_add_u32 s3, s3, 983040\n"
    "v_mfma_f32_16x16x32_bf16 v[4:7], v[48:51], v[136:139], 0\n"
    "v_mfma_f32_16x16x32_bf16 v[4:7], v[52:55], v[140:143], v[4:7]\n"
    "s_mul_i32 s0, s37, 6\n"
    "v_add_u32 v84, s0, v104\n"
    "s_mov_b32 s86, 4096\n"
    "buffer_load_dwordx4 v[160:163], v82, s[4:7], s86 offen offset:0 sc1\n"
    "ds_read_b64_tr_b16 v[48:49], v64 offset:32768\n"
    "ds_read_b64_tr_b16 v[50:51], v64 offset:33792\n"
    "ds_read_b64_tr_b16 v[52:53], v64 offset:34816\n"
    "ds_read_b64_tr_b16 v[54:55], v64 offset:35840\n"
    "s_waitcnt lgkmcnt(8)\n"
    "v_mfma_f32_16x16x32_bf16 v[8:11], v[56:59], v[128:131], 0\n"
    "v_mfma_f32_16x16x32_bf16 v[8:11], v[60:63], v[132:135], v[8:11]\n"
    "s_xor_b32 s0, s37, 0x1000\n"
    "v_mfma_f32_16x16x32_bf16 v[12:15], v[56:59], v[136:139], 0\n"
    "v_mfma_f32_16x16x32_bf16 v[12:15], v[60:63], v[140:143], v[12:15]\n"
    "s_mul_i32 s0, s0, 6\n"
    "v_add_u32 v85, s0, v104\n"
    "buffer_load_dwordx4 v[164:167], v82, s[4:7], s86 offen offset:1024 sc1\n"
    "ds_read_b64_tr_b16 v[56:57], v65 offset:0\n"
    "ds_read_b64_tr_b16 v[58:59], v65 offset:1024\n"
    "ds_read_b64_tr_b16 v[60:61], v65 offset:2048\n"
    "ds_read_b64_tr_b16 v[62:63], v65 offset:3072\n"
    "s_waitcnt lgkmcnt(8)\n"
    "v_mfma_f32_16x16x32_bf16 v[16:19], v[248:251], v[128:131], 0\n"
    "v_mfma_f32_16x16x32_bf16 v[16:19], v[252:255], v[132:135], v[16:19]\n"
    "s_lshr_b32 s0, s37, 2\n"
    "s_add_u32 s0, s0, 49152\n"
    "v_mfma_f32_16x16x32_bf16 v[20:23], v[248:251], v[136:139], 0\n"
    "v_mfma_f32_16x16x32_bf16 v[20:23], v[252:255], v[140:143], v[20:23]\n"
    "v_bfe_u32 v100, v104, 4, 5\n"
    "v_lshl_add_u32 v100, v100, 2, s0\n"
    "buffer_load_dwordx4 v[168:171], v83, s[4:7], s86 offen offset:0 sc1\n"
    "ds_read_b64_tr_b16 v[248:249], v66 offset:0\n"
    "ds_read_b64_tr_b16 v[250:251], v66 offset:1024\n"
    "ds_read_b64_tr_b16 v[252:253], v66 offset:2048\n"
    "ds_read_b64_tr_b16 v[254:255], v66 offset:3072\n"
    "s_waitcnt lgkmcnt(8)\n"
    "v_mfma_f32_16x16x32_bf16 v[24:27], v[48:51], v[128:131], 0\n"
    "v_mfma_f32_16x16x32_bf16 v[24:27], v[52:55], v[132:135], v[24:27]\n"
    "v_lshl_add_u32 v101, v114, 2, s0\n"
    "v_mfma_f32_16x16x32_bf16 v[28:31], v[48:51], v[136:139], 0\n"
    "v_mfma_f32_16x16x32_bf16 v[28:31], v[52:55], v[140:143], v[28:31]\n"
    "v_lshl_add_u32 v102, v115, 4, s0\n"
    "buffer_load_dwordx4 v[172:175], v83, s[4:7], s86 offen offset:1024 sc1\n"
    "ds_read_b64_tr_b16 v[48:49], v111 offset:4096\n"
    "ds_read_b64_tr_b16 v[50:51], v111 offset:5120\n"
    "ds_read_b64_tr_b16 v[52:53], v111 offset:6144\n"
    "ds_read_b64_tr_b16 v[54:55], v111 offset:7168\n"
    "s_waitcnt lgkmcnt(8)\n"
    "v_mfma_f32_16x16x32_bf16 v[32:35], v[128:131], v[56:59], 0\n"
    "v_mfma_f32_16x16x32_bf16 v[32:35], v[132:135], v[60:63], v[32:35]\n"
    "s_lshl_b32 s1, s69, 6\n"
    "s_add_u32 s1, s1, 55296\n"
    "v_mfma_f32_16x16x32_bf16 v[36:39], v[136:139], v[56:59], 0\n"
    "v_mfma_f32_16x16x32_bf16 v[36:39], v[140:143], v[60:63], v[36:39]\n"
    "v_lshl_add_u32 v68, v115, 4, s1\n"
    "v_lshl_add_u32 v69, v114, 2, s1\n"
    "ds_read_b64_tr_b16 v[56:57], v64 offset:4096\n"
    "ds_read_b64_tr_b16 v[58:59], v64 offset:5120\n"
    "ds_read_b64_tr_b16 v[60:61], v64 offset:6144\n"
    "ds_read_b64_tr_b16 v[62:63], v64 offset:7168\n"
    "s_waitcnt lgkmcnt(8)\n"
    "v_mfma_f32_16x16x32_bf16 v[40:43], v[128:131], v[248:251], 0\n"
    "v_mfma_f32_16x16x32_bf16 v[40:43], v[132:135], v[252:255], v[40:43]\n"
    "v_mfma_f32_16x16x32_bf16 v[44:47], v[136:139], v[248:251], 0\n"
    "v_mfma_f32_16x16x32_bf16 v[44:47], v[140:143], v[252:255], v[44:47]\n"
    "s_waitcnt vmcnt(4)\n"
    "v_max_u32 v124, v144, v145\n"
    "v_max3_u32 v124, v124, v146, v147\n"
    "v_max3_u32 v124, v124, v148, v149\n"
    "v_max3_u32 v124, v124, v150, v151\n"
    "v_max3_u32 v124, v124, v152, v153\n"
    "v_max3_u32 v124, v124, v154, v155\n"
    "v_max3_u32 v124, v124, v156, v157\n"
    "v_max3_u32 v124, v124, v158, v159\n"
    "v_cmp_eq_u32 vcc, -1, v124\n"
    "s_cbranch_vccnz Lno_32_%=\n"
    "Lok_33_%=:\n"
    "ds_read_b64_tr_b16 v[248:249], v111 offset:36864\n"
    "ds_read_b64_tr_b16 v[250:251], v111 offset:37888\n"
    "ds_read_b64_tr_b16 v[252:253], v111 offset:38912\n"
    "ds_read_b64_tr_b16 v[254:255], v111 offset:39936\n"
    "s_waitcnt lgkmcnt(8)\n"
    "ds_read_b128 v[176:179], v68 offset:0\n"
    "ds_read_b128 v[180:183], v68 offset:384\n"
    "ds_read_b128 v[184:187], v68 offset:128\n"
    "ds_read_b128 v[188:191], v68 offset:512\n"
    "v_mfma_f32_16x16x32_bf16 v[0:3], v[48:51], v[144:147], v[0:3]\n"
    "v_mfma_f32_16x16x32_bf16 v[0:3], v[52:55], v[148:151], v[0:3]\n"
    "v_mfma_f32_16x16x32_bf16 v[4:7], v[48:51], v[152:155], v[4:7]\n"
    "v_mfma_f32_16x16x32_bf16 v[4:7], v[52:55], v[156:159], v[4:7]\n"
    "s_mov_b32 s84, 6144\n"
    "buffer_load_dwordx4 v[128:131], v82, s[4:7], s84 offen offset:0 sc1\n"
    "ds_read_b64_tr_b16 v[48:49], v64 offset:36864\n"
    "ds_read_b64_tr_b16 v[50:51], v64 offset:37888\n"
    "ds_read_b64_tr_b16 v[52:53], v64 offset:38912\n"
    "ds_read_b64_tr_b16 v[54:55], v64 offset:39936\n"
    "s_waitcnt lgkmcnt(12)\n"
    "ds_read_b32 v70, v69 offset:256\n"
    "ds_read_b32 v71, v69 offset:640\n"
    "v_mfma_f32_16x16x32_bf16 v[8:11], v[56:59], v[144:147], v[8:11]\n"
    "v_mfma_f32_16x16x32_bf16 v[8:11], v[60:63], v[148:151], v[8:11]\n"
    "v_mfma_f32_16x16x32_bf16 v[12:15], v[56:59], v[152:155], v[12:15]\n"
    "v_mfma_f32_16x16x32_bf16 v[12:15], v[60:63], v[156:159], v[12:15]\n"
    "buffer_load_dwordx4 v[132:135], v82, s[4:7], s84 offen offset:1024 sc1\n"
    "ds_read_b64_tr_b16 v[56:57], v65 offset:4096\n"
    "ds_read_b64_tr_b16 v[58:59], v65 offset:5120\n"
    "ds_read_b64_tr_b16 v[60:61], v65 offset:6144\n"
    "ds_read_b64_tr_b16 v[62:63], v65 offset:7168\n"
    "s_waitcnt lgkmcnt(14)\n"
    "v_mfma_f32_16x16x32_bf16 v[16:19], v[248:251], v[144:147], v[16:19]\n"
    "v_mfma_f32_16x16x32_bf16 v[16:19], v[252:255], v[148:151], v[16:19]\n"
    "v_mfma_f32_16x16x32_bf16 v[20:23], v[248:251], v[152:155], v[20:23]\n"
    "v_mfma_f32_16x16x32_bf16 v[20:23], v[252:255], v[156:159], v[20:23]\n"
    "buffer_load_dwordx4 v[136:139], v83, s[4:7], s84 offen offset:0 sc1\n"
    "buffer_load_dwordx2 v[72:73], v112, s[4:7], s3 offen offset:0 sc1\n"
    "buffer_load_dwordx2 v[74:75], v112, s[4:7], s3 offen offset:512 sc1\n"
    "buffer_load_dwordx2 v[76:77], v112, s[4:7], s3 offen offset:1024 sc1\n"
    "buffer_load_dwordx2 v[78:79], v112, s[4:7], s3 offen offset:1536 sc1\n"
    "ds_read_b64_tr_b16 v[248:249], v66 offset:4096\n"
    "ds_read_b64_tr_b16 v[250:251], v66 offset:5120\n"
    "ds_read_b64_tr_b16 v[252:253], v66 offset:6144\n"
    "ds_read_b64_tr_b16 v[254:255], v66 offset:7168\n"
    "s_waitcnt lgkmcnt(8)\n"
    "v_mfma_f32_16x16x32_bf16 v[24:27], v[48:51], v[144:147], v[24:27]\n"
    "v_mfma_f32_16x16x32_bf16 v[24:27], v[52:55], v[148:151], v[24:27]\n"
    "v_add_f32 v176, v176, v180\n"
    "v_mfma_f32_16x16x32_bf16 v[28:31], v[48:51], v[152:155], v[28:31]\n"
    "v_mfma_f32_16x16x32_bf16 v[28:31], v[52:55], v[156:159], v[28:31]\n"
    "v_add_f32 v177, v177, v181\n"
    "v_add_f32 v178, v178, v182\n"
    "buffer_load_dwordx4 v[140:143], v83, s[4:7], s84 offen offset:1024 sc1\n"
    "ds_read_b64_tr_b16 v[48:49], v111 offset:8192\n"
    "ds_read_b64_tr_b16 v[50:51], v111 offset:9216\n"
    "ds_read_b64_tr_b16 v[52:53], v111 offset:10240\n"
    "ds_read_b64_tr_b16 v[54:55], v111 offset:11264\n"
    "s_waitcnt lgkmcnt(8)\n"
    "v_mfma_f32_16x16x32_bf16 v[32:35], v[144:147], v[56:59], v[32:35]\n"
    "v_mfma_f32_16x16x32_bf16 v[32:35], v[148:151], v[60:63], v[32:35]\n"
    "v_add_f32 v179, v179, v183\n"
    "v_mfma_f32_16x16x32_bf16 v[36:39], v[152:155], v[56:59], v[36:39]\n"
    "v_mfma_f32_16x16x32_bf16 v[36:39], v[156:159], v[60:63], v[36:39]\n"
    "v_add_f32 v184, v184, v188\n"
    "v_add_f32 v185, v185, v189\n"
    "ds_read_b64_tr_b16 v[56:57], v64 offset:8192\n"
    "ds_read_b64_tr_b16 v[58:59], v64 offset:9216\n"
    "ds_read_b64_tr_b16 v[60:61], v64 offset:10240\n"
    "ds_read_b64_tr_b16 v[62:63], v64 offset:11264\n"
    "s_waitcnt lgkmcnt(8)\n"
    "v_mfma_f32_16x16x32_bf16 v[40:43], v[144:147], v[248:251], v[40:43]\n"
    "v_mfma_f32_16x16x32_bf16 v[40:43], v[148:151], v[252:255], v[40:43]\n"
    "v_add_f32 v186, v186, v190\n"
    "v_mfma_f32_16x16x32_bf16 v[44:47], v[152:155], v[248:251], v[44:47]\n"
    "v_mfma_f32_16x16x32_bf16 v[44:47], v[156:159], v[252:255], v[44:47]\n"
    "v_add_f32 v187, v187, v191\n"
    "v_add_f32 v70, v70, v71\n"
    "s_waitcnt vmcnt(8)\n"
    "v_max_u32 v124, v160, v161\n"
    "v_max3_u32 v124, v124, v162, v163\n"
    "v_max3_u32 v124, v124, v164, v165\n"
    "v_max3_u32 v124, v124, v166, v167\n"
    "v_max3_u32 v124, v124, v168, v169\n"
    "v_max3_u32 v124, v124, v170, v171\n"
    "v_max3_u32 v124, v124, v172, v173\n"
    "v_max3_u32 v124, v124, v174, v175\n"
    "v_cmp_eq_u32 vcc, -1, v124\n"
    "s_cbranch_vccnz Lno_34_%=\n"
    "Lok_35_%=:\n"
    "s_waitcnt vmcnt(1)\n"
    "Lrt_36_%=:\n"
    "v_max3_u32 v80, v72, v73, v74\n"
    "v_max3_u32 v80, v80, v75, v76\n"
    "v_max3_u32 v80, v80, v77, v78\n"
    "v_max_u32 v80, v80, v79\n"
    "v_cmp_eq_u32 vcc, -1, v80\n"
    "s_cbranch_vccz Lrk_37_%=\n"
    "buffer_load_dwordx2 v[72:73], v112, s[4:7], s3 offen offset:0 sc1\n"
    "buffer_load_dwordx2 v[74:75], v112, s[4:7], s3 offen offset:512 sc1\n"
    "buffer_load_dwordx2 v[76:77], v112, s[4:7], s3 offen offset:1024 sc1\n"
    "buffer_load_dwordx2 v[78:79], v112, s[4:7], s3 offen offset:1536 sc1\n"
    "s_add_u32 s96, s96, 1\n"
    "s_cmp_lt_u32 s96, 65536\n"
    "s_waitcnt vmcnt(0)\n"
    "s_cbranch_scc1 Lrt_36_%=\n"
    "Lrk_37_%=:\n"
    "ds_read_b64_tr_b16 v[248:249], v111 offset:40960\n"
    "ds_read_b64_tr_b16 v[250:251], v111 offset:41984\n"
    "ds_read_b64_tr_b16 v[252:253], v111 offset:43008\n"
    "ds_read_b64_tr_b16 v[254:255], v111 offset:44032\n"
    "s_waitcnt lgkmcnt(8)\n"
    "v_mfma_f32_16x16x32_bf16 v[0:3], v[48:51], v[160:163], v[0:3]\n"
    "v_mfma_f32_16x16x32_bf16 v[0:3], v[52:55], v[164:167], v[0:3]\n"
    "v_add_f32 v94, v72, v74\n"
    "v_add_f32 v95, v76, v78\n"
    "v_add_f32 v94, v94, v95\n"
    "v_mfma_f32_16x16x32_bf16 v[4:7], v[48:51], v[168:171], v[4:7]\n"
    "v_mfma_f32_16x16x32_bf16 v[4:7], v[52:55], v[172:175], v[4:7]\n"
    "v_mov_b32 v95, v94\n"
    "s_nop 1\n"
    "v_permlane32_swap_b32 v94, v95\n"
    "v_add_f32 v94, v94, v95\n"
    "ds_read_b64_tr_b16 v[48:49], v64 offset:40960\n"
    "ds_read_b64_tr_b16 v[50:51], v64 offset:41984\n"
    "ds_read_b64_tr_b16 v[52:53], v64 offset:43008\n"
    "ds_read_b64_tr_b16 v[54:55], v64 offset:44032\n"
    "s_waitcnt lgkmcnt(8)\n"
    "v_mfma_f32_16x16x32_bf16 v[8:11], v[56:59], v[160:163], v[8:11]\n"
    "v_mfma_f32_16x16x32_bf16 v[8:11], v[60:63], v[164:167], v[8:11]\n"
    "v_mul_f32 v94, 0x3e000000, v94\n"
    "v_sub_f32 v72, v72, v94\n"
    "v_sub_f32 v74, v74, v94\n"
    "v_mfma_f32_16x16x32_bf16 v[12:15], v[56:59], v[168:171], v[12:15]\n"
    "v_mfma_f32_16x16x32_bf16 v[12:15], v[60:63], v[172:175], v[12:15]\n"
    "v_sub_f32 v76, v76, v94\n"
    "v_sub_f32 v78, v78, v94\n"
    "v_add_f32 v95, v73, v75\n"
    "ds_read_b64_tr_b16 v[56:57], v65 offset:8192\n"
    "ds_read_b64_tr_b16 v[58:59], v65 offset:9216\n"
    "ds_read_b64_tr_b16 v[60:61], v65 offset:10240\n"
    "ds_read_b64_tr_b16 v[62:63], v65 offset:11264\n"
    "s_waitcnt lgkmcnt(8)\n"
    "v_mfma_f32_16x16x32_bf16 v[16:19], v[248:251], v[160:163], v[16:19]\n"
    "v_mfma_f32_16x16x32_bf16 v[16:19], v[252:255], v[164:167], v[16:19]\n"
    "v_add_f32 v96, v77, v79\n"
    "v_mul_f32 v97, v72, v72\n"
    "v_mul_f32 v98, v76, v76\n"
    "v_mfma_f32_16x16x32_bf16 v[20:23], v[248:251], v[168:171], v[20:23]\n"
    "v_mfma_f32_16x16x32_bf16 v[20:23], v[252:255], v[172:175], v[20:23]\n"
    "v_fmac_f32 v97, v74, v74\n"
    "v_fmac_f32 v98, v78, v78\n"
    "v_add_f32 v95, v95, v96\n"
    "ds_read_b64_tr_b16 v[248:249], v66 offset:8192\n"
    "ds_read_b64_tr_b16 v[250:251], v66 offset:9216\n"
    "ds_read_b64_tr_b16 v[252:253], v66 offset:10240\n"
    "ds_read_b64_tr_b16 v[254:255], v66 offset:11264\n"
    "s_waitcnt lgkmcnt(8)\n"
    "v_mfma_f32_16x16x32_bf16 v[24:27], v[48:51], v[160:163], v[24:27]\n"
    "v_mfma_f32_16x16x32_bf16 v[24:27], v[52:55], v[164:167], v[24:27]\n"
    "v_add_f32 v97, v97, v98\n"
    "v_fmac_f32 v95, 0x42800000, v97\n"
    "v_mov_b32 v96, v95\n"
    "v_mfma_f32_16x16x32_bf16 v[28:31], v[48:51], v[168:171], v[28:31]\n"
    "v_mfma_f32_16x16x32_bf16 v[28:31], v[52:55], v[172:175], v[28:31]\n"
    "v_permlane32_swap_b32 v95, v96\n"
    "v_add_f32 v95, v95, v96\n"
    "v_mov_b32 v96, 0x3727c5ac\n"
    "ds_read_b64_tr_b16 v[48:49], v111 offset:12288\n"
    "ds_read_b64_tr_b16 v[50:51], v111 offset:13312\n"
    "ds_read_b64_tr_b16 v[52:53], v111 offset:14336\n"
    "ds_read_b64_tr_b16 v[54:55], v111 offset:15360\n"
    "s_waitcnt lgkmcnt(8)\n"
    "v_mfma_f32_16x16x32_bf16 v[32:35], v[160:163], v[56:59], v[32:35]\n"
    "v_mfma_f32_16x16x32_bf16 v[32:35], v[164:167], v[60:63], v[32:35]\n"
    "v_fmac_f32 v96, 0x3b000000, v95\n"
    "v_mfma_f32_16x16x32_bf16 v[36:39], v[168:171], v[56:59], v[36:39]\n"
    "v_mfma_f32_16x16x32_bf16 v[36:39], v[172:175], v[60:63], v[36:39]\n"
    "v_rsq_f32 v96, v96\n"
    "s_nop 0\n"
    "v_mul_f32 v94, v94, v96\n"
    "ds_read_b64_tr_b16 v[56:57], v64 offset:12288\n"
    "ds_read_b64_tr_b16 v[58:59], v64 offset:13312\n"
    "ds_read_b64_tr_b16 v[60:61], v64 offset:14336\n"
    "ds_read_b64_tr_b16 v[62:63], v64 offset:15360\n"
    "s_waitcnt lgkmcnt(8)\n"
    "ds_write_b32 v100, v96\n"
    "ds_write_b32 v100, v94 offset:128\n"
    "v_mfma_f32_16x16x32_bf16 v[40:43], v[160:163], v[248:251], v[40:43]\n"
    "v_mfma_f32_16x16x32_bf16 v[40:43], v[164:167], v[252:255], v[40:43]\n"
    "v_mfma_f32_16x16x32_bf16 v[44:47], v[168:171], v[248:251], v[44:47]\n"
    "v_mfma_f32_16x16x32_bf16 v[44:47], v[172:175], v[252:255], v[44:47]\n"
    "s_waitcnt vmcnt(0)\n"
    "v_max_u32 v124, v128, v129\n"
    "v_max3_u32 v124, v124, v130, v131\n"
    "v_max3_u32 v124, v124, v132, v133\n"
    "v_max3_u32 v124, v124, v134, v135\n"
    "v_max3_u32 v124, v124, v136, v137\n"
    "v_max3_u32 v124, v124, v138, v139\n"
    "v_max3_u32 v124, v124, v140, v141\n"
    "v_max3_u32 v124, v124, v142, v143\n"
    "v_cmp_eq_u32 vcc, -1, v124\n"
    "s_cbranch_vccnz Lno_38_%=\n"
    "Lok_39_%=:\n"
    "ds_read_b64_tr_b16 v[248:249], v111 offset:45056\n"
    "ds_read_b64_tr_b16 v[250:251], v111 offset:46080\n"
    "ds_read_b64_tr_b16 v[252:253], v111 offset:47104\n"
    "ds_read_b64_tr_b16 v[254:255], v111 offset:48128\n"
    "s_waitcnt lgkmcnt(10)\n"
    "ds_read_b32 v86, v101 offset:0\n"
    "ds_read_b32 v88, v101 offset:64\n"
    "ds_read_b32 v90, v101 offset:128\n"
    "ds_read_b32 v92, v101 offset:192\n"
    "v_mfma_f32_16x16x32_bf16 v[0:3], v[48:51], v[128:131], v[0:3]\n"
    "v_mfma_f32_16x16x32_bf16 v[0:3], v[52:55], v[132:135], v[0:3]\n"
    "v_mfma_f32_16x16x32_bf16 v[4:7], v[48:51], v[136:139], v[4:7]\n"
    "v_mfma_f32_16x16x32_bf16 v[4:7], v[52:55], v[140:143], v[4:7]\n"
    "ds_read_b64_tr_b16 v[48:49], v64 offset:45056\n"
    "ds_read_b64_tr_b16 v[50:51], v64 offset:46080\n"
    "ds_read_b64_tr_b16 v[52:53], v64 offset:47104\n"
    "ds_read_b64_tr_b16 v[54:55], v64 offset:48128\n"
    "s_waitcnt lgkmcnt(14)\n"
    "ds_read_b128 v[192:195], v102 offset:0\n"
    "ds_read_b128 v[196:199], v102 offset:64\n"
    "ds_read_b128 v[200:203], v102 offset:128\n"
    "ds_read_b128 v[204:207], v102 offset:192\n"
    "v_mfma_f32_16x16x32_bf16 v[8:11], v[56:59], v[128:131], v[8:11]\n"
    "v_mfma_f32_16x16x32_bf16 v[8:11], v[60:63], v[132:135], v[8:11]\n"
    "v_mfma_f32_16x16x32_bf16 v[12:15], v[56:59], v[136:139], v[12:15]\n"
    "v_mfma_f32_16x16x32_bf16 v[12:15], v[60:63], v[140:143], v[12:15]\n"
    "ds_read_b64_tr_b16 v[56:57], v65 offset:12288\n"
    "ds_read_b64_tr_b16 v[58:59], v65 offset:13312\n"
    "ds_read_b64_tr_b16 v[60:61], v65 offset:14336\n"
    "ds_read_b64_tr_b16 v[62:63], v65 offset:15360\n"
    "s_waitcnt lgkmcnt(4)\n"
    "v_mfma_f32_16x16x32_bf16 v[16:19], v[248:251], v[128:131], v[16:19]\n"
    "v_mfma_f32_16x16x32_bf16 v[16:19], v[252:255], v[132:135], v[16:19]\n"
    "v_mul_f32 v87, 0x3eb8aa3b, v86\n"
    "v_mul_f32 v89, 0x3eb8aa3b, v88\n"
    "v_mul_f32 v91, 0x3eb8aa3b, v90\n"
    "v_mfma_f32_16x16x32_bf16 v[20:23], v[248:251], v[136:139], v[20:23]\n"
    "v_mfma_f32_16x16x32_bf16 v[20:23], v[252:255], v[140:143], v[20:23]\n"
    "v_mul_f32 v93, 0x3eb8aa3b, v92\n"
    "v_mul_f32 v232, v91, v176\n"
    "v_mul_f32 v233, v91, v177\n"
    "v_mul_f32 v234, v91, v178\n"
    "s_cmp_eq_u32 s69, 0\n"
    "s_cbranch_scc1 Lx1_40_%=\n"
    "ds_write_b128 v84, v[0:3] offset:0\n"
    "ds_write_b128 v84, v[4:7] offset:1024\n"
    "s_branch Lx2_41_%=\n"
    "Lx1_40_%=:\n"
    "s_nop 1\n"
    "ds_write_b128 v84, v[8:11] offset:0\n"
    "ds_write_b128 v84, v[12:15] offset:1024\n"
    "Lx2_41_%=:\n"
    "ds_read_b64_tr_b16 v[248:249], v66 offset:12288\n"
    "ds_read_b64_tr_b16 v[250:251], v66 offset:13312\n"
    "ds_read_b64_tr_b16 v[252:253], v66 offset:14336\n"
    "ds_read_b64_tr_b16 v[254:255], v66 offset:15360\n"
    "s_waitcnt lgkmcnt(14)\n"
    "v_mfma_f32_16x16x32_bf16 v[24:27], v[48:51], v[128:131], v[24:27]\n"
    "v_mfma_f32_16x16x32_bf16 v[24:27], v[52:55], v[132:135], v[24:27]\n"
    "v_mul_f32 v235, v91, v179\n"
    "v_mul_f32 v236, v93, v176\n"
    "v_mul_f32 v237, v93, v177\n"
    "v_mfma_f32_16x16x32_bf16 v[28:31], v[48:51], v[136:139], v[28:31]\n"
    "v_mfma_f32_16x16x32_bf16 v[28:31], v[52:55], v[140:143], v[28:31]\n"
    "v_mul_f32 v238, v93, v178\n"
    "v_mul_f32 v239, v93, v179\n"
    "v_mul_f32 v240, v90, v184\n"
    "v_mul_f32 v241, v90, v185\n"
    "s_waitcnt lgkmcnt(6)\n"
    "v_mfma_f32_16x16x32_bf16 v[32:35], v[128:131], v[56:59], v[32:35]\n"
    "v_mfma_f32_16x16x32_bf16 v[32:35], v[132:135], v[60:63], v[32:35]\n"
    "v_mul_f32 v242, v90, v186\n"
    "v_mul_f32 v243, v90, v187\n"
    "v_mul_f32 v244, v92, v184\n"
    "v_mfma_f32_16x16x32_bf16 v[36:39], v[136:139], v[56:59], v[36:39]\n"
    "v_mfma_f32_16x16x32_bf16 v[36:39], v[140:143], v[60:63], v[36:39]\n"
    "v_mul_f32 v245, v92, v185\n"
    "v_mul_f32 v246, v92, v186\n"
    "v_mul_f32 v247, v92, v187\n"
    "v_mul_f32 v200, v70, v200\n"
    "s_cmp_eq_u32 s69, 0\n"
    "s_cbranch_scc1 Lx1_42_%=\n"
    "ds_write_b128 v84, v[16:19] offset:2048\n"
    "ds_write_b128 v84, v[20:23] offset:3072\n"
    "s_branch Lx2_43_%=\n"
    "Lx1_42_%=:\n"
    "s_nop 1\n"
    "ds_write_b128 v84, v[24:27] offset:2048\n"
    "ds_write_b128 v84, v[28:31] offset:3072\n"
    "Lx2_43_%=:\n"
    "s_waitcnt lgkmcnt(2)\n"
    "v_mfma_f32_16x16x32_bf16 v[40:43], v[128:131], v[248:251], v[40:43]\n"
    "v_mfma_f32_16x16x32_bf16 v[40:43], v[132:135], v[252:255], v[40:43]\n"
    "v_mul_f32 v201, v70, v201\n"
    "v_mul_f32 v202, v70, v202\n"
    "v_mul_f32 v203, v70, v203\n"
    "v_mfma_f32_16x16x32_bf16 v[44:47], v[136:139], v[248:251], v[44:47]\n"
    "v_mfma_f32_16x16x32_bf16 v[44:47], v[140:143], v[252:255], v[44:47]\n"
    "v_mul_f32 v204, v70, v204\n"
    "v_mul_f32 v205, v70, v205\n"
    "v_mul_f32 v206, v70, v206\n"
    "v_mul_f32 v207, v70, v207\n"
    "s_add_u32 s1, s70, s38\n"
    "s_add_u32 s2, s1, 2097152\n"
    "s_add_u32 s3, s1, 4194304\n"
    "s_cmp_eq_u32 s69, 0\n"
    "s_cbranch_scc1 Lkh_44_%=\n"
    "ds_write_b128 v84, v[32:35] offset:4096\n"
    "ds_write_b128 v84, v[36:39] offset:5120\n"
    "v_pk_fma_f32 v[208:209], v[8:9], v[86:87], v[232:233] op_sel:[0,1,0] op_sel_hi:[1,1,1] neg_lo:[0,0,1] neg_hi:[0,0,1]\n"
    "v_pk_fma_f32 v[216:217], v[24:25], v[86:87], v[240:241] op_sel_hi:[1,0,1] neg_lo:[0,0,1] neg_hi:[0,0,1]\n"
    "v_pk_fma_f32 v[224:225], v[40:41], v[192:193], v[200:201] neg_lo:[0,0,1] neg_hi:[0,0,1]\n"
    "v_pk_fma_f32 v[210:211], v[10:11], v[86:87], v[234:235] op_sel:[0,1,0] op_sel_hi:[1,1,1] neg_lo:[0,0,1] neg_hi:[0,0,1]\n"
    "v_pk_fma_f32 v[218:219], v[26:27], v[86:87], v[242:243] op_sel_hi:[1,0,1] neg_lo:[0,0,1] neg_hi:[0,0,1]\n"
    "v_pk_fma_f32 v[226:227], v[42:43], v[194:195], v[202:203] neg_lo:[0,0,1] neg_hi:[0,0,1]\n"
    "v_pk_fma_f32 v[212:213], v[12:13], v[88:89], v[236:237] op_sel:[0,1,0] op_sel_hi:[1,1,1] neg_lo:[0,0,1] neg_hi:[0,0,1]\n"
    "v_pk_fma_f32 v[220:221], v[28:29], v[88:89], v[244:245] op_sel_hi:[1,0,1] neg_lo:[0,0,1] neg_hi:[0,0,1]\n"
    "v_pk_fma_f32 v[228:229], v[44:45], v[196:197], v[204:205] neg_lo:[0,0,1] neg_hi:[0,0,1]\n"
    "v_pk_fma_f32 v[214:215], v[14:15], v[88:89], v[238:239] op_sel:[0,1,0] op_sel_hi:[1,1,1] neg_lo:[0,0,1] neg_hi:[0,0,1]\n"
    "v_pk_fma_f32 v[222:223], v[30:31], v[88:89], v[246:247] op_sel_hi:[1,0,1] neg_lo:[0,0,1] neg_hi:[0,0,1]\n"
    "v_pk_fma_f32 v[230:231], v[46:47], v[198:199], v[206:207] neg_lo:[0,0,1] neg_hi:[0,0,1]\n"
    "s_branch Lkj_45_%=\n"
    "Lkh_44_%=:\n"
    "s_nop 7\n"
    "ds_write_b128 v84, v[40:43] offset:4096\n"
    "ds_write_b128 v84, v[44:47] offset:5120\n"
    "v_pk_fma_f32 v[208:209], v[0:1], v[86:87], v[232:233] op_sel:[0,1,0] op_sel_hi:[1,1,1] neg_lo:[0,0,1] neg_hi:[0,0,1]\n"
    "v_pk_fma_f32 v[216:217], v[16:17], v[86:87], v[240:241] op_sel_hi:[1,0,1] neg_lo:[0,0,1] neg_hi:[0,0,1]\n"
    "v_pk_fma_f32 v[224:225], v[32:33], v[192:193], v[200:201] neg_lo:[0,0,1] neg_hi:[0,0,1]\n"
    "v_pk_fma_f32 v[210:211], v[2:3], v[86:87], v[234:235] op_sel:[0,1,0] op_sel_hi:[1,1,1] neg_lo:[0,0,1] neg_hi:[0,0,1]\n"
    "v_pk_fma_f32 v[218:219], v[18:19], v[86:87], v[242:243] op_sel_hi:[1,0,1] neg_lo:[0,0,1] neg_hi:[0,0,1]\n"
    "v_pk_fma_f32 v[226:227], v[34:35], v[194:195], v[202:203] neg_lo:[0,0,1] neg_hi:[0,0,1]\n"
    "v_pk_fma_f32 v[212:213], v[4:5], v[88:89], v[236:237] op_sel:[0,1,0] op_sel_hi:[1,1,1] neg_lo:[0,0,1] neg_hi:[0,0,1]\n"
    "v_pk_fma_f32 v[220:221], v[20:21], v[88:89], v[244:245] op_sel_hi:[1,0,1] neg_lo:[0,0,1] neg_hi:[0,0,1]\n"
    "v_pk_fma_f32 v[228:229], v[36:37], v[196:197], v[204:205] neg_lo:[0,0,1] neg_hi:[0,0,1]\n"
    "v_pk_fma_f32 v[214:215], v[6:7], v[88:89], v[238:239] op_sel:[0,1,0] op_sel_hi:[1,1,1] neg_lo:[0,0,1] neg_hi:[0,0,1]\n"
    "v_pk_fma_f32 v[222:223], v[22:23], v[88:89], v[246:247] op_sel_hi:[1,0,1] neg_lo:[0,0,1] neg_hi:[0,0,1]\n"
    "v_pk_fma_f32 v[230:231], v[38:39], v[198:199], v[206:207] neg_lo:[0,0,1] neg_hi:[0,0,1]\n"
    "Lkj_45_%=:\n"
    "s_waitcnt lgkmcnt(0)\n"
    "s_barrier\n"
    "ds_read_b128 v[128:131], v85 offset:2048\n"
    "ds_read_b128 v[132:135], v85 offset:3072\n"
    "ds_read_b128 v[136:139], v85 offset:4096\n"
    "ds_read_b128 v[140:143], v85 offset:5120\n"
    "ds_read_b128 v[144:147], v85 offset:0\n"
    "ds_read_b128 v[148:151], v85 offset:1024\n"
    "s_waitcnt lgkmcnt(4)\n"
    "v_pk_fma_f32 v[216:217], v[128:129], v[86:87], v[216:217] op_sel_hi:[1,0,1]\n"
    "v_pk_fma_f32 v[218:219], v[130:131], v[86:87], v[218:219] op_sel_hi:[1,0,1]\n"
    "v_pk_fma_f32 v[220:221], v[132:133], v[88:89], v[220:221] op_sel_hi:[1,0,1]\n"
    "v_pk_fma_f32 v[222:223], v[134:135], v[88:89], v[222:223] op_sel_hi:[1,0,1]\n"
    "v_cvt_pk_bf16_f32 v0, v216, v217\n"
    "v_cvt_pk_bf16_f32 v1, v218, v219\n"
    "v_cvt_pk_bf16_f32 v2, v220, v221\n"
    "v_cvt_pk_bf16_f32 v3, v222, v223\n"
    "s_nop 0\n"
    "v_permlane16_swap_b32 v0, v2\n"
    "v_permlane16_swap_b32 v1, v3\n"
    "v_min_u32 v0, -2, v0\n"
    "v_min_u32 v1, -2, v1\n"
    "v_min_u32 v2, -2, v2\n"
    "v_min_u32 v3, -2, v3\n"
    "buffer_store_dwordx4 v[0:3], v104, s[4:7], s2 offen sc1\n"
    "s_waitcnt lgkmcnt(2)\n"
    "v_pk_fma_f32 v[224:225], v[136:137], v[192:193], v[224:225]\n"
    "v_pk_fma_f32 v[226:227], v[138:139], v[194:195], v[226:227]\n"
    "v_pk_fma_f32 v[228:229], v[140:141], v[196:197], v[228:229]\n"
    "v_pk_fma_f32 v[230:231], v[142:143], v[198:199], v[230:231]\n"
    "v_cvt_pk_bf16_f32 v4, v224, v225\n"
    "v_cvt_pk_bf16_f32 v5, v226, v227\n"
    "v_cvt_pk_bf16_f32 v6, v228, v229\n"
    "v_cvt_pk_bf16_f32 v7, v230, v231\n"
    "s_nop 0\n"
    "v_permlane16_swap_b32 v4, v6\n"
    "v_permlane16_swap_b32 v5, v7\n"
    "v_min_u32 v4, -2, v4\n"
    "v_min_u32 v5, -2, v5\n"
    "v_min_u32 v6, -2, v6\n"
    "v_min_u32 v7, -2, v7\n"
    "buffer_store_dwordx4 v[4:7], v125, s[4:7], s3 offen sc1\n"
    "s_waitcnt lgkmcnt(0)\n"
    "v_pk_fma_f32 v[208:209], v[144:145], v[86:87], v[208:209] op_sel:[0,1,0] op_sel_hi:[1,1,1]\n"
    "v_pk_fma_f32 v[210:211], v[146:147], v[86:87], v[210:211] op_sel:[0,1,0] op_sel_hi:[1,1,1]\n"
    "v_pk_fma_f32 v[212:213], v[148:149], v[88:89], v[212:213] op_sel:[0,1,0] op_sel_hi:[1,1,1]\n"
    "v_pk_fma_f32 v[214:215], v[150:151], v[88:89], v[214:215] op_sel:[0,1,0] op_sel_hi:[1,1,1]\n"
    "v_cvt_pk_bf16_f32 v8, v208, v209\n"
    "v_cvt_pk_bf16_f32 v9, v210, v211\n"
    "v_cvt_pk_bf16_f32 v10, v212, v213\n"
    "v_cvt_pk_bf16_f32 v11, v214, v215\n"
    "s_nop 0\n"
    "v_permlane16_swap_b32 v8, v10\n"
    "v_permlane16_swap_b32 v9, v11\n"
    "v_min_u32 v8, -2, v8\n"
    "v_min_u32 v9, -2, v9\n"
    "v_min_u32 v10, -2, v10\n"
    "v_min_u32 v11, -2, v11\n"
    "buffer_store_dwordx4 v[8:11], v104, s[4:7], s1 offen sc1\n"
    "s_lshl_b32 s2, s71, 16\n"
    "s_add_u32 s3, s2, s58\n"
    "s_cmp_eq_u32 s71, 0\n"
    "s_cbranch_scc0 Lc_50_%=\n"
    "s_waitcnt vmcnt(0) lgkmcnt(0)\n"
    "s_barrier\n"
    "s_cmp_eq_u32 s36, 0\n"
    "s_cbranch_scc0 Lw_46_%=\n"
    "v_mov_b32 v68, s72\n"
    "v_mov_b32 v69, s73\n"
    "s_mov_b64 exec, 1\n"
    "buffer_store_dwordx2 v[68:69], off, s[4:7], s58 sc1\n"
    "s_mov_b64 exec, -1\n"
    "Lw_46_%=:\n"
    "s_cmp_lt_u32 s36, 6\n"
    "s_cbranch_scc0 Lp_24_%=\n"
    "s_sub_u32 s0, s55, s54\n"
    "s_lshl_b32 s1, s37, 2\n"
    "s_add_u32 s0, s0, s1\n"
    "s_add_u32 s0, s0, 65536\n"
    "v_add_u32 v16, s0, v104\n"
    "ds_read_b128 v[24:27], v16\n"
    "s_waitcnt lgkmcnt(0)\n"
    "buffer_store_dwordx4 v[24:27], v104, s[4:7], s55 offen sc1\n"
    "Lp_24_%=:\n"
    "s_cmp_eq_u32 s36, 7\n"
    "s_cbranch_scc0 Lw_25_%=\n"
    "ds_read_b128 v[24:27], v104 offset:55296\n"
    "s_waitcnt lgkmcnt(0)\n"
    "buffer_store_dwordx4 v[24:27], v104, s[4:7], s63 offen sc1\n"
    "Lw_25_%=:\n"
    "s_cmp_eq_u32 s36, 0\n"
    "s_cbranch_scc0 Lw_49_%=\n"
    "v_and_b32 v70, 0xf0, v104\n"
    "v_lshlrev_b32 v70, 4, v70\n"
    "s_mov_b32 s2, 0\n"
    "Lfp_47_%=:\n"
    "buffer_load_dwordx2 v[68:69], v70, s[4:7], s59 offen sc1\n"
    "s_add_u32 s2, s2, 1\n"
    "s_waitcnt vmcnt(0)\n"
    "v_cmp_ne_u64 vcc, s[72:73], v[68:69]\n"
    "s_cbranch_vccz Lfq_48_%=\n"
    "s_cmp_lt_u32 s2, 65536\n"
    "s_cbranch_scc1 Lfp_47_%=\n"
    "Lfq_48_%=:\n"
    "Lw_49_%=:\n"
    "s_waitcnt lgkmcnt(0)\n"
    "s_barrier\n"
    "Lc_50_%=:\n"
    "s_add_u32 s0, s70, s45\n"
    "v_add_u32 v88, s0, v104\n"
    "s_add_u32 s0, s70, s46\n"
    "v_add_u32 v89, s0, v104\n"
    "s_waitcnt vmcnt(0)\n"
    "buffer_load_dwordx4 v[48:51], v88, s[4:7], 0 offen sc1\n"
    "s_mov_b32 s0, 2097152\n"
    "buffer_load_dwordx4 v[72:75], v89, s[4:7], s0 offen offset:0 sc1\n"
    "s_mov_b32 s0, 4194304\n"
    "buffer_load_dwordx4 v[76:79], v89, s[4:7], s0 offen offset:0 sc1\n"
    "s_mov_b32 s0, 2097152\n"
    "buffer_load_dwordx4 v[80:83], v89, s[4:7], s0 offen offset:1024 sc1\n"
    "s_mov_b32 s0, 4194304\n"
    "buffer_load_dwordx4 v[84:87], v89, s[4:7], s0 offen offset:1024 sc1\n"
    "v_mov_b32 v64, 0xdddd\n"
    "v_mov_b32 v0, 0\n"
    "v_mov_b32 v1, 0\n"
    "v_mov_b32 v2, 0\n"
    "v_mov_b32 v3, 0\n"
    "v_mov_b32 v4, 0\n"
    "v_mov_b32 v5, 0\n"
    "v_mov_b32 v6, 0\n"
    "v_mov_b32 v7, 0\n"
    "v_mov_b32 v8, 0\n"
    "v_mov_b32 v9, 0\n"
    "v_mov_b32 v10, 0\n"
    "v_mov_b32 v11, 0\n"
    "v_mov_b32 v12, 0\n"
    "v_mov_b32 v13, 0\n"
    "v_mov_b32 v14, 0\n"
    "v_mov_b32 v15, 0\n"
    "s_waitcnt vmcnt(5) lgkmcnt(0)\n"
    "s_barrier\n"
    "s_cmp_eq_u32 s36, 0\n"
    "s_cbranch_scc0 Lw_51_%=\n"
    "v_mov_b32 v68, s72\n"
    "v_mov_b32 v69, s73\n"
    "s_mov_b64 exec, 1\n"
    "buffer_store_dwordx2 v[68:69], off, s[4:7], s3 sc1\n"
    "s_mov_b64 exec, -1\n"
    "Lw_51_%=:\n"
    "s_cmp_eq_u32 s36, 1\n"
    "s_cbranch_scc0 Lw_52_%=\n"
    "v_mov_b32 v56, 0x3f803f80\n"
    "v_mov_b32 v57, 0x3f803f80\n"
    "v_mov_b32 v58, 0x3f803f80\n"
    "v_mov_b32 v59, 0x3f803f80\n"
    "v_mul_u32_u24 v68, 66, v104\n"
    "v_add_u32 v68, 66560, v68\n"
    "ds_write_b128 v68, v[56:59]\n"
    "ds_write_b128 v68, v[0:3] offset:16\n"
    "Lw_52_%=:\n"
    "v_mov_b32 v65, v104\n"
    "v_bfe_u32 v102, v104, 4, 5\n"
    "v_and_b32 v68, 0x3fff, v113\n"
    "v_and_b32 v69, 27, v102\n"
    "v_cmp_eq_u32 vcc, 16, v69\n"
    "v_mov_b32 v69, 1040\n"
    "v_mov_b32 v71, 1024\n"
    "v_cndmask_b32 v69, v69, v71, vcc\n"
    "v_cmp_gt_u32 vcc, 16, v102\n"
    "v_add_u32 v71, 512, v68\n"
    "s_nop 0\n"
    "v_cndmask_b32 v66, v69, v68, vcc\n"
    "v_cndmask_b32 v67, v69, v71, vcc\n"
    "v_add_u32 v66, 65536, v66\n"
    "v_add_u32 v67, 65536, v67\n"
    "s_lshl_b32 s0, s67, 11\n"
    "s_add_u32 s0, s0, 0\n"
    "v_add_u32 v90, s0, v109\n"
    "v_xor_b32 v91, 32, v90\n"
    "v_xor_b32 v92, 64, v90\n"
    "v_xor_b32 v93, 96, v90\n"
    "s_lshl_b32 s0, s67, 11\n"
    "s_add_u32 s0, s0, 65536\n"
    "v_add_u32 v94, s0, v109\n"
    "v_xor_b32 v95, 32, v94\n"
    "v_xor_b32 v96, 64, v94\n"
    "v_xor_b32 v97, 96, v94\n"
    "s_lshl_b32 s0, s67, 11\n"
    "s_add_u32 s0, s0, 131072\n"
    "v_add_u32 v98, s0, v109\n"
    "v_xor_b32 v99, 32, v98\n"
    "v_xor_b32 v100, 64, v98\n"
    "v_xor_b32 v101, 96, v98\n"
    "s_lshl_b32 s1, s37, 1\n"
    "v_add_u32 v68, s1, v104\n"
    "s_mul_i32 s2, s36, 2112\n"
    "s_add_u32 s2, s2, 65536\n"
    "v_add_u32 v69, s2, v104\n"
    "s_mov_b32 s96, 0\n"
    "s_waitcnt vmcnt(0)\n"
    "Lrt_53_%=:\n"
    "v_max3_u32 v70, v48, v49, v50\n"
    "v_max_u32 v70, v70, v51\n"
    "v_max3_u32 v70, v70, v72, v73\n"
    "v_max3_u32 v70, v70, v74, v75\n"
    "v_max3_u32 v70, v70, v76, v77\n"
    "v_max3_u32 v70, v70, v78, v79\n"
    "v_max3_u32 v70, v70, v80, v81\n"
    "v_max3_u32 v70, v70, v82, v83\n"
    "v_max3_u32 v70, v70, v84, v85\n"
    "v_max3_u32 v70, v70, v86, v87\n"
    "v_cmp_eq_u32 vcc, -1, v70\n"
    "s_cbranch_vccz Lrk_54_%=\n"
    "buffer_load_dwordx4 v[48:51], v88, s[4:7], 0 offen sc1\n"
    "s_mov_b32 s0, 2097152\n"
    "buffer_load_dwordx4 v[72:75], v89, s[4:7], s0 offen offset:0 sc1\n"
    "s_mov_b32 s0, 4194304\n"
    "buffer_load_dwordx4 v[76:79], v89, s[4:7], s0 offen offset:0 sc1\n"
    "s_mov_b32 s0, 2097152\n"
    "buffer_load_dwordx4 v[80:83], v89, s[4:7], s0 offen offset:1024 sc1\n"
    "s_mov_b32 s0, 4194304\n"
    "buffer_load_dwordx4 v[84:87], v89, s[4:7], s0 offen offset:1024 sc1\n"
    "s_add_u32 s96, s96, 1\n"
    "s_cmp_lt_u32 s96, 65536\n"
    "s_waitcnt vmcnt(0)\n"
    "s_cbranch_scc1 Lrt_53_%=\n"
    "Lrk_54_%=:\n"
    "ds_write_b128 v68, v[72:75] offset:0\n"
    "ds_write_b128 v69, v[76:79] offset:0\n"
    "ds_write_b128 v68, v[80:83] offset:1024\n"
    "ds_write_b128 v69, v[84:87] offset:1056\n"
    "s_waitcnt lgkmcnt(0)\n"
    "s_barrier\n"
    "s_mov_b32 s86, s1\n"
    "s_mov_b32 s85, s2\n"
    "s_lshl_b32 s2, s71, 16\n"
    "s_add_u32 s84, s2, s59\n"
    "v_and_b32 v71, 0xf0, v104\n"
    "v_lshlrev_b32 v71, 4, v71\n"
    "s_cmp_eq_u32 s71, 0\n"
    "s_cbranch_scc0 Lat1_65_%=\n"
    "ds_read_b128 v[52:55], v65\n"
    "s_add_u32 m0, s86, 16384\n"
    "s_mov_b32 s0, 2113536\n"
    "buffer_load_dwordx4 v89, s[4:7], s0 offen sc1 lds\n"
    "s_add_u32 m0, s86, 16384\n"
    "s_mov_b32 s0, 2113536\n"
    "buffer_load_dwordx4 v89, s[4:7], s0 offen offset:1024 sc1 lds\n"
    "s_add_u32 m0, s85, 16896\n"
    "s_mov_b32 s0, 4210688\n"
    "buffer_load_dwordx4 v89, s[4:7], s0 offen sc1 lds\n"
    "s_add_u32 m0, s85, 17952\n"
    "s_mov_b32 s0, 4211712\n"
    "buffer_load_dwordx4 v89, s[4:7], s0 offen sc1 lds\n"
    "s_add_u32 s0, s64, 0\n"
    "buffer_load_dword v70, v105, s[8:11], s0 offen\n"
    "s_add_u32 s0, s64, 28672\n"
    "buffer_load_dword v70, v105, s[8:11], s0 offen\n"
    "s_add_u32 s0, s64, 57344\n"
    "buffer_load_dword v70, v105, s[8:11], s0 offen\n"
    "s_add_u32 s0, s64, 86016\n"
    "buffer_load_dword v70, v105, s[8:11], s0 offen\n"
    "buffer_load_dword v70, v107, s[16:19], s76 offen\n"
    "s_waitcnt lgkmcnt(0)\n"
    "v_mfma_f32_32x32x16_bf16 v[16:31], v[52:55], v[48:51], 0\n"
    "ds_read_b128 v[52:55], v65 offset:1024\n"
    "ds_read_b128 v[56:59], v66 offset:0\n"
    "s_nop 9\n"
    "v_exp_f32 v16, v16\n"
    "v_exp_f32 v17, v17\n"
    "ds_read_b128 v[60:63], v67 offset:0\n"
    "v_exp_f32 v18, v18\n"
    "v_exp_f32 v19, v19\n"
    "v_exp_f32 v20, v20\n"
    "v_exp_f32 v21, v21\n"
    "s_waitcnt lgkmcnt(2)\n"
    "v_mfma_f32_32x32x16_bf16 v[32:47], v[52:55], v[48:51], 0\n"
    "ds_read_b128 v[52:55], v65 offset:2048\n"
    "v_exp_f32 v22, v22\n"
    "v_exp_f32 v23, v23\n"
    "v_exp_f32 v24, v24\n"
    "v_exp_f32 v25, v25\n"
    "s_waitcnt lgkmcnt(2)\n"
    "v_smfmac_f32_32x32x32_bf16 v[0:15], v[56:59], v[16:23], v64\n"
    "ds_read_b128 v[56:59], v66 offset:1056\n"
    "v_exp_f32 v26, v26\n"
    "v_exp_f32 v27, v27\n"
    "v_exp_f32 v28, v28\n"
    "v_exp_f32 v29, v29\n"
    "v_exp_f32 v30, v30\n"
    "v_exp_f32 v31, v31\n"
    "v_exp_f32 v32, v32\n"
    "v_exp_f32 v33, v33\n"
    "s_waitcnt lgkmcnt(2)\n"
    "v_smfmac_f32_32x32x32_bf16 v[0:15], v[60:63], v[24:31], v64\n"
    "ds_read_b128 v[60:63], v67 offset:1056\n"
    "v_exp_f32 v34, v34\n"
    "v_exp_f32 v35, v35\n"
    "v_exp_f32 v36, v36\n"
    "v_exp_f32 v37, v37\n"
    "s_waitcnt lgkmcnt(2)\n"
    "v_mfma_f32_32x32x16_bf16 v[16:31], v[52:55], v[48:51], 0\n"
    "ds_read_b128 v[52:55], v65 offset:3072\n"
    "v_exp_f32 v38, v38\n"
    "v_exp_f32 v39, v39\n"
    "v_exp_f32 v40, v40\n"
    "v_exp_f32 v41, v41\n"
    "s_waitcnt lgkmcnt(2)\n"
    "v_smfmac_f32_32x32x32_bf16 v[0:15], v[56:59], v[32:39], v64\n"
    "ds_read_b128 v[56:59], v66 offset:2112\n"
    "v_exp_f32 v42, v42\n"
    "v_exp_f32 v43, v43\n"
    "v_exp_f32 v44, v44\n"
    "v_exp_f32 v45, v45\n"
    "v_exp_f32 v46, v46\n"
    "v_exp_f32 v47, v47\n"
    "v_mov_b32 v72, -1\n"
    "v_mov_b32 v73, -1\n"
    "v_mov_b32 v74, -1\n"
    "v_mov_b32 v75, -1\n"
    "v_lshrrev_b32 v77, 1, v104\n"
    "v_exp_f32 v16, v16\n"
    "v_exp_f32 v17, v17\n"
    "s_waitcnt lgkmcnt(2)\n"
    "v_smfmac_f32_32x32x32_bf16 v[0:15], v[60:63], v[40:47], v64\n"
    "ds_read_b128 v[60:63], v67 offset:2112\n"
    "v_exp_f32 v18, v18\n"
    "v_exp_f32 v19, v19\n"
    "v_exp_f32 v20, v20\n"
    "v_exp_f32 v21, v21\n"
    "s_waitcnt lgkmcnt(2)\n"
    "v_mfma_f32_32x32x16_bf16 v[32:47], v[52:55], v[48:51], 0\n"
    "ds_read_b128 v[52:55], v65 offset:4096\n"
    "v_exp_f32 v22, v22\n"
    "v_exp_f32 v23, v23\n"
    "v_exp_f32 v24, v24\n"
    "v_exp_f32 v25, v25\n"
    "s_waitcnt lgkmcnt(2)\n"
    "v_smfmac_f32_32x32x32_bf16 v[0:15], v[56:59], v[16:23], v64\n"
    "ds_read_b128 v[56:59], v66 offset:3168\n"
    "v_exp_f32 v26, v26\n"
    "v_exp_f32 v27, v27\n"
    "v_exp_f32 v28, v28\n"
    "v_exp_f32 v29, v29\n"
    "v_exp_f32 v30, v30\n"
    "v_exp_f32 v31, v31\n"
    "v_exp_f32 v32, v32\n"
    "v_exp_f32 v33, v33\n"
    "s_waitcnt lgkmcnt(2)\n"
    "v_smfmac_f32_32x32x32_bf16 v[0:15], v[60:63], v[24:31], v64\n"
    "ds_read_b128 v[60:63], v67 offset:3168\n"
    "v_exp_f32 v34, v34\n"
    "v_exp_f32 v35, v35\n"
    "v_exp_f32 v36, v36\n"
    "v_exp_f32 v37, v37\n"
    "s_waitcnt lgkmcnt(2)\n"
    "v_mfma_f32_32x32x16_bf16 v[16:31], v[52:55], v[48:51], 0\n"
    "ds_read_b128 v[52:55], v65 offset:5120\n"
    "v_exp_f32 v38, v38\n"
    "v_exp_f32 v39, v39\n"
    "v_exp_f32 v40, v40\n"
    "v_exp_f32 v41, v41\n"
    "s_waitcnt lgkmcnt(2)\n"
    "v_smfmac_f32_32x32x32_bf16 v[0:15], v[56:59], v[32:39], v64\n"
    "ds_read_b128 v[56:59], v66 offset:4224\n"
    "v_exp_f32 v42, v42\n"
    "v_exp_f32 v43, v43\n"
    "v_exp_f32 v44, v44\n"
    "v_exp_f32 v45, v45\n"
    "v_exp_f32 v46, v46\n"
    "v_exp_f32 v47, v47\n"
    "v_exp_f32 v16, v16\n"
    "v_exp_f32 v17, v17\n"
    "s_waitcnt lgkmcnt(2)\n"
    "v_smfmac_f32_32x32x32_bf16 v[0:15], v[60:63], v[40:47], v64\n"
    "ds_read_b128 v[60:63], v67 offset:4224\n"
    "v_exp_f32 v18, v18\n"
    "v_exp_f32 v19, v19\n"
    "v_exp_f32 v20, v20\n"
    "v_exp_f32 v21, v21\n"
    "s_waitcnt lgkmcnt(2)\n"
    "v_mfma_f32_32x32x16_bf16 v[32:47], v[52:55], v[48:51], 0\n"
    "ds_read_b128 v[52:55], v65 offset:6144\n"
    "v_exp_f32 v22, v22\n"
    "v_exp_f32 v23, v23\n"
    "v_exp_f32 v24, v24\n"
    "v_exp_f32 v25, v25\n"
    "s_waitcnt lgkmcnt(2)\n"
    "v_smfmac_f32_32x32x32_bf16 v[0:15], v[56:59], v[16:23], v64\n"
    "ds_read_b128 v[56:59], v66 offset:5280\n"
    "v_exp_f32 v26, v26\n"
    "v_exp_f32 v27, v27\n"
    "v_exp_f32 v28, v28\n"
    "v_exp_f32 v29, v29\n"
    "v_exp_f32 v30, v30\n"
    "v_exp_f32 v31, v31\n"
    "v_exp_f32 v32, v32\n"
    "v_exp_f32 v33, v33\n"
    "s_waitcnt lgkmcnt(2)\n"
    "v_smfmac_f32_32x32x32_bf16 v[0:15], v[60:63], v[24:31], v64\n"
    "ds_read_b128 v[60:63], v67 offset:5280\n"
    "v_exp_f32 v34, v34\n"
    "v_exp_f32 v35, v35\n"
    "v_exp_f32 v36, v36\n"
    "v_exp_f32 v37, v37\n"
    "s_waitcnt lgkmcnt(2)\n"
    "v_mfma_f32_32x32x16_bf16 v[16:31], v[52:55], v[48:51], 0\n"
    "ds_read_b128 v[52:55], v65 offset:7168\n"
    "v_exp_f32 v38, v38\n"
    "v_exp_f32 v39, v39\n"
    "v_exp_f32 v40, v40\n"
    "v_exp_f32 v41, v41\n"
    "s_waitcnt lgkmcnt(2)\n"
    "v_smfmac_f32_32x32x32_bf16 v[0:15], v[56:59], v[32:39], v64\n"
    "ds_read_b128 v[56:59], v66 offset:6336\n"
    "v_exp_f32 v42, v42\n"
    "v_exp_f32 v43, v43\n"
    "v_exp_f32 v44, v44\n"
    "v_exp_f32 v45, v45\n"
    "v_exp_f32 v46, v46\n"
    "v_exp_f32 v47, v47\n"
    "v_exp_f32 v16, v16\n"
    "v_exp_f32 v17, v17\n"
    "s_waitcnt lgkmcnt(2)\n"
    "v_smfmac_f32_32x32x32_bf16 v[0:15], v[60:63], v[40:47], v64\n"
    "ds_read_b128 v[60:63], v67 offset:6336\n"
    "v_exp_f32 v18, v18\n"
    "v_exp_f32 v19, v19\n"
    "v_exp_f32 v20, v20\n"
    "v_exp_f32 v21, v21\n"
    "s_waitcnt lgkmcnt(2)\n"
    "v_mfma_f32_32x32x16_bf16 v[32:47], v[52:55], v[48:51], 0\n"
    "ds_read_b128 v[52:55], v65 offset:8192\n"
    "v_exp_f32 v22, v22\n"
    "v_exp_f32 v23, v23\n"
    "v_exp_f32 v24, v24\n"
    "v_exp_f32 v25, v25\n"
    "s_waitcnt lgkmcnt(2)\n"
    "v_smfmac_f32_32x32x32_bf16 v[0:15], v[56:59], v[16:23], v64\n"
    "ds_read_b128 v[56:59], v66 offset:7392\n"
    "v_exp_f32 v26, v26\n"
    "v_exp_f32 v27, v27\n"
    "v_exp_f32 v28, v28\n"
    "v_exp_f32 v29, v29\n"
    "v_exp_f32 v30, v30\n"
    "v_exp_f32 v31, v31\n"
    "v_exp_f32 v32, v32\n"
    "v_exp_f32 v33, v33\n"
    "s_waitcnt lgkmcnt(2)\n"
    "v_smfmac_f32_32x32x32_bf16 v[0:15], v[60:63], v[24:31], v64\n"
    "ds_read_b128 v[60:63], v67 offset:7392\n"
    "v_exp_f32 v34, v34\n"
    "v_exp_f32 v35, v35\n"
    "v_exp_f32 v36, v36\n"
    "v_exp_f32 v37, v37\n"
    "s_waitcnt lgkmcnt(2)\n"
    "v_mfma_f32_32x32x16_bf16 v[16:31], v[52:55], v[48:51], 0\n"
    "ds_read_b128 v[52:55], v65 offset:9216\n"
    "v_exp_f32 v38, v38\n"
    "v_exp_f32 v39, v39\n"
    "v_exp_f32 v40, v40\n"
    "v_exp_f32 v41, v41\n"
    "s_waitcnt lgkmcnt(2)\n"
    "v_smfmac_f32_32x32x32_bf16 v[0:15], v[56:59], v[32:39], v64\n"
    "ds_read_b128 v[56:59], v66 offset:8448\n"
    "v_exp_f32 v42, v42\n"
    "v_exp_f32 v43, v43\n"
    "v_exp_f32 v44, v44\n"
    "v_exp_f32 v45, v45\n"
    "v_exp_f32 v46, v46\n"
    "v_exp_f32 v47, v47\n"
    "s_add_u32 s0, s70, s41\n"
    "buffer_store_dwordx4 v[72:75], v104, s[4:7], s0 offen sc1\n"
    "v_exp_f32 v16, v16\n"
    "v_exp_f32 v17, v17\n"
    "s_waitcnt lgkmcnt(2)\n"
    "v_smfmac_f32_32x32x32_bf16 v[0:15], v[60:63], v[40:47], v64\n"
    "ds_read_b128 v[60:63], v67 offset:8448\n"
    "v_exp_f32 v18, v18\n"
    "v_exp_f32 v19, v19\n"
    "v_exp_f32 v20, v20\n"
    "v_exp_f32 v21, v21\n"
    "s_waitcnt lgkmcnt(2)\n"
    "v_mfma_f32_32x32x16_bf16 v[32:47], v[52:55], v[48:51], 0\n"
    "ds_read_b128 v[52:55], v65 offset:10240\n"
    "v_exp_f32 v22, v22\n"
    "v_exp_f32 v23, v23\n"
    "v_exp_f32 v24, v24\n"
    "v_exp_f32 v25, v25\n"
    "s_waitcnt lgkmcnt(2)\n"
    "v_smfmac_f32_32x32x32_bf16 v[0:15], v[56:59], v[16:23], v64\n"
    "ds_read_b128 v[56:59], v66 offset:9504\n"
    "v_exp_f32 v26, v26\n"
    "v_exp_f32 v27, v27\n"
    "v_exp_f32 v28, v28\n"
    "v_exp_f32 v29, v29\n"
    "v_exp_f32 v30, v30\n"
    "v_exp_f32 v31, v31\n"
    "v_exp_f32 v32, v32\n"
    "v_exp_f32 v33, v33\n"
    "s_waitcnt lgkmcnt(2)\n"
    "v_smfmac_f32_32x32x32_bf16 v[0:15], v[60:63], v[24:31], v64\n"
    "ds_read_b128 v[60:63], v67 offset:9504\n"
    "v_exp_f32 v34, v34\n"
    "v_exp_f32 v35, v35\n"
    "v_exp_f32 v36, v36\n"
    "v_exp_f32 v37, v37\n"
    "s_waitcnt lgkmcnt(2)\n"
    "v_mfma_f32_32x32x16_bf16 v[16:31], v[52:55], v[48:51], 0\n"
    "ds_read_b128 v[52:55], v65 offset:11264\n"
    "v_exp_f32 v38, v38\n"
    "v_exp_f32 v39, v39\n"
    "v_exp_f32 v40, v40\n"
    "v_exp_f32 v41, v41\n"
    "s_waitcnt lgkmcnt(2)\n"
    "v_smfmac_f32_32x32x32_bf16 v[0:15], v[56:59], v[32:39], v64\n"
    "ds_read_b128 v[56:59], v66 offset:10560\n"
    "v_exp_f32 v42, v42\n"
    "v_exp_f32 v43, v43\n"
    "v_exp_f32 v44, v44\n"
    "v_exp_f32 v45, v45\n"
    "v_exp_f32 v46, v46\n"
    "v_exp_f32 v47, v47\n"
    "v_exp_f32 v16, v16\n"
    "v_exp_f32 v17, v17\n"
    "s_waitcnt lgkmcnt(2)\n"
    "v_smfmac_f32_32x32x32_bf16 v[0:15], v[60:63], v[40:47], v64\n"
    "ds_read_b128 v[60:63], v67 offset:10560\n"
    "v_exp_f32 v18, v18\n"
    "v_exp_f32 v19, v19\n"
    "v_exp_f32 v20, v20\n"
    "v_exp_f32 v21, v21\n"
    "s_waitcnt lgkmcnt(2)\n"
    "v_mfma_f32_32x32x16_bf16 v[32:47], v[52:55], v[48:51], 0\n"
    "ds_read_b128 v[52:55], v65 offset:12288\n"
    "v_exp_f32 v22, v22\n"
    "v_exp_f32 v23, v23\n"
    "v_exp_f32 v24, v24\n"
    "v_exp_f32 v25, v25\n"
    "s_waitcnt lgkmcnt(2)\n"
    "v_smfmac_f32_32x32x32_bf16 v[0:15], v[56:59], v[16:23], v64\n"
    "ds_read_b128 v[56:59], v66 offset:11616\n"
    "v_exp_f32 v26, v26\n"
    "v_exp_f32 v27, v27\n"
    "v_exp_f32 v28, v28\n"
    "v_exp_f32 v29, v29\n"
    "v_exp_f32 v30, v30\n"
    "v_exp_f32 v31, v31\n"
    "v_exp_f32 v32, v32\n"
    "v_exp_f32 v33, v33\n"
    "s_waitcnt lgkmcnt(2)\n"
    "v_smfmac_f32_32x32x32_bf16 v[0:15], v[60:63], v[24:31], v64\n"
    "ds_read_b128 v[60:63], v67 offset:11616\n"
    "v_exp_f32 v34, v34\n"
    "v_exp_f32 v35, v35\n"
    "v_exp_f32 v36, v36\n"
    "v_exp_f32 v37, v37\n"
    "s_waitcnt lgkmcnt(2)\n"
    "v_mfma_f32_32x32x16_bf16 v[16:31], v[52:55], v[48:51], 0\n"
    "ds_read_b128 v[52:55], v65 offset:13312\n"
    "v_exp_f32 v38, v38\n"
    "v_exp_f32 v39, v39\n"
    "v_exp_f32 v40, v40\n"
    "v_exp_f32 v41, v41\n"
    "s_waitcnt lgkmcnt(2)\n"
    "v_smfmac_f32_32x32x32_bf16 v[0:15], v[56:59], v[32:39], v64\n"
    "ds_read_b128 v[56:59], v66 offset:12672\n"
    "v_exp_f32 v42, v42\n"
    "v_exp_f32 v43, v43\n"
    "v_exp_f32 v44, v44\n"
    "v_exp_f32 v45, v45\n"
    "v_exp_f32 v46, v46\n"
    "v_exp_f32 v47, v47\n"
    "v_exp_f32 v16, v16\n"
    "v_exp_f32 v17, v17\n"
    "s_waitcnt lgkmcnt(2)\n"
    "v_smfmac_f32_32x32x32_bf16 v[0:15], v[60:63], v[40:47], v64\n"
    "ds_read_b128 v[60:63], v67 offset:12672\n"
    "v_exp_f32 v18, v18\n"
    "v_exp_f32 v19, v19\n"
    "v_exp_f32 v20, v20\n"
    "v_exp_f32 v21, v21\n"
    "s_waitcnt lgkmcnt(2)\n"
    "v_mfma_f32_32x32x16_bf16 v[32:47], v[52:55], v[48:51], 0\n"
    "ds_read_b128 v[52:55], v65 offset:14336\n"
    "v_exp_f32 v22, v22\n"
    "v_exp_f32 v23, v23\n"
    "v_exp_f32 v24, v24\n"
    "v_exp_f32 v25, v25\n"
    "s_waitcnt lgkmcnt(2)\n"
    "v_smfmac_f32_32x32x32_bf16 v[0:15], v[56:59], v[16:23], v64\n"
    "ds_read_b128 v[56:59], v66 offset:13728\n"
    "v_exp_f32 v26, v26\n"
    "v_exp_f32 v27, v27\n"
    "v_exp_f32 v28, v28\n"
    "v_exp_f32 v29, v29\n"
    "v_exp_f32 v30, v30\n"
    "v_exp_f32 v31, v31\n"
    "s_waitcnt vmcnt(6) lgkmcnt(2)\n"
    "s_barrier\n"
    "s_cmp_eq_u32 s36, 1\n"
    "s_cbranch_scc0 Lw_56_%=\n"
    "s_add_u32 s0, s60, 0\n"
    "buffer_store_dwordx2 v[72:73], v77, s[4:7], s0 offen sc1\n"
    "Lw_56_%=:\n"
    "s_cmp_eq_u32 s36, 2\n"
    "s_cbranch_scc0 Lw_57_%=\n"
    "s_add_u32 s0, s60, 131072\n"
    "buffer_store_dwordx2 v[72:73], v77, s[4:7], s0 offen sc1\n"
    "Lw_57_%=:\n"
    "s_cmp_eq_u32 s36, 3\n"
    "s_cbranch_scc0 Lw_58_%=\n"
    "s_add_u32 s0, s62, 131072\n"
    "buffer_store_dwordx2 v[72:73], v77, s[4:7], s0 offen sc1\n"
    "Lw_58_%=:\n"
    "s_add_u32 s0, s70, s40\n"
    "buffer_store_dwordx4 v[72:75], v113, s[4:7], s0 offen sc1\n"
    "v_exp_f32 v32, v32\n"
    "v_exp_f32 v33, v33\n"
    "s_waitcnt lgkmcnt(2)\n"
    "v_smfmac_f32_32x32x32_bf16 v[0:15], v[60:63], v[24:31], v64\n"
    "ds_read_b128 v[60:63], v67 offset:13728\n"
    "v_exp_f32 v34, v34\n"
    "v_exp_f32 v35, v35\n"
    "v_exp_f32 v36, v36\n"
    "v_exp_f32 v37, v37\n"
    "s_waitcnt lgkmcnt(2)\n"
    "v_mfma_f32_32x32x16_bf16 v[16:31], v[52:55], v[48:51], 0\n"
    "ds_read_b128 v[52:55], v65 offset:15360\n"
    "s_add_u32 m0, s86, 32768\n"
    "s_mov_b32 s0, 2129920\n"
    "buffer_load_dwordx4 v89, s[4:7], s0 offen sc1 lds\n"
    "v_exp_f32 v38, v38\n"
    "v_exp_f32 v39, v39\n"
    "v_exp_f32 v40, v40\n"
    "v_exp_f32 v41, v41\n"
    "s_waitcnt lgkmcnt(2)\n"
    "v_smfmac_f32_32x32x32_bf16 v[0:15], v[56:59], v[32:39], v64\n"
    "ds_read_b128 v[56:59], v66 offset:14784\n"
    "v_exp_f32 v42, v42\n"
    "v_exp_f32 v43, v43\n"
    "v_exp_f32 v44, v44\n"
    "s_add_u32 m0, s86, 32768\n"
    "s_mov_b32 s0, 2129920\n"
    "buffer_load_dwordx4 v89, s[4:7], s0 offen offset:1024 sc1 lds\n"
    "v_exp_f32 v45, v45\n"
    "v_exp_f32 v46, v46\n"
    "v_exp_f32 v47, v47\n"
    "v_exp_f32 v16, v16\n"
    "v_exp_f32 v17, v17\n"
    "s_waitcnt lgkmcnt(2)\n"
    "v_smfmac_f32_32x32x32_bf16 v[0:15], v[60:63], v[40:47], v64\n"
    "ds_read_b128 v[60:63], v67 offset:14784\n"
    "v_exp_f32 v18, v18\n"
    "v_exp_f32 v19, v19\n"
    "v_exp_f32 v20, v20\n"
    "v_exp_f32 v21, v21\n"
    "s_waitcnt lgkmcnt(2)\n"
    "v_mfma_f32_32x32x16_bf16 v[32:47], v[52:55], v[48:51], 0\n"
    "ds_read_b128 v[52:55], v65 offset:16384\n"
    "s_add_u32 m0, s85, 33792\n"
    "s_mov_b32 s0, 4227072\n"
    "buffer_load_dwordx4 v89, s[4:7], s0 offen sc1 lds\n"
    "v_exp_f32 v22, v22\n"
    "v_exp_f32 v23, v23\n"
    "v_exp_f32 v24, v24\n"
    "v_exp_f32 v25, v25\n"
    "s_waitcnt lgkmcnt(2)\n"
    "v_smfmac_f32_32x32x32_bf16 v[0:15], v[56:59], v[16:23], v64\n"
    "ds_read_b128 v[56:59], v66 offset:15840\n"
    "v_exp_f32 v26, v26\n"
    "v_exp_f32 v27, v27\n"
    "v_exp_f32 v28, v28\n"
    "s_add_u32 m0, s85, 34848\n"
    "s_mov_b32 s0, 4228096\n"
    "buffer_load_dwordx4 v89, s[4:7], s0 offen sc1 lds\n"
    "v_exp_f32 v29, v29\n"
    "v_exp_f32 v30, v30\n"
    "v_exp_f32 v31, v31\n"
    "v_exp_f32 v32, v32\n"
    "v_exp_f32 v33, v33\n"
    "s_waitcnt lgkmcnt(2)\n"
    "v_smfmac_f32_32x32x32_bf16 v[0:15], v[60:63], v[24:31], v64\n"
    "ds_read_b128 v[60:63], v67 offset:15840\n"
    "v_exp_f32 v34, v34\n"
    "v_exp_f32 v35, v35\n"
    "v_exp_f32 v36, v36\n"
    "v_exp_f32 v37, v37\n"
    "s_waitcnt lgkmcnt(2)\n"
    "v_mfma_f32_32x32x16_bf16 v[16:31], v[52:55], v[48:51], 0\n"
    "ds_read_b128 v[52:55], v65 offset:17408\n"
    "s_add_u32 m0, s81, 139264\n"
    "s_add_u32 s0, s50, 0\n"
    "buffer_load_dwordx4 v105, s[8:11], s0 offen lds\n"
    "v_exp_f32 v38, v38\n"
    "v_exp_f32 v39, v39\n"
    "v_exp_f32 v40, v40\n"
    "v_exp_f32 v41, v41\n"
    "s_waitcnt lgkmcnt(2)\n"
    "v_smfmac_f32_32x32x32_bf16 v[0:15], v[56:59], v[32:39], v64\n"
    "ds_read_b128 v[56:59], v66 offset:16896\n"
    "v_exp_f32 v42, v42\n"
    "v_exp_f32 v43, v43\n"
    "v_exp_f32 v44, v44\n"
    "s_add_u32 m0, s81, 147456\n"
    "s_add_u32 s0, s50, 57344\n"
    "buffer_load_dwordx4 v105, s[8:11], s0 offen lds\n"
    "v_exp_f32 v45, v45\n"
    "v_exp_f32 v46, v46\n"
    "v_exp_f32 v47, v47\n"
    "v_exp_f32 v16, v16\n"
    "v_exp_f32 v17, v17\n"
    "s_waitcnt lgkmcnt(2)\n"
    "v_smfmac_f32_32x32x32_bf16 v[0:15], v[60:63], v[40:47], v64\n"
    "ds_read_b128 v[60:63], v67 offset:16896\n"
    "v_exp_f32 v18, v18\n"
    "v_exp_f32 v19, v19\n"
    "v_exp_f32 v20, v20\n"
    "v_exp_f32 v21, v21\n"
    "s_waitcnt lgkmcnt(2)\n"
    "v_mfma_f32_32x32x16_bf16 v[32:47], v[52:55], v[48:51], 0\n"
    "ds_read_b128 v[52:55], v65 offset:18432\n"
    "s_add_u32 m0, s81, 155648\n"
    "s_add_u32 s0, s50, 114688\n"
    "buffer_load_dwordx4 v105, s[8:11], s0 offen lds\n"
    "v_exp_f32 v22, v22\n"
    "v_exp_f32 v23, v23\n"
    "v_exp_f32 v24, v24\n"
    "v_exp_f32 v25, v25\n"
    "s_waitcnt lgkmcnt(2)\n"
    "v_smfmac_f32_32x32x32_bf16 v[0:15], v[56:59], v[16:23], v64\n"
    "ds_read_b128 v[56:59], v66 offset:17952\n"
    "v_exp_f32 v26, v26\n"
    "v_exp_f32 v27, v27\n"
    "v_exp_f32 v28, v28\n"
    "s_add_u32 m0, s81, 0\n"
    "s_add_u32 s0, s50, 172032\n"
    "buffer_load_dwordx4 v105, s[8:11], s0 offen lds\n"
    "v_exp_f32 v29, v29\n"
    "v_exp_f32 v30, v30\n"
    "v_exp_f32 v31, v31\n"
    "v_exp_f32 v32, v32\n"
    "v_exp_f32 v33, v33\n"
    "s_waitcnt lgkmcnt(2)\n"
    "v_smfmac_f32_32x32x32_bf16 v[0:15], v[60:63], v[24:31], v64\n"
    "ds_read_b128 v[60:63], v67 offset:17952\n"
    "v_exp_f32 v34, v34\n"
    "v_exp_f32 v35, v35\n"
    "v_exp_f32 v36, v36\n"
    "v_exp_f32 v37, v37\n"
    "s_waitcnt lgkmcnt(2)\n"
    "v_mfma_f32_32x32x16_bf16 v[16:31], v[52:55], v[48:51], 0\n"
    "ds_read_b128 v[52:55], v65 offset:19456\n"
    "v_exp_f32 v38, v38\n"
    "v_exp_f32 v39, v39\n"
    "v_exp_f32 v40, v40\n"
    "v_exp_f32 v41, v41\n"
    "s_waitcnt lgkmcnt(2)\n"
    "v_smfmac_f32_32x32x32_bf16 v[0:15], v[56:59], v[32:39], v64\n"
    "ds_read_b128 v[56:59], v66 offset:19008\n"
    "v_exp_f32 v42, v42\n"
    "v_exp_f32 v43, v43\n"
    "v_exp_f32 v44, v44\n"
    "s_add_u32 m0, s81, 65536\n"
    "s_add_u32 s0, s50, 229376\n"
    "buffer_load_dwordx4 v105, s[8:11], s0 offen lds\n"
    "v_exp_f32 v45, v45\n"
    "v_exp_f32 v46, v46\n"
    "v_exp_f32 v47, v47\n"
    "v_exp_f32 v16, v16\n"
    "v_exp_f32 v17, v17\n"
    "s_waitcnt lgkmcnt(2)\n"
    "v_smfmac_f32_32x32x32_bf16 v[0:15], v[60:63], v[40:47], v64\n"
    "ds_read_b128 v[60:63], v67 offset:19008\n"
    "v_exp_f32 v18, v18\n"
    "v_exp_f32 v19, v19\n"
    "v_exp_f32 v20, v20\n"
    "v_exp_f32 v21, v21\n"
    "s_waitcnt lgkmcnt(2)\n"
    "v_mfma_f32_32x32x16_bf16 v[32:47], v[52:55], v[48:51], 0\n"
    "ds_read_b128 v[52:55], v65 offset:20480\n"
    "v_exp_f32 v22, v22\n"
    "v_exp_f32 v23, v23\n"
    "v_exp_f32 v24, v24\n"
    "v_exp_f32 v25, v25\n"
    "s_waitcnt lgkmcnt(2)\n"
    "v_smfmac_f32_32x32x32_bf16 v[0:15], v[56:59], v[16:23], v64\n"
    "ds_read_b128 v[56:59], v66 offset:20064\n"
    "v_exp_f32 v26, v26\n"
    "v_exp_f32 v27, v27\n"
    "v_exp_f32 v28, v28\n"
    "v_exp_f32 v29, v29\n"
    "v_exp_f32 v30, v30\n"
    "v_exp_f32 v31, v31\n"
    "v_exp_f32 v32, v32\n"
    "v_exp_f32 v33, v33\n"
    "s_waitcnt lgkmcnt(2)\n"
    "v_smfmac_f32_32x32x32_bf16 v[0:15], v[60:63], v[24:31], v64\n"
    "ds_read_b128 v[60:63], v67 offset:20064\n"
    "v_exp_f32 v34, v34\n"
    "v_exp_f32 v35, v35\n"
    "v_exp_f32 v36, v36\n"
    "v_exp_f32 v37, v37\n"
    "s_waitcnt lgkmcnt(2)\n"
    "v_mfma_f32_32x32x16_bf16 v[16:31], v[52:55], v[48:51], 0\n"
    "ds_read_b128 v[52:55], v65 offset:21504\n"
    "v_exp_f32 v38, v38\n"
    "v_exp_f32 v39, v39\n"
    "v_exp_f32 v40, v40\n"
    "v_exp_f32 v41, v41\n"
    "s_waitcnt lgkmcnt(2)\n"
    "v_smfmac_f32_32x32x32_bf16 v[0:15], v[56:59], v[32:39], v64\n"
    "ds_read_b128 v[56:59], v66 offset:21120\n"
    "v_exp_f32 v42, v42\n"
    "v_exp_f32 v43, v43\n"
    "v_exp_f32 v44, v44\n"
    "v_exp_f32 v45, v45\n"
    "v_exp_f32 v46, v46\n"
    "v_exp_f32 v47, v47\n"
    "v_exp_f32 v16, v16\n"
    "v_exp_f32 v17, v17\n"
    "s_waitcnt lgkmcnt(2)\n"
    "v_smfmac_f32_32x32x32_bf16 v[0:15], v[60:63], v[40:47], v64\n"
    "ds_read_b128 v[60:63], v67 offset:21120\n"
    "v_exp_f32 v18, v18\n"
    "v_exp_f32 v19, v19\n"
    "v_exp_f32 v20, v20\n"
    "v_exp_f32 v21, v21\n"
    "s_waitcnt lgkmcnt(2)\n"
    "v_mfma_f32_32x32x16_bf16 v[32:47], v[52:55], v[48:51], 0\n"
    "ds_read_b128 v[52:55], v65 offset:22528\n"
    "v_exp_f32 v22, v22\n"
    "v_exp_f32 v23, v23\n"
    "v_exp_f32 v24, v24\n"
    "v_exp_f32 v25, v25\n"
    "s_waitcnt lgkmcnt(2)\n"
    "v_smfmac_f32_32x32x32_bf16 v[0:15], v[56:59], v[16:23], v64\n"
    "ds_read_b128 v[56:59], v66 offset:22176\n"
    "v_exp_f32 v26, v26\n"
    "v_exp_f32 v27, v27\n"
    "v_exp_f32 v28, v28\n"
    "v_exp_f32 v29, v29\n"
    "v_exp_f32 v30, v30\n"
    "v_exp_f32 v31, v31\n"
    "s_waitcnt vmcnt(0) lgkmcnt(2)\n"
    "s_barrier\n"
    "s_mov_b32 s0, s43\n"
    "s_add_u32 s0, s0, 2097152\n"
    "buffer_store_dwordx4 v[72:75], v104, s[4:7], s0 offen sc1\n"
    "s_add_u32 s0, s70, s42\n"
    "buffer_store_dwordx4 v[72:75], v104, s[4:7], s0 offen sc1\n"
    "v_exp_f32 v32, v32\n"
    "v_exp_f32 v33, v33\n"
    "s_waitcnt lgkmcnt(2)\n"
    "v_smfmac_f32_32x32x32_bf16 v[0:15], v[60:63], v[24:31], v64\n"
    "ds_read_b128 v[60:63], v67 offset:22176\n"
    "v_exp_f32 v34, v34\n"
    "v_exp_f32 v35, v35\n"
    "v_exp_f32 v36, v36\n"
    "v_exp_f32 v37, v37\n"
    "s_waitcnt lgkmcnt(2)\n"
    "v_mfma_f32_32x32x16_bf16 v[16:31], v[52:55], v[48:51], 0\n"
    "ds_read_b128 v[52:55], v65 offset:23552\n"
    "s_add_u32 m0, s86, 49152\n"
    "s_mov_b32 s0, 2146304\n"
    "buffer_load_dwordx4 v89, s[4:7], s0 offen sc1 lds\n"
    "v_exp_f32 v38, v38\n"
    "v_exp_f32 v39, v39\n"
    "v_exp_f32 v40, v40\n"
    "v_exp_f32 v41, v41\n"
    "s_waitcnt lgkmcnt(2)\n"
    "v_smfmac_f32_32x32x32_bf16 v[0:15], v[56:59], v[32:39], v64\n"
    "ds_read_b128 v[56:59], v66 offset:23232\n"
    "v_exp_f32 v42, v42\n"
    "v_exp_f32 v43, v43\n"
    "v_exp_f32 v44, v44\n"
    "s_add_u32 m0, s86, 49152\n"
    "s_mov_b32 s0, 2146304\n"
    "buffer_load_dwordx4 v89, s[4:7], s0 offen offset:1024 sc1 lds\n"
    "ds_read_b64_tr_b16 v[128:129], v98 offset:8192\n"
    "ds_read_b64_tr_b16 v[132:133], v99 offset:8192\n"
    "ds_read_b64_tr_b16 v[136:137], v100 offset:8192\n"
    "ds_read_b64_tr_b16 v[140:141], v101 offset:8192\n"
    "v_exp_f32 v45, v45\n"
    "v_exp_f32 v46, v46\n"
    "v_exp_f32 v47, v47\n"
    "v_exp_f32 v16, v16\n"
    "v_exp_f32 v17, v17\n"
    "s_waitcnt lgkmcnt(6)\n"
    "v_smfmac_f32_32x32x32_bf16 v[0:15], v[60:63], v[40:47], v64\n"
    "ds_read_b128 v[60:63], v67 offset:23232\n"
    "v_exp_f32 v18, v18\n"
    "v_exp_f32 v19, v19\n"
    "v_exp_f32 v20, v20\n"
    "v_exp_f32 v21, v21\n"
    "s_waitcnt lgkmcnt(6)\n"
    "v_mfma_f32_32x32x16_bf16 v[32:47], v[52:55], v[48:51], 0\n"
    "ds_read_b128 v[52:55], v65 offset:24576\n"
    "s_add_u32 m0, s85, 50688\n"
    "s_mov_b32 s0, 4243456\n"
    "buffer_load_dwordx4 v89, s[4:7], s0 offen sc1 lds\n"
    "v_exp_f32 v22, v22\n"
    "v_exp_f32 v23, v23\n"
    "v_exp_f32 v24, v24\n"
    "v_exp_f32 v25, v25\n"
    "s_waitcnt lgkmcnt(6)\n"
    "v_smfmac_f32_32x32x32_bf16 v[0:15], v[56:59], v[16:23], v64\n"
    "ds_read_b128 v[56:59], v66 offset:24288\n"
    "v_exp_f32 v26, v26\n"
    "v_exp_f32 v27, v27\n"
    "v_exp_f32 v28, v28\n"
    "s_add_u32 m0, s85, 51744\n"
    "s_mov_b32 s0, 4244480\n"
    "buffer_load_dwordx4 v89, s[4:7], s0 offen sc1 lds\n"
    "ds_read_b64_tr_b16 v[130:131], v98 offset:16384\n"
    "ds_read_b64_tr_b16 v[134:135], v99 offset:16384\n"
    "ds_read_b64_tr_b16 v[138:139], v100 offset:16384\n"
    "ds_read_b64_tr_b16 v[142:143], v101 offset:16384\n"
    "v_exp_f32 v29, v29\n"
    "v_exp_f32 v30, v30\n"
    "v_exp_f32 v31, v31\n"
    "v_exp_f32 v32, v32\n"
    "v_exp_f32 v33, v33\n"
    "s_waitcnt lgkmcnt(6)\n"
    "v_smfmac_f32_32x32x32_bf16 v[0:15], v[60:63], v[24:31], v64\n"
    "ds_read_b128 v[60:63], v67 offset:24288\n"
    "v_exp_f32 v34, v34\n"
    "v_exp_f32 v35, v35\n"
    "v_exp_f32 v36, v36\n"
    "v_exp_f32 v37, v37\n"
    "s_waitcnt lgkmcnt(6)\n"
    "v_mfma_f32_32x32x16_bf16 v[16:31], v[52:55], v[48:51], 0\n"
    "ds_read_b128 v[52:55], v65 offset:25600\n"
    "s_add_u32 m0, s81, 8192\n"
    "s_add_u32 s0, s50, 286720\n"
    "buffer_load_dwordx4 v105, s[8:11], s0 offen lds\n"
    "v_exp_f32 v38, v38\n"
    "v_exp_f32 v39, v39\n"
    "v_exp_f32 v40, v40\n"
    "v_exp_f32 v41, v41\n"
    "s_waitcnt lgkmcnt(6)\n"
    "v_smfmac_f32_32x32x32_bf16 v[0:15], v[56:59], v[32:39], v64\n"
    "ds_read_b128 v[56:59], v66 offset:25344\n"
    "v_exp_f32 v42, v42\n"
    "v_exp_f32 v43, v43\n"
    "v_exp_f32 v44, v44\n"
    "s_add_u32 m0, s81, 73728\n"
    "s_add_u32 s0, s50, 344064\n"
    "buffer_load_dwordx4 v105, s[8:11], s0 offen lds\n"
    "ds_read_b64_tr_b16 v[144:145], v98 offset:24576\n"
    "ds_read_b64_tr_b16 v[148:149], v99 offset:24576\n"
    "ds_read_b64_tr_b16 v[152:153], v100 offset:24576\n"
    "ds_read_b64_tr_b16 v[156:157], v101 offset:24576\n"
    "v_exp_f32 v45, v45\n"
    "v_exp_f32 v46, v46\n"
    "v_exp_f32 v47, v47\n"
    "v_exp_f32 v16, v16\n"
    "v_exp_f32 v17, v17\n"
    "s_waitcnt lgkmcnt(6)\n"
    "v_smfmac_f32_32x32x32_bf16 v[0:15], v[60:63], v[40:47], v64\n"
    "ds_read_b128 v[60:63], v67 offset:25344\n"
    "v_exp_f32 v18, v18\n"
    "v_exp_f32 v19, v19\n"
    "v_exp_f32 v20, v20\n"
    "v_exp_f32 v21, v21\n"
    "s_waitcnt lgkmcnt(6)\n"
    "v_mfma_f32_32x32x16_bf16 v[32:47], v[52:55], v[48:51], 0\n"
    "ds_read_b128 v[52:55], v65 offset:26624\n"
    "v_exp_f32 v22, v22\n"
    "v_exp_f32 v23, v23\n"
    "v_exp_f32 v24, v24\n"
    "v_exp_f32 v25, v25\n"
    "s_waitcnt lgkmcnt(6)\n"
    "v_smfmac_f32_32x32x32_bf16 v[0:15], v[56:59], v[16:23], v64\n"
    "ds_read_b128 v[56:59], v66 offset:26400\n"
    "v_exp_f32 v26, v26\n"
    "v_exp_f32 v27, v27\n"
    "v_exp_f32 v28, v28\n"
    "ds_read_b64_tr_b16 v[146:147], v90 offset:0\n"
    "ds_read_b64_tr_b16 v[150:151], v91 offset:0\n"
    "ds_read_b64_tr_b16 v[154:155], v92 offset:0\n"
    "ds_read_b64_tr_b16 v[158:159], v93 offset:0\n"
    "s_add_u32 s0, s65, 0\n"
    "buffer_load_dword v70, v106, s[12:15], s0 offen\n"
    "v_exp_f32 v29, v29\n"
    "v_exp_f32 v30, v30\n"
    "v_exp_f32 v31, v31\n"
    "v_exp_f32 v32, v32\n"
    "v_exp_f32 v33, v33\n"
    "s_waitcnt lgkmcnt(6)\n"
    "v_smfmac_f32_32x32x32_bf16 v[0:15], v[60:63], v[24:31], v64\n"
    "ds_read_b128 v[60:63], v67 offset:26400\n"
    "v_exp_f32 v34, v34\n"
    "v_exp_f32 v35, v35\n"
    "v_exp_f32 v36, v36\n"
    "v_exp_f32 v37, v37\n"
    "s_waitcnt lgkmcnt(6)\n"
    "v_mfma_f32_32x32x16_bf16 v[16:31], v[52:55], v[48:51], 0\n"
    "ds_read_b128 v[52:55], v65 offset:27648\n"
    "v_exp_f32 v38, v38\n"
    "v_exp_f32 v39, v39\n"
    "v_exp_f32 v40, v40\n"
    "v_exp_f32 v41, v41\n"
    "s_waitcnt lgkmcnt(6)\n"
    "v_smfmac_f32_32x32x32_bf16 v[0:15], v[56:59], v[32:39], v64\n"
    "ds_read_b128 v[56:59], v66 offset:27456\n"
    "v_exp_f32 v42, v42\n"
    "v_exp_f32 v43, v43\n"
    "v_exp_f32 v44, v44\n"
    "ds_read_b64_tr_b16 v[160:161], v94 offset:0\n"
    "ds_read_b64_tr_b16 v[164:165], v95 offset:0\n"
    "ds_read_b64_tr_b16 v[168:169], v96 offset:0\n"
    "ds_read_b64_tr_b16 v[172:173], v97 offset:0\n"
    "v_exp_f32 v45, v45\n"
    "v_exp_f32 v46, v46\n"
    "v_exp_f32 v47, v47\n"
    "v_exp_f32 v16, v16\n"
    "v_exp_f32 v17, v17\n"
    "s_waitcnt lgkmcnt(6)\n"
    "v_smfmac_f32_32x32x32_bf16 v[0:15], v[60:63], v[40:47], v64\n"
    "ds_read_b128 v[60:63], v67 offset:27456\n"
    "v_exp_f32 v18, v18\n"
    "v_exp_f32 v19, v19\n"
    "v_exp_f32 v20, v20\n"
    "v_exp_f32 v21, v21\n"
    "s_waitcnt lgkmcnt(6)\n"
    "v_mfma_f32_32x32x16_bf16 v[32:47], v[52:55], v[48:51], 0\n"
    "ds_read_b128 v[52:55], v65 offset:28672\n"
    "v_exp_f32 v22, v22\n"
    "v_exp_f32 v23, v23\n"
    "v_exp_f32 v24, v24\n"
    "v_exp_f32 v25, v25\n"
    "s_waitcnt lgkmcnt(6)\n"
    "v_smfmac_f32_32x32x32_bf16 v[0:15], v[56:59], v[16:23], v64\n"
    "ds_read_b128 v[56:59], v66 offset:28512\n"
    "v_exp_f32 v26, v26\n"
    "v_exp_f32 v27, v27\n"
    "v_exp_f32 v28, v28\n"
    "v_exp_f32 v29, v29\n"
    "v_exp_f32 v30, v30\n"
    "v_exp_f32 v31, v31\n"
    "v_exp_f32 v32, v32\n"
    "v_exp_f32 v33, v33\n"
    "s_waitcnt lgkmcnt(2)\n"
    "v_smfmac_f32_32x32x32_bf16 v[0:15], v[60:63], v[24:31], v64\n"
    "ds_read_b128 v[60:63], v67 offset:28512\n"
    "v_exp_f32 v34, v34\n"
    "v_exp_f32 v35, v35\n"
    "v_exp_f32 v36, v36\n"
    "v_exp_f32 v37, v37\n"
    "s_waitcnt lgkmcnt(2)\n"
    "v_mfma_f32_32x32x16_bf16 v[16:31], v[52:55], v[48:51], 0\n"
    "ds_read_b128 v[52:55], v65 offset:29696\n"
    "v_exp_f32 v38, v38\n"
    "v_exp_f32 v39, v39\n"
    "v_exp_f32 v40, v40\n"
    "v_exp_f32 v41, v41\n"
    "s_waitcnt lgkmcnt(2)\n"
    "v_smfmac_f32_32x32x32_bf16 v[0:15], v[56:59], v[32:39], v64\n"
    "ds_read_b128 v[56:59], v66 offset:29568\n"
    "v_exp_f32 v42, v42\n"
    "v_exp_f32 v43, v43\n"
    "v_exp_f32 v44, v44\n"
    "v_exp_f32 v45, v45\n"
    "v_exp_f32 v46, v46\n"
    "v_exp_f32 v47, v47\n"
    "v_exp_f32 v16, v16\n"
    "v_exp_f32 v17, v17\n"
    "s_waitcnt lgkmcnt(2)\n"
    "v_smfmac_f32_32x32x32_bf16 v[0:15], v[60:63], v[40:47], v64\n"
    "ds_read_b128 v[60:63], v67 offset:29568\n"
    "v_exp_f32 v18, v18\n"
    "v_exp_f32 v19, v19\n"
    "v_exp_f32 v20, v20\n"
    "v_exp_f32 v21, v21\n"
    "s_waitcnt lgkmcnt(2)\n"
    "v_mfma_f32_32x32x16_bf16 v[32:47], v[52:55], v[48:51], 0\n"
    "ds_read_b128 v[52:55], v65 offset:30720\n"
    "v_exp_f32 v22, v22\n"
    "v_exp_f32 v23, v23\n"
    "v_exp_f32 v24, v24\n"
    "v_exp_f32 v25, v25\n"
    "s_waitcnt lgkmcnt(2)\n"
    "v_smfmac_f32_32x32x32_bf16 v[0:15], v[56:59], v[16:23], v64\n"
    "ds_read_b128 v[56:59], v66 offset:30624\n"
    "v_exp_f32 v26, v26\n"
    "v_exp_f32 v27, v27\n"
    "v_exp_f32 v28, v28\n"
    "v_exp_f32 v29, v29\n"
    "v_exp_f32 v30, v30\n"
    "v_exp_f32 v31, v31\n"
    "s_waitcnt vmcnt(1) lgkmcnt(2)\n"
    "s_barrier\n"
    "s_cmp_eq_u32 s36, 0\n"
    "s_cbranch_scc0 Lw_60_%=\n"
    "s_sub_u32 s0, s63, 0x130000\n"
    "s_lshr_b32 s0, s0, 2\n"
    "s_add_u32 s3, s0, 0x31000\n"
    "v_mov_b32 v68, s72\n"
    "v_mov_b32 v69, s73\n"
    "s_mov_b64 exec, 1\n"
    "buffer_store_dwordx2 v[68:69], off, s[4:7], s3 sc1\n"
    "s_mov_b64 exec, -1\n"
    "Lw_60_%=:\n"
    "s_add_u32 s0, s70, s38\n"
    "s_add_u32 s0, s0, 12582912\n"
    "buffer_store_dwordx4 v[72:75], v104, s[4:7], s0 offen sc1\n"
    "v_exp_f32 v32, v32\n"
    "v_exp_f32 v33, v33\n"
    "s_waitcnt lgkmcnt(2)\n"
    "v_smfmac_f32_32x32x32_bf16 v[0:15], v[60:63], v[24:31], v64\n"
    "ds_read_b128 v[60:63], v67 offset:30624\n"
    "v_exp_f32 v34, v34\n"
    "v_exp_f32 v35, v35\n"
    "v_exp_f32 v36, v36\n"
    "v_exp_f32 v37, v37\n"
    "s_waitcnt lgkmcnt(2)\n"
    "v_mfma_f32_32x32x16_bf16 v[16:31], v[52:55], v[48:51], 0\n"
    "ds_read_b128 v[52:55], v65 offset:31744\n"
    "s_add_u32 m0, s81, 139264\n"
    "s_add_u32 s0, s50, 401408\n"
    "buffer_load_dwordx4 v105, s[8:11], s0 offen lds\n"
    "v_exp_f32 v38, v38\n"
    "v_exp_f32 v39, v39\n"
    "v_exp_f32 v40, v40\n"
    "v_exp_f32 v41, v41\n"
    "s_waitcnt lgkmcnt(2)\n"
    "v_smfmac_f32_32x32x32_bf16 v[0:15], v[56:59], v[32:39], v64\n"
    "ds_read_b128 v[56:59], v66 offset:31680\n"
    "v_exp_f32 v42, v42\n"
    "v_exp_f32 v43, v43\n"
    "v_exp_f32 v44, v44\n"
    "s_add_u32 m0, s81, 147456\n"
    "s_add_u32 s0, s50, 458752\n"
    "buffer_load_dwordx4 v105, s[8:11], s0 offen lds\n"
    "ds_read_b64_tr_b16 v[162:163], v90 offset:8192\n"
    "ds_read_b64_tr_b16 v[166:167], v91 offset:8192\n"
    "ds_read_b64_tr_b16 v[170:171], v92 offset:8192\n"
    "ds_read_b64_tr_b16 v[174:175], v93 offset:8192\n"
    "v_exp_f32 v45, v45\n"
    "v_exp_f32 v46, v46\n"
    "v_exp_f32 v47, v47\n"
    "v_exp_f32 v16, v16\n"
    "v_exp_f32 v17, v17\n"
    "s_waitcnt lgkmcnt(6)\n"
    "v_smfmac_f32_32x32x32_bf16 v[0:15], v[60:63], v[40:47], v64\n"
    "ds_read_b128 v[60:63], v67 offset:31680\n"
    "v_exp_f32 v18, v18\n"
    "v_exp_f32 v19, v19\n"
    "v_exp_f32 v20, v20\n"
    "v_exp_f32 v21, v21\n"
    "s_waitcnt lgkmcnt(6)\n"
    "v_mfma_f32_32x32x16_bf16 v[32:47], v[52:55], v[48:51], 0\n"
    "ds_read_b128 v[52:55], v65 offset:32768\n"
    "s_add_u32 m0, s81, 155648\n"
    "s_add_u32 s0, s50, 516096\n"
    "buffer_load_dwordx4 v105, s[8:11], s0 offen lds\n"
    "v_exp_f32 v22, v22\n"
    "v_exp_f32 v23, v23\n"
    "v_exp_f32 v24, v24\n"
    "v_exp_f32 v25, v25\n"
    "s_waitcnt lgkmcnt(6)\n"
    "v_smfmac_f32_32x32x32_bf16 v[0:15], v[56:59], v[16:23], v64\n"
    "ds_read_b128 v[56:59], v66 offset:32736\n"
    "v_exp_f32 v26, v26\n"
    "v_exp_f32 v27, v27\n"
    "v_exp_f32 v28, v28\n"
    "s_add_u32 m0, s81, 0\n"
    "s_add_u32 s0, s50, 573440\n"
    "buffer_load_dwordx4 v105, s[8:11], s0 offen lds\n"
    "ds_read_b64_tr_b16 v[176:177], v94 offset:8192\n"
    "ds_read_b64_tr_b16 v[180:181], v95 offset:8192\n"
    "ds_read_b64_tr_b16 v[184:185], v96 offset:8192\n"
    "ds_read_b64_tr_b16 v[188:189], v97 offset:8192\n"
    "v_exp_f32 v29, v29\n"
    "v_exp_f32 v30, v30\n"
    "v_exp_f32 v31, v31\n"
    "v_exp_f32 v32, v32\n"
    "v_exp_f32 v33, v33\n"
    "s_waitcnt lgkmcnt(6)\n"
    "v_smfmac_f32_32x32x32_bf16 v[0:15], v[60:63], v[24:31], v64\n"
    "ds_read_b128 v[60:63], v67 offset:32736\n"
    "v_exp_f32 v34, v34\n"
    "v_exp_f32 v35, v35\n"
    "v_exp_f32 v36, v36\n"
    "v_exp_f32 v37, v37\n"
    "s_waitcnt lgkmcnt(6)\n"
    "v_mfma_f32_32x32x16_bf16 v[16:31], v[52:55], v[48:51], 0\n"
    "ds_read_b128 v[52:55], v65 offset:33792\n"
    "s_add_u32 m0, s81, 65536\n"
    "s_add_u32 s0, s50, 630784\n"
    "buffer_load_dwordx4 v105, s[8:11], s0 offen lds\n"
    "v_exp_f32 v38, v38\n"
    "v_exp_f32 v39, v39\n"
    "v_exp_f32 v40, v40\n"
    "v_exp_f32 v41, v41\n"
    "s_waitcnt lgkmcnt(6)\n"
    "v_smfmac_f32_32x32x32_bf16 v[0:15], v[56:59], v[32:39], v64\n"
    "ds_read_b128 v[56:59], v66 offset:33792\n"
    "v_exp_f32 v42, v42\n"
    "v_exp_f32 v43, v43\n"
    "v_exp_f32 v44, v44\n"
    "s_add_u32 m0, s81, 16384\n"
    "s_add_u32 s0, s50, 688128\n"
    "buffer_load_dwordx4 v105, s[8:11], s0 offen lds\n"
    "v_exp_f32 v45, v45\n"
    "v_exp_f32 v46, v46\n"
    "v_exp_f32 v47, v47\n"
    "v_add_u32 v66, 33792, v66\n"
    "v_add_u32 v67, 33792, v67\n"
    "v_exp_f32 v16, v16\n"
    "v_exp_f32 v17, v17\n"
    "s_waitcnt lgkmcnt(2)\n"
    "v_smfmac_f32_32x32x32_bf16 v[0:15], v[60:63], v[40:47], v64\n"
    "ds_read_b128 v[60:63], v67 offset:0\n"
    "v_exp_f32 v18, v18\n"
    "v_exp_f32 v19, v19\n"
    "v_exp_f32 v20, v20\n"
    "v_exp_f32 v21, v21\n"
    "s_waitcnt lgkmcnt(2)\n"
    "v_mfma_f32_32x32x16_bf16 v[32:47], v[52:55], v[48:51], 0\n"
    "ds_read_b128 v[52:55], v65 offset:34816\n"
    "v_exp_f32 v22, v22\n"
    "v_exp_f32 v23, v23\n"
    "v_exp_f32 v24, v24\n"
    "v_exp_f32 v25, v25\n"
    "s_waitcnt lgkmcnt(2)\n"
    "v_smfmac_f32_32x32x32_bf16 v[0:15], v[56:59], v[16:23], v64\n"
    "ds_read_b128 v[56:59], v66 offset:1056\n"
    "v_exp_f32 v26, v26\n"
    "v_exp_f32 v27, v27\n"
    "v_exp_f32 v28, v28\n"
    "s_add_u32 m0, s81, 81920\n"
    "s_add_u32 s0, s50, 745472\n"
    "buffer_load_dwordx4 v105, s[8:11], s0 offen lds\n"
    "v_exp_f32 v29, v29\n"
    "v_exp_f32 v30, v30\n"
    "v_exp_f32 v31, v31\n"
    "v_exp_f32 v32, v32\n"
    "v_exp_f32 v33, v33\n"
    "s_waitcnt lgkmcnt(2)\n"
    "v_smfmac_f32_32x32x32_bf16 v[0:15], v[60:63], v[24:31], v64\n"
    "ds_read_b128 v[60:63], v67 offset:1056\n"
    "v_exp_f32 v34, v34\n"
    "v_exp_f32 v35, v35\n"
    "v_exp_f32 v36, v36\n"
    "v_exp_f32 v37, v37\n"
    "s_waitcnt lgkmcnt(2)\n"
    "v_mfma_f32_32x32x16_bf16 v[16:31], v[52:55], v[48:51], 0\n"
    "ds_read_b128 v[52:55], v65 offset:35840\n"
    "v_exp_f32 v38, v38\n"
    "v_exp_f32 v39, v39\n"
    "v_exp_f32 v40, v40\n"
    "v_exp_f32 v41, v41\n"
    "s_waitcnt lgkmcnt(2)\n"
    "v_smfmac_f32_32x32x32_bf16 v[0:15], v[56:59], v[32:39], v64\n"
    "ds_read_b128 v[56:59], v66 offset:2112\n"
    "v_exp_f32 v42, v42\n"
    "v_exp_f32 v43, v43\n"
    "v_exp_f32 v44, v44\n"
    "s_add_u32 s0, s65, 32768\n"
    "buffer_load_dword v70, v106, s[12:15], s0 offen\n"
    "v_exp_f32 v45, v45\n"
    "v_exp_f32 v46, v46\n"
    "v_exp_f32 v47, v47\n"
    "v_exp_f32 v16, v16\n"
    "v_exp_f32 v17, v17\n"
    "s_waitcnt lgkmcnt(2)\n"
    "v_smfmac_f32_32x32x32_bf16 v[0:15], v[60:63], v[40:47], v64\n"
    "ds_read_b128 v[60:63], v67 offset:2112\n"
    "v_exp_f32 v18, v18\n"
    "v_exp_f32 v19, v19\n"
    "v_exp_f32 v20, v20\n"
    "v_exp_f32 v21, v21\n"
    "s_waitcnt lgkmcnt(2)\n"
    "v_mfma_f32_32x32x16_bf16 v[32:47], v[52:55], v[48:51], 0\n"
    "ds_read_b128 v[52:55], v65 offset:36864\n"
    "v_exp_f32 v22, v22\n"
    "v_exp_f32 v23, v23\n"
    "v_exp_f32 v24, v24\n"
    "v_exp_f32 v25, v25\n"
    "s_waitcnt lgkmcnt(2)\n"
    "v_smfmac_f32_32x32x32_bf16 v[0:15], v[56:59], v[16:23], v64\n"
    "ds_read_b128 v[56:59], v66 offset:3168\n"
    "v_exp_f32 v26, v26\n"
    "v_exp_f32 v27, v27\n"
    "v_exp_f32 v28, v28\n"
    "v_exp_f32 v29, v29\n"
    "v_exp_f32 v30, v30\n"
    "v_exp_f32 v31, v31\n"
    "v_exp_f32 v32, v32\n"
    "v_exp_f32 v33, v33\n"
    "s_waitcnt lgkmcnt(2)\n"
    "v_smfmac_f32_32x32x32_bf16 v[0:15], v[60:63], v[24:31], v64\n"
    "ds_read_b128 v[60:63], v67 offset:3168\n"
    "v_exp_f32 v34, v34\n"
    "v_exp_f32 v35, v35\n"
    "v_exp_f32 v36, v36\n"
    "v_exp_f32 v37, v37\n"
    "s_waitcnt lgkmcnt(2)\n"
    "v_mfma_f32_32x32x16_bf16 v[16:31], v[52:55], v[48:51], 0\n"
    "ds_read_b128 v[52:55], v65 offset:37888\n"
    "v_exp_f32 v38, v38\n"
    "v_exp_f32 v39, v39\n"
    "v_exp_f32 v40, v40\n"
    "v_exp_f32 v41, v41\n"
    "s_waitcnt lgkmcnt(2)\n"
    "v_smfmac_f32_32x32x32_bf16 v[0:15], v[56:59], v[32:39], v64\n"
    "ds_read_b128 v[56:59], v66 offset:4224\n"
    "v_exp_f32 v42, v42\n"
    "v_exp_f32 v43, v43\n"
    "v_exp_f32 v44, v44\n"
    "v_exp_f32 v45, v45\n"
    "v_exp_f32 v46, v46\n"
    "v_exp_f32 v47, v47\n"
    "v_exp_f32 v16, v16\n"
    "v_exp_f32 v17, v17\n"
    "s_waitcnt lgkmcnt(2)\n"
    "v_smfmac_f32_32x32x32_bf16 v[0:15], v[60:63], v[40:47], v64\n"
    "ds_read_b128 v[60:63], v67 offset:4224\n"
    "v_exp_f32 v18, v18\n"
    "v_exp_f32 v19, v19\n"
    "v_exp_f32 v20, v20\n"
    "v_exp_f32 v21, v21\n"
    "s_waitcnt lgkmcnt(2)\n"
    "v_mfma_f32_32x32x16_bf16 v[32:47], v[52:55], v[48:51], 0\n"
    "ds_read_b128 v[52:55], v65 offset:38912\n"
    "v_exp_f32 v22, v22\n"
    "v_exp_f32 v23, v23\n"
    "v_exp_f32 v24, v24\n"
    "v_exp_f32 v25, v25\n"
    "s_waitcnt lgkmcnt(2)\n"
    "v_smfmac_f32_32x32x32_bf16 v[0:15], v[56:59], v[16:23], v64\n"
    "ds_read_b128 v[56:59], v66 offset:5280\n"
    "v_exp_f32 v26, v26\n"
    "v_exp_f32 v27, v27\n"
    "v_exp_f32 v28, v28\n"
    "v_exp_f32 v29, v29\n"
    "v_exp_f32 v30, v30\n"
    "v_exp_f32 v31, v31\n"
    "s_waitcnt vmcnt(1) lgkmcnt(2)\n"
    "s_barrier\n"
    "s_cmp_eq_u32 s69, 0\n"
    "s_cbranch_scc0 Lc_59_%=\n"
    "s_add_u32 s0, s70, s77\n"
    "s_add_u32 s0, s0, 12582912\n"
    "buffer_store_dwordx4 v[72:75], v104, s[4:7], s0 offen sc1\n"
    "Lc_59_%=:\n"
    "v_exp_f32 v32, v32\n"
    "v_exp_f32 v33, v33\n"
    "s_waitcnt lgkmcnt(2)\n"
    "v_smfmac_f32_32x32x32_bf16 v[0:15], v[60:63], v[24:31], v64\n"
    "ds_read_b128 v[60:63], v67 offset:5280\n"
    "v_exp_f32 v34, v34\n"
    "v_exp_f32 v35, v35\n"
    "v_exp_f32 v36, v36\n"
    "v_exp_f32 v37, v37\n"
    "s_waitcnt lgkmcnt(2)\n"
    "v_mfma_f32_32x32x16_bf16 v[16:31], v[52:55], v[48:51], 0\n"
    "ds_read_b128 v[52:55], v65 offset:39936\n"
    "s_add_u32 m0, s81, 24576\n"
    "s_add_u32 s0, s50, 802816\n"
    "buffer_load_dwordx4 v105, s[8:11], s0 offen lds\n"
    "v_exp_f32 v38, v38\n"
    "v_exp_f32 v39, v39\n"
    "v_exp_f32 v40, v40\n"
    "v_exp_f32 v41, v41\n"
    "s_waitcnt lgkmcnt(2)\n"
    "v_smfmac_f32_32x32x32_bf16 v[0:15], v[56:59], v[32:39], v64\n"
    "ds_read_b128 v[56:59], v66 offset:6336\n"
    "v_exp_f32 v42, v42\n"
    "v_exp_f32 v43, v43\n"
    "v_exp_f32 v44, v44\n"
    "s_add_u32 m0, s81, 90112\n"
    "s_add_u32 s0, s50, 860160\n"
    "buffer_load_dwordx4 v105, s[8:11], s0 offen lds\n"
    "ds_read_b64_tr_b16 v[178:179], v98 offset:8192\n"
    "ds_read_b64_tr_b16 v[182:183], v99 offset:8192\n"
    "ds_read_b64_tr_b16 v[186:187], v100 offset:8192\n"
    "ds_read_b64_tr_b16 v[190:191], v101 offset:8192\n"
    "v_exp_f32 v45, v45\n"
    "v_exp_f32 v46, v46\n"
    "v_exp_f32 v47, v47\n"
    "v_exp_f32 v16, v16\n"
    "v_exp_f32 v17, v17\n"
    "s_waitcnt lgkmcnt(6)\n"
    "v_smfmac_f32_32x32x32_bf16 v[0:15], v[60:63], v[40:47], v64\n"
    "ds_read_b128 v[60:63], v67 offset:6336\n"
    "v_exp_f32 v18, v18\n"
    "v_exp_f32 v19, v19\n"
    "v_exp_f32 v20, v20\n"
    "v_exp_f32 v21, v21\n"
    "s_waitcnt lgkmcnt(6)\n"
    "v_mfma_f32_32x32x16_bf16 v[32:47], v[52:55], v[48:51], 0\n"
    "ds_read_b128 v[52:55], v65 offset:40960\n"
    "s_add_u32 m0, s37, 8192\n"
    "s_add_u32 s0, s52, 16384\n"
    "buffer_load_dwordx4 v107, s[16:19], s0 offen lds\n"
    "v_exp_f32 v22, v22\n"
    "v_exp_f32 v23, v23\n"
    "v_exp_f32 v24, v24\n"
    "v_exp_f32 v25, v25\n"
    "s_waitcnt lgkmcnt(6)\n"
    "v_smfmac_f32_32x32x32_bf16 v[0:15], v[56:59], v[16:23], v64\n"
    "ds_read_b128 v[56:59], v66 offset:7392\n"
    "v_exp_f32 v26, v26\n"
    "v_exp_f32 v27, v27\n"
    "v_exp_f32 v28, v28\n"
    "s_add_u32 m0, s81, 73728\n"
    "s_add_u32 s0, s50, 974848\n"
    "buffer_load_dwordx4 v105, s[8:11], s0 offen lds\n"
    "ds_read_b64_tr_b16 v[192:193], v98 offset:16384\n"
    "ds_read_b64_tr_b16 v[196:197], v99 offset:16384\n"
    "ds_read_b64_tr_b16 v[200:201], v100 offset:16384\n"
    "ds_read_b64_tr_b16 v[204:205], v101 offset:16384\n"
    "v_exp_f32 v29, v29\n"
    "v_exp_f32 v30, v30\n"
    "v_exp_f32 v31, v31\n"
    "v_exp_f32 v32, v32\n"
    "v_exp_f32 v33, v33\n"
    "s_waitcnt lgkmcnt(6)\n"
    "v_smfmac_f32_32x32x32_bf16 v[0:15], v[60:63], v[24:31], v64\n"
    "ds_read_b128 v[60:63], v67 offset:7392\n"
    "v_exp_f32 v34, v34\n"
    "v_exp_f32 v35, v35\n"
    "v_exp_f32 v36, v36\n"
    "v_exp_f32 v37, v37\n"
    "s_waitcnt lgkmcnt(6)\n"
    "v_mfma_f32_32x32x16_bf16 v[16:31], v[52:55], v[48:51], 0\n"
    "ds_read_b128 v[52:55], v65 offset:41984\n"
    "v_exp_f32 v38, v38\n"
    "v_exp_f32 v39, v39\n"
    "v_exp_f32 v40, v40\n"
    "v_exp_f32 v41, v41\n"
    "s_waitcnt lgkmcnt(6)\n"
    "v_smfmac_f32_32x32x32_bf16 v[0:15], v[56:59], v[32:39], v64\n"
    "ds_read_b128 v[56:59], v66 offset:8448\n"
    "v_exp_f32 v42, v42\n"
    "v_exp_f32 v43, v43\n"
    "v_exp_f32 v44, v44\n"
    "ds_read_b64_tr_b16 v[194:195], v98 offset:24576\n"
    "ds_read_b64_tr_b16 v[198:199], v99 offset:24576\n"
    "ds_read_b64_tr_b16 v[202:203], v100 offset:24576\n"
    "ds_read_b64_tr_b16 v[206:207], v101 offset:24576\n"
    "s_add_u32 s0, s65, 65536\n"
    "buffer_load_dword v70, v106, s[12:15], s0 offen\n"
    "v_exp_f32 v45, v45\n"
    "v_exp_f32 v46, v46\n"
    "v_exp_f32 v47, v47\n"
    "v_exp_f32 v16, v16\n"
    "v_exp_f32 v17, v17\n"
    "s_waitcnt lgkmcnt(6)\n"
    "v_smfmac_f32_32x32x32_bf16 v[0:15], v[60:63], v[40:47], v64\n"
    "ds_read_b128 v[60:63], v67 offset:8448\n"
    "v_exp_f32 v18, v18\n"
    "v_exp_f32 v19, v19\n"
    "v_exp_f32 v20, v20\n"
    "v_exp_f32 v21, v21\n"
    "s_waitcnt lgkmcnt(6)\n"
    "v_mfma_f32_32x32x16_bf16 v[32:47], v[52:55], v[48:51], 0\n"
    "ds_read_b128 v[52:55], v65 offset:43008\n"
    "v_exp_f32 v22, v22\n"
    "v_exp_f32 v23, v23\n"
    "v_exp_f32 v24, v24\n"
    "v_exp_f32 v25, v25\n"
    "s_waitcnt lgkmcnt(6)\n"
    "v_smfmac_f32_32x32x32_bf16 v[0:15], v[56:59], v[16:23], v64\n"
    "ds_read_b128 v[56:59], v66 offset:9504\n"
    "v_exp_f32 v26, v26\n"
    "v_exp_f32 v27, v27\n"
    "v_exp_f32 v28, v28\n"
    "ds_read_b64_tr_b16 v[208:209], v90 offset:0\n"
    "ds_read_b64_tr_b16 v[212:213], v91 offset:0\n"
    "ds_read_b64_tr_b16 v[216:217], v92 offset:0\n"
    "ds_read_b64_tr_b16 v[220:221], v93 offset:0\n"
    "v_exp_f32 v29, v29\n"
    "v_exp_f32 v30, v30\n"
    "v_exp_f32 v31, v31\n"
    "v_exp_f32 v32, v32\n"
    "v_exp_f32 v33, v33\n"
    "s_waitcnt lgkmcnt(6)\n"
    "v_smfmac_f32_32x32x32_bf16 v[0:15], v[60:63], v[24:31], v64\n"
    "ds_read_b128 v[60:63], v67 offset:9504\n"
    "v_exp_f32 v34, v34\n"
    "v_exp_f32 v35, v35\n"
    "v_exp_f32 v36, v36\n"
    "v_exp_f32 v37, v37\n"
    "s_waitcnt lgkmcnt(6)\n"
    "v_mfma_f32_32x32x16_bf16 v[16:31], v[52:55], v[48:51], 0\n"
    "ds_read_b128 v[52:55], v65 offset:44032\n"
    "v_exp_f32 v38, v38\n"
    "v_exp_f32 v39, v39\n"
    "v_exp_f32 v40, v40\n"
    "v_exp_f32 v41, v41\n"
    "s_waitcnt lgkmcnt(6)\n"
    "v_smfmac_f32_32x32x32_bf16 v[0:15], v[56:59], v[32:39], v64\n"
    "ds_read_b128 v[56:59], v66 offset:10560\n"
    "v_exp_f32 v42, v42\n"
    "v_exp_f32 v43, v43\n"
    "v_exp_f32 v44, v44\n"
    "ds_read_b64_tr_b16 v[210:211], v94 offset:0\n"
    "ds_read_b64_tr_b16 v[214:215], v95 offset:0\n"
    "ds_read_b64_tr_b16 v[218:219], v96 offset:0\n"
    "ds_read_b64_tr_b16 v[222:223], v97 offset:0\n"
    "v_exp_f32 v45, v45\n"
    "v_exp_f32 v46, v46\n"
    "v_exp_f32 v47, v47\n"
    "v_exp_f32 v16, v16\n"
    "v_exp_f32 v17, v17\n"
    "s_waitcnt lgkmcnt(6)\n"
    "v_smfmac_f32_32x32x32_bf16 v[0:15], v[60:63], v[40:47], v64\n"
    "ds_read_b128 v[60:63], v67 offset:10560\n"
    "v_exp_f32 v18, v18\n"
    "v_exp_f32 v19, v19\n"
    "v_exp_f32 v20, v20\n"
    "v_exp_f32 v21, v21\n"
    "s_waitcnt lgkmcnt(6)\n"
    "v_mfma_f32_32x32x16_bf16 v[32:47], v[52:55], v[48:51], 0\n"
    "ds_read_b128 v[52:55], v65 offset:45056\n"
    "v_exp_f32 v22, v22\n"
    "v_exp_f32 v23, v23\n"
    "v_exp_f32 v24, v24\n"
    "v_exp_f32 v25, v25\n"
    "s_waitcnt lgkmcnt(6)\n"
    "v_smfmac_f32_32x32x32_bf16 v[0:15], v[56:59], v[16:23], v64\n"
    "ds_read_b128 v[56:59], v66 offset:11616\n"
    "v_exp_f32 v26, v26\n"
    "v_exp_f32 v27, v27\n"
    "v_exp_f32 v28, v28\n"
    "ds_read_b64_tr_b16 v[224:225], v90 offset:16384\n"
    "ds_read_b64_tr_b16 v[228:229], v91 offset:16384\n"
    "ds_read_b64_tr_b16 v[232:233], v92 offset:16384\n"
    "ds_read_b64_tr_b16 v[236:237], v93 offset:16384\n"
    "v_exp_f32 v29, v29\n"
    "v_exp_f32 v30, v30\n"
    "v_exp_f32 v31, v31\n"
    "v_exp_f32 v32, v32\n"
    "v_exp_f32 v33, v33\n"
    "s_waitcnt lgkmcnt(6)\n"
    "v_smfmac_f32_32x32x32_bf16 v[0:15], v[60:63], v[24:31], v64\n"
    "ds_read_b128 v[60:63], v67 offset:11616\n"
    "v_exp_f32 v34, v34\n"
    "v_exp_f32 v35, v35\n"
    "v_exp_f32 v36, v36\n"
    "v_exp_f32 v37, v37\n"
    "s_waitcnt lgkmcnt(6)\n"
    "v_mfma_f32_32x32x16_bf16 v[16:31], v[52:55], v[48:51], 0\n"
    "ds_read_b128 v[52:55], v65 offset:46080\n"
    "v_exp_f32 v38, v38\n"
    "v_exp_f32 v39, v39\n"
    "v_exp_f32 v40, v40\n"
    "v_exp_f32 v41, v41\n"
    "s_waitcnt lgkmcnt(6)\n"
    "v_smfmac_f32_32x32x32_bf16 v[0:15], v[56:59], v[32:39], v64\n"
    "ds_read_b128 v[56:59], v66 offset:12672\n"
    "v_exp_f32 v42, v42\n"
    "v_exp_f32 v43, v43\n"
    "v_exp_f32 v44, v44\n"
    "ds_read_b64_tr_b16 v[226:227], v94 offset:16384\n"
    "ds_read_b64_tr_b16 v[230:231], v95 offset:16384\n"
    "ds_read_b64_tr_b16 v[234:235], v96 offset:16384\n"
    "ds_read_b64_tr_b16 v[238:239], v97 offset:16384\n"
    "v_exp_f32 v45, v45\n"
    "v_exp_f32 v46, v46\n"
    "v_exp_f32 v47, v47\n"
    "v_exp_f32 v16, v16\n"
    "v_exp_f32 v17, v17\n"
    "s_waitcnt lgkmcnt(6)\n"
    "v_smfmac_f32_32x32x32_bf16 v[0:15], v[60:63], v[40:47], v64\n"
    "ds_read_b128 v[60:63], v67 offset:12672\n"
    "v_exp_f32 v18, v18\n"
    "v_exp_f32 v19, v19\n"
    "v_exp_f32 v20, v20\n"
    "v_exp_f32 v21, v21\n"
    "s_waitcnt lgkmcnt(6)\n"
    "v_mfma_f32_32x32x16_bf16 v[32:47], v[52:55], v[48:51], 0\n"
    "ds_read_b128 v[52:55], v65 offset:47104\n"
    "v_exp_f32 v22, v22\n"
    "v_exp_f32 v23, v23\n"
    "v_exp_f32 v24, v24\n"
    "v_exp_f32 v25, v25\n"
    "s_waitcnt lgkmcnt(6)\n"
    "v_smfmac_f32_32x32x32_bf16 v[0:15], v[56:59], v[16:23], v64\n"
    "ds_read_b128 v[56:59], v66 offset:13728\n"
    "v_exp_f32 v26, v26\n"
    "v_exp_f32 v27, v27\n"
    "v_exp_f32 v28, v28\n"
    "v_exp_f32 v29, v29\n"
    "v_exp_f32 v30, v30\n"
    "v_exp_f32 v31, v31\n"
    "s_waitcnt vmcnt(3) lgkmcnt(2)\n"
    "s_barrier\n"
    "s_add_u32 s0, s70, s40\n"
    "s_add_u32 s0, s0, 12582912\n"
    "buffer_store_dwordx4 v[72:75], v113, s[4:7], s0 offen sc1\n"
    "v_exp_f32 v32, v32\n"
    "v_exp_f32 v33, v33\n"
    "s_waitcnt lgkmcnt(2)\n"
    "v_smfmac_f32_32x32x32_bf16 v[0:15], v[60:63], v[24:31], v64\n"
    "ds_read_b128 v[60:63], v67 offset:13728\n"
    "v_exp_f32 v34, v34\n"
    "v_exp_f32 v35, v35\n"
    "v_exp_f32 v36, v36\n"
    "v_exp_f32 v37, v37\n"
    "s_waitcnt lgkmcnt(2)\n"
    "v_mfma_f32_32x32x16_bf16 v[16:31], v[52:55], v[48:51], 0\n"
    "ds_read_b128 v[52:55], v65 offset:48128\n"
    "s_add_u32 m0, s37, 0\n"
    "s_add_u32 s0, s52, 0\n"
    "buffer_load_dwordx4 v107, s[16:19], s0 offen lds\n"
    "v_exp_f32 v38, v38\n"
    "v_exp_f32 v39, v39\n"
    "v_exp_f32 v40, v40\n"
    "v_exp_f32 v41, v41\n"
    "s_waitcnt lgkmcnt(2)\n"
    "v_smfmac_f32_32x32x32_bf16 v[0:15], v[56:59], v[32:39], v64\n"
    "ds_read_b128 v[56:59], v66 offset:14784\n"
    "v_exp_f32 v42, v42\n"
    "v_exp_f32 v43, v43\n"
    "v_exp_f32 v44, v44\n"
    "s_add_u32 m0, s37, 16384\n"
    "s_add_u32 s0, s52, 32768\n"
    "buffer_load_dwordx4 v107, s[16:19], s0 offen lds\n"
    "ds_read_b64_tr_b16 v[240:241], v90 offset:24576\n"
    "ds_read_b64_tr_b16 v[244:245], v91 offset:24576\n"
    "ds_read_b64_tr_b16 v[248:249], v92 offset:24576\n"
    "ds_read_b64_tr_b16 v[252:253], v93 offset:24576\n"
    "v_exp_f32 v45, v45\n"
    "v_exp_f32 v46, v46\n"
    "v_exp_f32 v47, v47\n"
    "v_exp_f32 v16, v16\n"
    "v_exp_f32 v17, v17\n"
    "s_waitcnt lgkmcnt(6)\n"
    "v_smfmac_f32_32x32x32_bf16 v[0:15], v[60:63], v[40:47], v64\n"
    "ds_read_b128 v[60:63], v67 offset:14784\n"
    "v_exp_f32 v18, v18\n"
    "v_exp_f32 v19, v19\n"
    "v_exp_f32 v20, v20\n"
    "v_exp_f32 v21, v21\n"
    "s_waitcnt lgkmcnt(6)\n"
    "v_mfma_f32_32x32x16_bf16 v[32:47], v[52:55], v[48:51], 0\n"
    "ds_read_b128 v[52:55], v65 offset:49152\n"
    "s_add_u32 m0, s37, 32768\n"
    "s_add_u32 s0, s52, 65536\n"
    "buffer_load_dwordx4 v107, s[16:19], s0 offen lds\n"
    "v_exp_f32 v22, v22\n"
    "v_exp_f32 v23, v23\n"
    "v_exp_f32 v24, v24\n"
    "v_exp_f32 v25, v25\n"
    "s_waitcnt lgkmcnt(6)\n"
    "v_smfmac_f32_32x32x32_bf16 v[0:15], v[56:59], v[16:23], v64\n"
    "ds_read_b128 v[56:59], v66 offset:15840\n"
    "v_exp_f32 v26, v26\n"
    "v_exp_f32 v27, v27\n"
    "v_exp_f32 v28, v28\n"
    "s_add_u32 m0, s37, 147456\n"
    "s_add_u32 s0, s52, 98304\n"
    "buffer_load_dwordx4 v107, s[16:19], s0 offen lds\n"
    "ds_read_b64_tr_b16 v[242:243], v94 offset:24576\n"
    "ds_read_b64_tr_b16 v[246:247], v95 offset:24576\n"
    "ds_read_b64_tr_b16 v[250:251], v96 offset:24576\n"
    "ds_read_b64_tr_b16 v[254:255], v97 offset:24576\n"
    "v_exp_f32 v29, v29\n"
    "v_exp_f32 v30, v30\n"
    "v_exp_f32 v31, v31\n"
    "v_exp_f32 v32, v32\n"
    "v_exp_f32 v33, v33\n"
    "s_waitcnt lgkmcnt(6)\n"
    "v_smfmac_f32_32x32x32_bf16 v[0:15], v[60:63], v[24:31], v64\n"
    "ds_read_b128 v[60:63], v67 offset:15840\n"
    "v_exp_f32 v34, v34\n"
    "v_exp_f32 v35, v35\n"
    "v_exp_f32 v36, v36\n"
    "v_exp_f32 v37, v37\n"
    "s_waitcnt lgkmcnt(6)\n"
    "v_mfma_f32_32x32x16_bf16 v[16:31], v[52:55], v[48:51], 0\n"
    "ds_read_b128 v[52:55], v65 offset:50176\n"
    "s_add_u32 m0, s37, 155648\n"
    "s_add_u32 s0, s52, 114688\n"
    "buffer_load_dwordx4 v107, s[16:19], s0 offen lds\n"
    "v_exp_f32 v38, v38\n"
    "v_exp_f32 v39, v39\n"
    "v_exp_f32 v40, v40\n"
    "v_exp_f32 v41, v41\n"
    "s_waitcnt lgkmcnt(6)\n"
    "v_smfmac_f32_32x32x32_bf16 v[0:15], v[56:59], v[32:39], v64\n"
    "ds_read_b128 v[56:59], v66 offset:16896\n"
    "v_exp_f32 v42, v42\n"
    "v_exp_f32 v43, v43\n"
    "v_exp_f32 v44, v44\n"
    "s_add_u32 m0, s81, 65536\n"
    "s_add_u32 s0, s50, 917504\n"
    "buffer_load_dwordx4 v105, s[8:11], s0 offen lds\n"
    "v_exp_f32 v45, v45\n"
    "v_exp_f32 v46, v46\n"
    "v_exp_f32 v47, v47\n"
    "v_exp_f32 v16, v16\n"
    "v_exp_f32 v17, v17\n"
    "s_waitcnt lgkmcnt(2)\n"
    "v_smfmac_f32_32x32x32_bf16 v[0:15], v[60:63], v[40:47], v64\n"
    "ds_read_b128 v[60:63], v67 offset:16896\n"
    "v_exp_f32 v18, v18\n"
    "v_exp_f32 v19, v19\n"
    "v_exp_f32 v20, v20\n"
    "v_exp_f32 v21, v21\n"
    "s_waitcnt lgkmcnt(2)\n"
    "v_mfma_f32_32x32x16_bf16 v[32:47], v[52:55], v[48:51], 0\n"
    "ds_read_b128 v[52:55], v65 offset:51200\n"
    "s_add_u32 m0, s81, 81920\n"
    "s_add_u32 s0, s50, 1032192\n"
    "buffer_load_dwordx4 v105, s[8:11], s0 offen lds\n"
    "v_exp_f32 v22, v22\n"
    "v_exp_f32 v23, v23\n"
    "v_exp_f32 v24, v24\n"
    "v_exp_f32 v25, v25\n"
    "s_waitcnt lgkmcnt(2)\n"
    "v_smfmac_f32_32x32x32_bf16 v[0:15], v[56:59], v[16:23], v64\n"
    "ds_read_b128 v[56:59], v66 offset:17952\n"
    "v_exp_f32 v26, v26\n"
    "v_exp_f32 v27, v27\n"
    "v_exp_f32 v28, v28\n"
    "s_add_u32 m0, s81, 98304\n"
    "s_add_u32 s0, s50, 1146880\n"
    "buffer_load_dwordx4 v105, s[8:11], s0 offen lds\n"
    "v_exp_f32 v29, v29\n"
    "v_exp_f32 v30, v30\n"
    "v_exp_f32 v31, v31\n"
    "v_exp_f32 v32, v32\n"
    "v_exp_f32 v33, v33\n"
    "s_waitcnt lgkmcnt(2)\n"
    "v_smfmac_f32_32x32x32_bf16 v[0:15], v[60:63], v[24:31], v64\n"
    "ds_read_b128 v[60:63], v67 offset:17952\n"
    "v_exp_f32 v34, v34\n"
    "v_exp_f32 v35, v35\n"
    "v_exp_f32 v36, v36\n"
    "v_exp_f32 v37, v37\n"
    "s_waitcnt lgkmcnt(2)\n"
    "v_mfma_f32_32x32x16_bf16 v[16:31], v[52:55], v[48:51], 0\n"
    "ds_read_b128 v[52:55], v65 offset:52224\n"
    "v_exp_f32 v38, v38\n"
    "v_exp_f32 v39, v39\n"
    "v_exp_f32 v40, v40\n"
    "v_exp_f32 v41, v41\n"
    "s_waitcnt lgkmcnt(2)\n"
    "v_smfmac_f32_32x32x32_bf16 v[0:15], v[56:59], v[32:39], v64\n"
    "ds_read_b128 v[56:59], v66 offset:19008\n"
    "v_exp_f32 v42, v42\n"
    "v_exp_f32 v43, v43\n"
    "v_exp_f32 v44, v44\n"
    "s_add_u32 s0, s65, 98304\n"
    "buffer_load_dword v70, v106, s[12:15], s0 offen\n"
    "v_exp_f32 v45, v45\n"
    "v_exp_f32 v46, v46\n"
    "v_exp_f32 v47, v47\n"
    "v_exp_f32 v16, v16\n"
    "v_exp_f32 v17, v17\n"
    "s_waitcnt lgkmcnt(2)\n"
    "v_smfmac_f32_32x32x32_bf16 v[0:15], v[60:63], v[40:47], v64\n"
    "ds_read_b128 v[60:63], v67 offset:19008\n"
    "v_exp_f32 v18, v18\n"
    "v_exp_f32 v19, v19\n"
    "v_exp_f32 v20, v20\n"
    "v_exp_f32 v21, v21\n"
    "s_waitcnt lgkmcnt(2)\n"
    "v_mfma_f32_32x32x16_bf16 v[32:47], v[52:55], v[48:51], 0\n"
    "ds_read_b128 v[52:55], v65 offset:53248\n"
    "v_exp_f32 v22, v22\n"
    "v_exp_f32 v23, v23\n"
    "v_exp_f32 v24, v24\n"
    "v_exp_f32 v25, v25\n"
    "s_waitcnt lgkmcnt(2)\n"
    "v_smfmac_f32_32x32x32_bf16 v[0:15], v[56:59], v[16:23], v64\n"
    "ds_read_b128 v[56:59], v66 offset:20064\n"
    "v_exp_f32 v26, v26\n"
    "v_exp_f32 v27, v27\n"
    "v_exp_f32 v28, v28\n"
    "v_exp_f32 v29, v29\n"
    "v_exp_f32 v30, v30\n"
    "v_exp_f32 v31, v31\n"
    "s_add_u32 s0, s70, s41\n"
    "s_add_u32 s0, s0, 12582912\n"
    "buffer_store_dwordx4 v[72:75], v104, s[4:7], s0 offen sc1\n"
    "v_exp_f32 v32, v32\n"
    "v_exp_f32 v33, v33\n"
    "s_waitcnt lgkmcnt(2)\n"
    "v_smfmac_f32_32x32x32_bf16 v[0:15], v[60:63], v[24:31], v64\n"
    "ds_read_b128 v[60:63], v67 offset:20064\n"
    "v_exp_f32 v34, v34\n"
    "v_exp_f32 v35, v35\n"
    "v_exp_f32 v36, v36\n"
    "v_exp_f32 v37, v37\n"
    "s_waitcnt lgkmcnt(2)\n"
    "v_mfma_f32_32x32x16_bf16 v[16:31], v[52:55], v[48:51], 0\n"
    "ds_read_b128 v[52:55], v65 offset:54272\n"
    "v_exp_f32 v38, v38\n"
    "v_exp_f32 v39, v39\n"
    "v_exp_f32 v40, v40\n"
    "v_exp_f32 v41, v41\n"
    "s_waitcnt lgkmcnt(2)\n"
    "v_smfmac_f32_32x32x32_bf16 v[0:15], v[56:59], v[32:39], v64\n"
    "ds_read_b128 v[56:59], v66 offset:21120\n"
    "v_exp_f32 v42, v42\n"
    "v_exp_f32 v43, v43\n"
    "v_exp_f32 v44, v44\n"
    "v_exp_f32 v45, v45\n"
    "v_exp_f32 v46, v46\n"
    "v_exp_f32 v47, v47\n"
    "v_exp_f32 v16, v16\n"
    "v_exp_f32 v17, v17\n"
    "s_waitcnt lgkmcnt(2)\n"
    "v_smfmac_f32_32x32x32_bf16 v[0:15], v[60:63], v[40:47], v64\n"
    "ds_read_b128 v[60:63], v67 offset:21120\n"
    "v_exp_f32 v18, v18\n"
    "v_exp_f32 v19, v19\n"
    "v_exp_f32 v20, v20\n"
    "v_exp_f32 v21, v21\n"
    "s_waitcnt lgkmcnt(2)\n"
    "v_mfma_f32_32x32x16_bf16 v[32:47], v[52:55], v[48:51], 0\n"
    "ds_read_b128 v[52:55], v65 offset:55296\n"
    "v_exp_f32 v22, v22\n"
    "v_exp_f32 v23, v23\n"
    "v_exp_f32 v24, v24\n"
    "v_exp_f32 v25, v25\n"
    "s_waitcnt lgkmcnt(2)\n"
    "v_smfmac_f32_32x32x32_bf16 v[0:15], v[56:59], v[16:23], v64\n"
    "ds_read_b128 v[56:59], v66 offset:22176\n"
    "v_exp_f32 v26, v26\n"
    "v_exp_f32 v27, v27\n"
    "v_exp_f32 v28, v28\n"
    "v_exp_f32 v29, v29\n"
    "v_exp_f32 v30, v30\n"
    "v_exp_f32 v31, v31\n"
    "s_waitcnt lgkmcnt(2)\n"
    "s_barrier\n"
    "v_exp_f32 v32, v32\n"
    "v_exp_f32 v33, v33\n"
    "s_waitcnt lgkmcnt(2)\n"
    "v_smfmac_f32_32x32x32_bf16 v[0:15], v[60:63], v[24:31], v64\n"
    "ds_read_b128 v[60:63], v67 offset:22176\n"
    "v_exp_f32 v34, v34\n"
    "v_exp_f32 v35, v35\n"
    "v_exp_f32 v36, v36\n"
    "v_exp_f32 v37, v37\n"
    "s_waitcnt lgkmcnt(2)\n"
    "v_mfma_f32_32x32x16_bf16 v[16:31], v[52:55], v[48:51], 0\n"
    "ds_read_b128 v[52:55], v65 offset:56320\n"
    "s_add_u32 m0, s37, 24576\n"
    "s_add_u32 s0, s52, 49152\n"
    "buffer_load_dwordx4 v107, s[16:19], s0 offen lds\n"
    "v_exp_f32 v38, v38\n"
    "v_exp_f32 v39, v39\n"
    "v_exp_f32 v40, v40\n"
    "v_exp_f32 v41, v41\n"
    "s_waitcnt lgkmcnt(2)\n"
    "v_smfmac_f32_32x32x32_bf16 v[0:15], v[56:59], v[32:39], v64\n"
    "ds_read_b128 v[56:59], v66 offset:23232\n"
    "v_exp_f32 v42, v42\n"
    "v_exp_f32 v43, v43\n"
    "v_exp_f32 v44, v44\n"
    "s_add_u32 m0, s37, 40960\n"
    "s_add_u32 s0, s52, 81920\n"
    "buffer_load_dwordx4 v107, s[16:19], s0 offen lds\n"
    "v_exp_f32 v45, v45\n"
    "v_exp_f32 v46, v46\n"
    "v_exp_f32 v47, v47\n"
    "v_exp_f32 v16, v16\n"
    "v_exp_f32 v17, v17\n"
    "s_waitcnt lgkmcnt(2)\n"
    "v_smfmac_f32_32x32x32_bf16 v[0:15], v[60:63], v[40:47], v64\n"
    "ds_read_b128 v[60:63], v67 offset:23232\n"
    "v_exp_f32 v18, v18\n"
    "v_exp_f32 v19, v19\n"
    "v_exp_f32 v20, v20\n"
    "v_exp_f32 v21, v21\n"
    "s_waitcnt lgkmcnt(2)\n"
    "v_mfma_f32_32x32x16_bf16 v[32:47], v[52:55], v[48:51], 0\n"
    "ds_read_b128 v[52:55], v65 offset:57344\n"
    "s_add_u32 m0, s81, 90112\n"
    "s_add_u32 s0, s50, 1089536\n"
    "buffer_load_dwordx4 v105, s[8:11], s0 offen lds\n"
    "v_exp_f32 v22, v22\n"
    "v_exp_f32 v23, v23\n"
    "v_exp_f32 v24, v24\n"
    "v_exp_f32 v25, v25\n"
    "s_waitcnt lgkmcnt(2)\n"
    "v_smfmac_f32_32x32x32_bf16 v[0:15], v[56:59], v[16:23], v64\n"
    "ds_read_b128 v[56:59], v66 offset:24288\n"
    "v_exp_f32 v26, v26\n"
    "v_exp_f32 v27, v27\n"
    "v_exp_f32 v28, v28\n"
    "s_add_u32 m0, s81, 106496\n"
    "s_add_u32 s0, s50, 1204224\n"
    "buffer_load_dwordx4 v105, s[8:11], s0 offen lds\n"
    "v_exp_f32 v29, v29\n"
    "v_exp_f32 v30, v30\n"
    "v_exp_f32 v31, v31\n"
    "v_exp_f32 v32, v32\n"
    "v_exp_f32 v33, v33\n"
    "s_waitcnt lgkmcnt(2)\n"
    "v_smfmac_f32_32x32x32_bf16 v[0:15], v[60:63], v[24:31], v64\n"
    "ds_read_b128 v[60:63], v67 offset:24288\n"
    "v_exp_f32 v34, v34\n"
    "v_exp_f32 v35, v35\n"
    "v_exp_f32 v36, v36\n"
    "v_exp_f32 v37, v37\n"
    "s_waitcnt lgkmcnt(2)\n"
    "v_mfma_f32_32x32x16_bf16 v[16:31], v[52:55], v[48:51], 0\n"
    "ds_read_b128 v[52:55], v65 offset:58368\n"
    "v_exp_f32 v38, v38\n"
    "v_exp_f32 v39, v39\n"
    "v_exp_f32 v40, v40\n"
    "v_exp_f32 v41, v41\n"
    "s_waitcnt lgkmcnt(2)\n"
    "v_smfmac_f32_32x32x32_bf16 v[0:15], v[56:59], v[32:39], v64\n"
    "ds_read_b128 v[56:59], v66 offset:25344\n"
    "v_exp_f32 v42, v42\n"
    "v_exp_f32 v43, v43\n"
    "v_exp_f32 v44, v44\n"
    "v_exp_f32 v45, v45\n"
    "v_exp_f32 v46, v46\n"
    "v_exp_f32 v47, v47\n"
    "s_add_u32 s0, s70, s42\n"
    "s_add_u32 s0, s0, 12582912\n"
    "buffer_store_dwordx4 v[72:75], v104, s[4:7], s0 offen sc1\n"
    "v_exp_f32 v16, v16\n"
    "v_exp_f32 v17, v17\n"
    "s_waitcnt lgkmcnt(2)\n"
    "v_smfmac_f32_32x32x32_bf16 v[0:15], v[60:63], v[40:47], v64\n"
    "ds_read_b128 v[60:63], v67 offset:25344\n"
    "v_exp_f32 v18, v18\n"
    "v_exp_f32 v19, v19\n"
    "v_exp_f32 v20, v20\n"
    "v_exp_f32 v21, v21\n"
    "s_waitcnt lgkmcnt(2)\n"
    "v_mfma_f32_32x32x16_bf16 v[32:47], v[52:55], v[48:51], 0\n"
    "ds_read_b128 v[52:55], v65 offset:59392\n"
    "v_exp_f32 v22, v22\n"
    "v_exp_f32 v23, v23\n"
    "v_exp_f32 v24, v24\n"
    "v_exp_f32 v25, v25\n"
    "s_waitcnt lgkmcnt(2)\n"
    "v_smfmac_f32_32x32x32_bf16 v[0:15], v[56:59], v[16:23], v64\n"
    "ds_read_b128 v[56:59], v66 offset:26400\n"
    "v_exp_f32 v26, v26\n"
    "v_exp_f32 v27, v27\n"
    "v_exp_f32 v28, v28\n"
    "v_exp_f32 v29, v29\n"
    "v_exp_f32 v30, v30\n"
    "v_exp_f32 v31, v31\n"
    "v_exp_f32 v32, v32\n"
    "v_exp_f32 v33, v33\n"
    "s_waitcnt lgkmcnt(2)\n"
    "v_smfmac_f32_32x32x32_bf16 v[0:15], v[60:63], v[24:31], v64\n"
    "ds_read_b128 v[60:63], v67 offset:26400\n"
    "v_exp_f32 v34, v34\n"
    "v_exp_f32 v35, v35\n"
    "v_exp_f32 v36, v36\n"
    "v_exp_f32 v37, v37\n"
    "s_waitcnt lgkmcnt(2)\n"
    "v_mfma_f32_32x32x16_bf16 v[16:31], v[52:55], v[48:51], 0\n"
    "ds_read_b128 v[52:55], v65 offset:60416\n"
    "v_exp_f32 v38, v38\n"
    "v_exp_f32 v39, v39\n"
    "v_exp_f32 v40, v40\n"
    "v_exp_f32 v41, v41\n"
    "s_waitcnt lgkmcnt(2)\n"
    "v_smfmac_f32_32x32x32_bf16 v[0:15], v[56:59], v[32:39], v64\n"
    "ds_read_b128 v[56:59], v66 offset:27456\n"
    "v_exp_f32 v42, v42\n"
    "v_exp_f32 v43, v43\n"
    "v_exp_f32 v44, v44\n"
    "v_exp_f32 v45, v45\n"
    "v_exp_f32 v46, v46\n"
    "v_exp_f32 v47, v47\n"
    "v_exp_f32 v16, v16\n"
    "v_exp_f32 v17, v17\n"
    "s_waitcnt lgkmcnt(2)\n"
    "v_smfmac_f32_32x32x32_bf16 v[0:15], v[60:63], v[40:47], v64\n"
    "ds_read_b128 v[60:63], v67 offset:27456\n"
    "v_exp_f32 v18, v18\n"
    "v_exp_f32 v19, v19\n"
    "v_exp_f32 v20, v20\n"
    "v_exp_f32 v21, v21\n"
    "s_waitcnt lgkmcnt(2)\n"
    "v_mfma_f32_32x32x16_bf16 v[32:47], v[52:55], v[48:51], 0\n"
    "ds_read_b128 v[52:55], v65 offset:61440\n"
    "v_exp_f32 v22, v22\n"
    "v_exp_f32 v23, v23\n"
    "v_exp_f32 v24, v24\n"
    "v_exp_f32 v25, v25\n"
    "s_waitcnt lgkmcnt(2)\n"
    "v_smfmac_f32_32x32x32_bf16 v[0:15], v[56:59], v[16:23], v64\n"
    "ds_read_b128 v[56:59], v66 offset:28512\n"
    "v_exp_f32 v26, v26\n"
    "v_exp_f32 v27, v27\n"
    "v_exp_f32 v28, v28\n"
    "s_cmp_eq_u32 s36, 0\n"
    "s_cbranch_scc0 Lw_63_%=\n"
    "v_lshlrev_b32 v86, 4, v104\n"
    "s_add_u32 s0, 200704, 0\n"
    "buffer_load_dwordx2 v[78:79], v86, s[4:7], s0 offen sc1\n"
    "s_add_u32 s0, 217088, 0\n"
    "buffer_load_dwordx2 v[80:81], v86, s[4:7], s0 offen sc1\n"
    "s_add_u32 s0, 233472, 0\n"
    "buffer_load_dwordx2 v[82:83], v86, s[4:7], s0 offen sc1\n"
    "s_add_u32 s0, 249856, 0\n"
    "buffer_load_dwordx2 v[84:85], v86, s[4:7], s0 offen sc1\n"
    "Lw_63_%=:\n"
    "v_exp_f32 v29, v29\n"
    "v_exp_f32 v30, v30\n"
    "v_exp_f32 v31, v31\n"
    "v_exp_f32 v32, v32\n"
    "v_exp_f32 v33, v33\n"
    "s_waitcnt lgkmcnt(2)\n"
    "v_smfmac_f32_32x32x32_bf16 v[0:15], v[60:63], v[24:31], v64\n"
    "ds_read_b128 v[60:63], v67 offset:28512\n"
    "v_exp_f32 v34, v34\n"
    "v_exp_f32 v35, v35\n"
    "v_exp_f32 v36, v36\n"
    "v_exp_f32 v37, v37\n"
    "s_waitcnt lgkmcnt(2)\n"
    "v_mfma_f32_32x32x16_bf16 v[16:31], v[52:55], v[48:51], 0\n"
    "ds_read_b128 v[52:55], v65 offset:62464\n"
    "v_exp_f32 v38, v38\n"
    "v_exp_f32 v39, v39\n"
    "v_exp_f32 v40, v40\n"
    "v_exp_f32 v41, v41\n"
    "s_waitcnt lgkmcnt(2)\n"
    "v_smfmac_f32_32x32x32_bf16 v[0:15], v[56:59], v[32:39], v64\n"
    "ds_read_b128 v[56:59], v66 offset:29568\n"
    "v_exp_f32 v42, v42\n"
    "v_exp_f32 v43, v43\n"
    "v_exp_f32 v44, v44\n"
    "v_exp_f32 v45, v45\n"
    "v_exp_f32 v46, v46\n"
    "v_exp_f32 v47, v47\n"
    "v_exp_f32 v16, v16\n"
    "v_exp_f32 v17, v17\n"
    "s_waitcnt lgkmcnt(2)\n"
    "v_smfmac_f32_32x32x32_bf16 v[0:15], v[60:63], v[40:47], v64\n"
    "ds_read_b128 v[60:63], v67 offset:29568\n"
    "v_exp_f32 v18, v18\n"
    "v_exp_f32 v19, v19\n"
    "v_exp_f32 v20, v20\n"
    "v_exp_f32 v21, v21\n"
    "s_waitcnt lgkmcnt(2)\n"
    "v_mfma_f32_32x32x16_bf16 v[32:47], v[52:55], v[48:51], 0\n"
    "ds_read_b128 v[52:55], v65 offset:63488\n"
    "v_exp_f32 v22, v22\n"
    "v_exp_f32 v23, v23\n"
    "v_exp_f32 v24, v24\n"
    "v_exp_f32 v25, v25\n"
    "s_waitcnt lgkmcnt(2)\n"
    "v_smfmac_f32_32x32x32_bf16 v[0:15], v[56:59], v[16:23], v64\n"
    "ds_read_b128 v[56:59], v66 offset:30624\n"
    "v_exp_f32 v26, v26\n"
    "v_exp_f32 v27, v27\n"
    "v_exp_f32 v28, v28\n"
    "v_exp_f32 v29, v29\n"
    "v_exp_f32 v30, v30\n"
    "v_exp_f32 v31, v31\n"
    "v_exp_f32 v32, v32\n"
    "v_exp_f32 v33, v33\n"
    "s_waitcnt lgkmcnt(2)\n"
    "v_smfmac_f32_32x32x32_bf16 v[0:15], v[60:63], v[24:31], v64\n"
    "ds_read_b128 v[60:63], v67 offset:30624\n"
    "v_exp_f32 v34, v34\n"
    "v_exp_f32 v35, v35\n"
    "v_exp_f32 v36, v36\n"
    "v_exp_f32 v37, v37\n"
    "s_waitcnt lgkmcnt(2)\n"
    "v_mfma_f32_32x32x16_bf16 v[16:31], v[52:55], v[48:51], 0\n"
    "ds_read_b128 v[52:55], v65 offset:64512\n"
    "v_exp_f32 v38, v38\n"
    "v_exp_f32 v39, v39\n"
    "v_exp_f32 v40, v40\n"
    "v_exp_f32 v41, v41\n"
    "s_waitcnt lgkmcnt(2)\n"
    "v_smfmac_f32_32x32x32_bf16 v[0:15], v[56:59], v[32:39], v64\n"
    "ds_read_b128 v[56:59], v66 offset:31680\n"
    "v_exp_f32 v42, v42\n"
    "v_exp_f32 v43, v43\n"
    "v_exp_f32 v44, v44\n"
    "v_exp_f32 v45, v45\n"
    "v_exp_f32 v46, v46\n"
    "v_exp_f32 v47, v47\n"
    "v_exp_f32 v16, v16\n"
    "v_exp_f32 v17, v17\n"
    "s_waitcnt lgkmcnt(2)\n"
    "v_smfmac_f32_32x32x32_bf16 v[0:15], v[60:63], v[40:47], v64\n"
    "ds_read_b128 v[60:63], v67 offset:31680\n"
    "v_exp_f32 v18, v18\n"
    "v_exp_f32 v19, v19\n"
    "v_exp_f32 v20, v20\n"
    "v_exp_f32 v21, v21\n"
    "s_waitcnt lgkmcnt(2)\n"
    "v_mfma_f32_32x32x16_bf16 v[32:47], v[52:55], v[48:51], 0\n"
    "v_exp_f32 v22, v22\n"
    "v_exp_f32 v23, v23\n"
    "v_exp_f32 v24, v24\n"
    "v_exp_f32 v25, v25\n"
    "s_waitcnt lgkmcnt(1)\n"
    "v_smfmac_f32_32x32x32_bf16 v[0:15], v[56:59], v[16:23], v64\n"
    "ds_read_b128 v[56:59], v66 offset:32736\n"
    "v_exp_f32 v26, v26\n"
    "v_exp_f32 v27, v27\n"
    "v_exp_f32 v28, v28\n"
    "s_cmp_eq_u32 s36, 0\n"
    "s_cbranch_scc0 Lw_64_%=\n"
    "s_mov_b32 s96, 0\n"
    "Lz0_61_%=:\n"
    "s_waitcnt vmcnt(0)\n"
    "v_cmp_ne_u64 vcc, s[72:73], v[78:79]\n"
    "s_nop 0\n"
    "s_mov_b64 s[0:1], vcc\n"
    "v_cmp_ne_u64 vcc, s[72:73], v[80:81]\n"
    "s_nop 0\n"
    "s_or_b64 s[0:1], s[0:1], vcc\n"
    "v_cmp_ne_u64 vcc, s[72:73], v[82:83]\n"
    "s_nop 0\n"
    "s_or_b64 s[0:1], s[0:1], vcc\n"
    "v_cmp_ne_u64 vcc, s[72:73], v[84:85]\n"
    "s_nop 0\n"
    "s_or_b64 s[0:1], s[0:1], vcc\n"
    "s_cmp_eq_u64 s[0:1], 0\n"
    "s_cbranch_scc1 Lz1_62_%=\n"
    "s_add_u32 s0, 200704, 0\n"
    "buffer_load_dwordx2 v[78:79], v86, s[4:7], s0 offen sc1\n"
    "s_add_u32 s0, 217088, 0\n"
    "buffer_load_dwordx2 v[80:81], v86, s[4:7], s0 offen sc1\n"
    "s_add_u32 s0, 233472, 0\n"
    "buffer_load_dwordx2 v[82:83], v86, s[4:7], s0 offen sc1\n"
    "s_add_u32 s0, 249856, 0\n"
    "buffer_load_dwordx2 v[84:85], v86, s[4:7], s0 offen sc1\n"
    "s_add_u32 s96, s96, 1\n"
    "s_cmp_lt_u32 s96, 65536\n"
    "s_cbranch_scc1 Lz0_61_%=\n"
    "Lz1_62_%=:\n"
    "Lw_64_%=:\n"
    "v_exp_f32 v29, v29\n"
    "v_exp_f32 v30, v30\n"
    "v_exp_f32 v31, v31\n"
    "v_exp_f32 v32, v32\n"
    "v_exp_f32 v33, v33\n"
    "s_waitcnt lgkmcnt(1)\n"
    "v_smfmac_f32_32x32x32_bf16 v[0:15], v[60:63], v[24:31], v64\n"
    "ds_read_b128 v[60:63], v67 offset:32736\n"
    "v_exp_f32 v34, v34\n"
    "v_exp_f32 v35, v35\n"
    "v_exp_f32 v36, v36\n"
    "v_exp_f32 v37, v37\n"
    "v_exp_f32 v38, v38\n"
    "v_exp_f32 v39, v39\n"
    "v_exp_f32 v40, v40\n"
    "v_exp_f32 v41, v41\n"
    "s_waitcnt lgkmcnt(1)\n"
    "v_smfmac_f32_32x32x32_bf16 v[0:15], v[56:59], v[32:39], v64\n"
    "v_exp_f32 v42, v42\n"
    "v_exp_f32 v43, v43\n"
    "v_exp_f32 v44, v44\n"
    "v_exp_f32 v45, v45\n"
    "v_exp_f32 v46, v46\n"
    "v_exp_f32 v47, v47\n"
    "s_branch Late_66_%=\n"
    "Lat1_65_%=:\n"
    "ds_read_b128 v[52:55], v65\n"
    "buffer_load_dwordx2 v[72:73], v71, s[4:7], s84 offen sc1\n"
    "s_waitcnt lgkmcnt(0)\n"
    "v_mfma_f32_32x32x16_bf16 v[16:31], v[52:55], v[48:51], 0\n"
    "ds_read_b128 v[52:55], v65 offset:1024\n"
    "ds_read_b128 v[56:59], v66 offset:0\n"
    "s_nop 9\n"
    "v_exp_f32 v16, v16\n"
    "v_exp_f32 v17, v17\n"
    "ds_read_b128 v[60:63], v67 offset:0\n"
    "v_exp_f32 v18, v18\n"
    "v_exp_f32 v19, v19\n"
    "v_exp_f32 v20, v20\n"
    "v_exp_f32 v21, v21\n"
    "s_waitcnt lgkmcnt(2)\n"
    "v_mfma_f32_32x32x16_bf16 v[32:47], v[52:55], v[48:51], 0\n"
    "ds_read_b128 v[52:55], v65 offset:2048\n"
    "v_exp_f32 v22, v22\n"
    "v_exp_f32 v23, v23\n"
    "v_exp_f32 v24, v24\n"
    "v_exp_f32 v25, v25\n"
    "s_waitcnt lgkmcnt(2)\n"
    "v_smfmac_f32_32x32x32_bf16 v[0:15], v[56:59], v[16:23], v64\n"
    "ds_read_b128 v[56:59], v66 offset:1056\n"
    "v_exp_f32 v26, v26\n"
    "v_exp_f32 v27, v27\n"
    "v_exp_f32 v28, v28\n"
    "v_exp_f32 v29, v29\n"
    "v_exp_f32 v30, v30\n"
    "v_exp_f32 v31, v31\n"
    "v_exp_f32 v32, v32\n"
    "v_exp_f32 v33, v33\n"
    "s_waitcnt lgkmcnt(2)\n"
    "v_smfmac_f32_32x32x32_bf16 v[0:15], v[60:63], v[24:31], v64\n"
    "ds_read_b128 v[60:63], v67 offset:1056\n"
    "v_exp_f32 v34, v34\n"
    "v_exp_f32 v35, v35\n"
    "v_exp_f32 v36, v36\n"
    "v_exp_f32 v37, v37\n"
    "s_waitcnt lgkmcnt(2)\n"
    "v_mfma_f32_32x32x16_bf16 v[16:31], v[52:55], v[48:51], 0\n"
    "ds_read_b128 v[52:55], v65 offset:3072\n"
    "v_exp_f32 v38, v38\n"
    "v_exp_f32 v39, v39\n"
    "v_exp_f32 v40, v40\n"
    "v_exp_f32 v41, v41\n"
    "s_waitcnt lgkmcnt(2)\n"
    "v_smfmac_f32_32x32x32_bf16 v[0:15], v[56:59], v[32:39], v64\n"
    "ds_read_b128 v[56:59], v66 offset:2112\n"
    "v_exp_f32 v42, v42\n"
    "v_exp_f32 v43, v43\n"
    "v_exp_f32 v44, v44\n"
    "v_exp_f32 v45, v45\n"
    "v_exp_f32 v46, v46\n"
    "v_exp_f32 v47, v47\n"
    "v_exp_f32 v16, v16\n"
    "v_exp_f32 v17, v17\n"
    "s_waitcnt lgkmcnt(2)\n"
    "v_smfmac_f32_32x32x32_bf16 v[0:15], v[60:63], v[40:47], v64\n"
    "ds_read_b128 v[60:63], v67 offset:2112\n"
    "v_exp_f32 v18, v18\n"
    "v_exp_f32 v19, v19\n"
    "v_exp_f32 v20, v20\n"
    "v_exp_f32 v21, v21\n"
    "s_waitcnt lgkmcnt(2)\n"
    "v_mfma_f32_32x32x16_bf16 v[32:47], v[52:55], v[48:51], 0\n"
    "ds_read_b128 v[52:55], v65 offset:4096\n"
    "v_exp_f32 v22, v22\n"
    "v_exp_f32 v23, v23\n"
    "v_exp_f32 v24, v24\n"
    "v_exp_f32 v25, v25\n"
    "s_waitcnt lgkmcnt(2)\n"
    "v_smfmac_f32_32x32x32_bf16 v[0:15], v[56:59], v[16:23], v64\n"
    "ds_read_b128 v[56:59], v66 offset:3168\n"
    "v_exp_f32 v26, v26\n"
    "v_exp_f32 v27, v27\n"
    "v_exp_f32 v28, v28\n"
    "v_exp_f32 v29, v29\n"
    "v_exp_f32 v30, v30\n"
    "v_exp_f32 v31, v31\n"
    "v_exp_f32 v32, v32\n"
    "v_exp_f32 v33, v33\n"
    "s_waitcnt lgkmcnt(2)\n"
    "v_smfmac_f32_32x32x32_bf16 v[0:15], v[60:63], v[24:31], v64\n"
    "ds_read_b128 v[60:63], v67 offset:3168\n"
    "v_exp_f32 v34, v34\n"
    "v_exp_f32 v35, v35\n"
    "v_exp_f32 v36, v36\n"
    "v_exp_f32 v37, v37\n"
    "s_waitcnt lgkmcnt(2)\n"
    "v_mfma_f32_32x32x16_bf16 v[16:31], v[52:55], v[48:51], 0\n"
    "ds_read_b128 v[52:55], v65 offset:5120\n"
    "v_exp_f32 v38, v38\n"
    "v_exp_f32 v39, v39\n"
    "v_exp_f32 v40, v40\n"
    "v_exp_f32 v41, v41\n"
    "s_waitcnt lgkmcnt(2)\n"
    "v_smfmac_f32_32x32x32_bf16 v[0:15], v[56:59], v[32:39], v64\n"
    "ds_read_b128 v[56:59], v66 offset:4224\n"
    "v_exp_f32 v42, v42\n"
    "v_exp_f32 v43, v43\n"
    "v_exp_f32 v44, v44\n"
    "v_exp_f32 v45, v45\n"
    "v_exp_f32 v46, v46\n"
    "v_exp_f32 v47, v47\n"
    "v_exp_f32 v16, v16\n"
    "v_exp_f32 v17, v17\n"
    "s_waitcnt lgkmcnt(2)\n"
    "v_smfmac_f32_32x32x32_bf16 v[0:15], v[60:63], v[40:47], v64\n"
    "ds_read_b128 v[60:63], v67 offset:4224\n"
    "v_exp_f32 v18, v18\n"
    "v_exp_f32 v19, v19\n"
    "v_exp_f32 v20, v20\n"
    "v_exp_f32 v21, v21\n"
    "s_waitcnt lgkmcnt(2)\n"
    "v_mfma_f32_32x32x16_bf16 v[32:47], v[52:55], v[48:51], 0\n"
    "ds_read_b128 v[52:55], v65 offset:6144\n"
    "v_exp_f32 v22, v22\n"
    "v_exp_f32 v23, v23\n"
    "v_exp_f32 v24, v24\n"
    "v_exp_f32 v25, v25\n"
    "s_waitcnt lgkmcnt(2)\n"
    "v_smfmac_f32_32x32x32_bf16 v[0:15], v[56:59], v[16:23], v64\n"
    "ds_read_b128 v[56:59], v66 offset:5280\n"
    "v_exp_f32 v26, v26\n"
    "v_exp_f32 v27, v27\n"
    "v_exp_f32 v28, v28\n"
    "v_exp_f32 v29, v29\n"
    "v_exp_f32 v30, v30\n"
    "v_exp_f32 v31, v31\n"
    "s_waitcnt vmcnt(0)\n"
    "Lrt_67_%=:\n"
    "v_cmp_ne_u64 vcc, s[72:73], v[72:73]\n"
    "s_cbranch_vccz Lrk_68_%=\n"
    "buffer_load_dwordx2 v[72:73], v71, s[4:7], s84 offen sc1\n"
    "s_add_u32 s96, s96, 1\n"
    "s_cmp_lt_u32 s96, 65536\n"
    "s_waitcnt vmcnt(0)\n"
    "s_cbranch_scc1 Lrt_67_%=\n"
    "Lrk_68_%=:\n"
    "s_add_u32 m0, s86, 16384\n"
    "s_mov_b32 s0, 2113536\n"
    "buffer_load_dwordx4 v89, s[4:7], s0 offen sc1 lds\n"
    "s_add_u32 m0, s86, 16384\n"
    "s_mov_b32 s0, 2113536\n"
    "buffer_load_dwordx4 v89, s[4:7], s0 offen offset:1024 sc1 lds\n"
    "s_add_u32 m0, s85, 16896\n"
    "s_mov_b32 s0, 4210688\n"
    "buffer_load_dwordx4 v89, s[4:7], s0 offen sc1 lds\n"
    "s_add_u32 m0, s85, 17952\n"
    "s_mov_b32 s0, 4211712\n"
    "buffer_load_dwordx4 v89, s[4:7], s0 offen sc1 lds\n"
    "v_exp_f32 v32, v32\n"
    "v_exp_f32 v33, v33\n"
    "s_waitcnt lgkmcnt(2)\n"
    "v_smfmac_f32_32x32x32_bf16 v[0:15], v[60:63], v[24:31], v64\n"
    "ds_read_b128 v[60:63], v67 offset:5280\n"
    "v_exp_f32 v34, v34\n"
    "v_exp_f32 v35, v35\n"
    "v_exp_f32 v36, v36\n"
    "v_exp_f32 v37, v37\n"
    "s_waitcnt lgkmcnt(2)\n"
    "v_mfma_f32_32x32x16_bf16 v[16:31], v[52:55], v[48:51], 0\n"
    "ds_read_b128 v[52:55], v65 offset:7168\n"
    "v_exp_f32 v38, v38\n"
    "v_exp_f32 v39, v39\n"
    "v_exp_f32 v40, v40\n"
    "v_exp_f32 v41, v41\n"
    "s_waitcnt lgkmcnt(2)\n"
    "v_smfmac_f32_32x32x32_bf16 v[0:15], v[56:59], v[32:39], v64\n"
    "ds_read_b128 v[56:59], v66 offset:6336\n"
    "v_exp_f32 v42, v42\n"
    "v_exp_f32 v43, v43\n"
    "v_exp_f32 v44, v44\n"
    "v_exp_f32 v45, v45\n"
    "v_exp_f32 v46, v46\n"
    "v_exp_f32 v47, v47\n"
    "v_exp_f32 v16, v16\n"
    "v_exp_f32 v17, v17\n"
    "s_waitcnt lgkmcnt(2)\n"
    "v_smfmac_f32_32x32x32_bf16 v[0:15], v[60:63], v[40:47], v64\n"
    "ds_read_b128 v[60:63], v67 offset:6336\n"
    "v_exp_f32 v18, v18\n"
    "v_exp_f32 v19, v19\n"
    "v_exp_f32 v20, v20\n"
    "v_exp_f32 v21, v21\n"
    "s_waitcnt lgkmcnt(2)\n"
    "v_mfma_f32_32x32x16_bf16 v[32:47], v[52:55], v[48:51], 0\n"
    "ds_read_b128 v[52:55], v65 offset:8192\n"
    "v_exp_f32 v22, v22\n"
    "v_exp_f32 v23, v23\n"
    "v_exp_f32 v24, v24\n"
    "v_exp_f32 v25, v25\n"
    "s_waitcnt lgkmcnt(2)\n"
    "v_smfmac_f32_32x32x32_bf16 v[0:15], v[56:59], v[16:23], v64\n"
    "ds_read_b128 v[56:59], v66 offset:7392\n"
    "v_exp_f32 v26, v26\n"
    "v_exp_f32 v27, v27\n"
    "v_exp_f32 v28, v28\n"
    "v_exp_f32 v29, v29\n"
    "v_exp_f32 v30, v30\n"
    "v_exp_f32 v31, v31\n"
    "v_exp_f32 v32, v32\n"
    "v_exp_f32 v33, v33\n"
    "s_waitcnt lgkmcnt(2)\n"
    "v_smfmac_f32_32x32x32_bf16 v[0:15], v[60:63], v[24:31], v64\n"
    "ds_read_b128 v[60:63], v67 offset:7392\n"
    "v_exp_f32 v34, v34\n"
    "v_exp_f32 v35, v35\n"
    "v_exp_f32 v36, v36\n"
    "v_exp_f32 v37, v37\n"
    "s_waitcnt lgkmcnt(2)\n"
    "v_mfma_f32_32x32x16_bf16 v[16:31], v[52:55], v[48:51], 0\n"
    "ds_read_b128 v[52:55], v65 offset:9216\n"
    "v_exp_f32 v38, v38\n"
    "v_exp_f32 v39, v39\n"
    "v_exp_f32 v40, v40\n"
    "v_exp_f32 v41, v41\n"
    "s_waitcnt lgkmcnt(2)\n"
    "v_smfmac_f32_32x32x32_bf16 v[0:15], v[56:59], v[32:39], v64\n"
    "ds_read_b128 v[56:59], v66 offset:8448\n"
    "v_exp_f32 v42, v42\n"
    "v_exp_f32 v43, v43\n"
    "v_exp_f32 v44, v44\n"
    "v_exp_f32 v45, v45\n"
    "v_exp_f32 v46, v46\n"
    "v_exp_f32 v47, v47\n"
    "v_exp_f32 v16, v16\n"
    "v_exp_f32 v17, v17\n"
    "s_waitcnt lgkmcnt(2)\n"
    "v_smfmac_f32_32x32x32_bf16 v[0:15], v[60:63], v[40:47], v64\n"
    "ds_read_b128 v[60:63], v67 offset:8448\n"
    "v_exp_f32 v18, v18\n"
    "v_exp_f32 v19, v19\n"
    "v_exp_f32 v20, v20\n"
    "v_exp_f32 v21, v21\n"
    "s_waitcnt lgkmcnt(2)\n"
    "v_mfma_f32_32x32x16_bf16 v[32:47], v[52:55], v[48:51], 0\n"
    "ds_read_b128 v[52:55], v65 offset:10240\n"
    "v_exp_f32 v22, v22\n"
    "v_exp_f32 v23, v23\n"
    "v_exp_f32 v24, v24\n"
    "v_exp_f32 v25, v25\n"
    "s_waitcnt lgkmcnt(2)\n"
    "v_smfmac_f32_32x32x32_bf16 v[0:15], v[56:59], v[16:23], v64\n"
    "ds_read_b128 v[56:59], v66 offset:9504\n"
    "v_exp_f32 v26, v26\n"
    "v_exp_f32 v27, v27\n"
    "v_exp_f32 v28, v28\n"
    "v_exp_f32 v29, v29\n"
    "v_exp_f32 v30, v30\n"
    "v_exp_f32 v31, v31\n"
    "v_exp_f32 v32, v32\n"
    "v_exp_f32 v33, v33\n"
    "s_waitcnt lgkmcnt(2)\n"
    "v_smfmac_f32_32x32x32_bf16 v[0:15], v[60:63], v[24:31], v64\n"
    "ds_read_b128 v[60:63], v67 offset:9504\n"
    "v_exp_f32 v34, v34\n"
    "v_exp_f32 v35, v35\n"
    "v_exp_f32 v36, v36\n"
    "v_exp_f32 v37, v37\n"
    "s_waitcnt lgkmcnt(2)\n"
    "v_mfma_f32_32x32x16_bf16 v[16:31], v[52:55], v[48:51], 0\n"
    "ds_read_b128 v[52:55], v65 offset:11264\n"
    "v_exp_f32 v38, v38\n"
    "v_exp_f32 v39, v39\n"
    "v_exp_f32 v40, v40\n"
    "v_exp_f32 v41, v41\n"
    "s_waitcnt lgkmcnt(2)\n"
    "v_smfmac_f32_32x32x32_bf16 v[0:15], v[56:59], v[32:39], v64\n"
    "ds_read_b128 v[56:59], v66 offset:10560\n"
    "v_exp_f32 v42, v42\n"
    "v_exp_f32 v43, v43\n"
    "v_exp_f32 v44, v44\n"
    "v_exp_f32 v45, v45\n"
    "v_exp_f32 v46, v46\n"
    "v_exp_f32 v47, v47\n"
    "v_exp_f32 v16, v16\n"
    "v_exp_f32 v17, v17\n"
    "s_waitcnt lgkmcnt(2)\n"
    "v_smfmac_f32_32x32x32_bf16 v[0:15], v[60:63], v[40:47], v64\n"
    "ds_read_b128 v[60:63], v67 offset:10560\n"
    "v_exp_f32 v18, v18\n"
    "v_exp_f32 v19, v19\n"
    "v_exp_f32 v20, v20\n"
    "v_exp_f32 v21, v21\n"
    "s_waitcnt lgkmcnt(2)\n"
    "v_mfma_f32_32x32x16_bf16 v[32:47], v[52:55], v[48:51], 0\n"
    "ds_read_b128 v[52:55], v65 offset:12288\n"
    "v_exp_f32 v22, v22\n"
    "v_exp_f32 v23, v23\n"
    "v_exp_f32 v24, v24\n"
    "v_exp_f32 v25, v25\n"
    "s_waitcnt lgkmcnt(2)\n"
    "v_smfmac_f32_32x32x32_bf16 v[0:15], v[56:59], v[16:23], v64\n"
    "ds_read_b128 v[56:59], v66 offset:11616\n"
    "v_exp_f32 v26, v26\n"
    "v_exp_f32 v27, v27\n"
    "v_exp_f32 v28, v28\n"
    "v_exp_f32 v29, v29\n"
    "v_exp_f32 v30, v30\n"
    "v_exp_f32 v31, v31\n"
    "v_exp_f32 v32, v32\n"
    "v_exp_f32 v33, v33\n"
    "s_waitcnt lgkmcnt(2)\n"
    "v_smfmac_f32_32x32x32_bf16 v[0:15], v[60:63], v[24:31], v64\n"
    "ds_read_b128 v[60:63], v67 offset:11616\n"
    "v_exp_f32 v34, v34\n"
    "v_exp_f32 v35, v35\n"
    "v_exp_f32 v36, v36\n"
    "v_exp_f32 v37, v37\n"
    "s_waitcnt lgkmcnt(2)\n"
    "v_mfma_f32_32x32x16_bf16 v[16:31], v[52:55], v[48:51], 0\n"
    "ds_read_b128 v[52:55], v65 offset:13312\n"
    "v_exp_f32 v38, v38\n"
    "v_exp_f32 v39, v39\n"
    "v_exp_f32 v40, v40\n"
    "v_exp_f32 v41, v41\n"
    "s_waitcnt lgkmcnt(2)\n"
    "v_smfmac_f32_32x32x32_bf16 v[0:15], v[56:59], v[32:39], v64\n"
    "ds_read_b128 v[56:59], v66 offset:12672\n"
    "v_exp_f32 v42, v42\n"
    "v_exp_f32 v43, v43\n"
    "v_exp_f32 v44, v44\n"
    "v_exp_f32 v45, v45\n"
    "v_exp_f32 v46, v46\n"
    "v_exp_f32 v47, v47\n"
    "v_exp_f32 v16, v16\n"
    "v_exp_f32 v17, v17\n"
    "s_waitcnt lgkmcnt(2)\n"
    "v_smfmac_f32_32x32x32_bf16 v[0:15], v[60:63], v[40:47], v64\n"
    "ds_read_b128 v[60:63], v67 offset:12672\n"
    "v_exp_f32 v18, v18\n"
    "v_exp_f32 v19, v19\n"
    "v_exp_f32 v20, v20\n"
    "v_exp_f32 v21, v21\n"
    "s_waitcnt lgkmcnt(2)\n"
    "v_mfma_f32_32x32x16_bf16 v[32:47], v[52:55], v[48:51], 0\n"
    "ds_read_b128 v[52:55], v65 offset:14336\n"
    "v_exp_f32 v22, v22\n"
    "v_exp_f32 v23, v23\n"
    "v_exp_f32 v24, v24\n"
    "v_exp_f32 v25, v25\n"
    "s_waitcnt lgkmcnt(2)\n"
    "v_smfmac_f32_32x32x32_bf16 v[0:15], v[56:59], v[16:23], v64\n"
    "ds_read_b128 v[56:59], v66 offset:13728\n"
    "v_exp_f32 v26, v26\n"
    "v_exp_f32 v27, v27\n"
    "v_exp_f32 v28, v28\n"
    "v_exp_f32 v29, v29\n"
    "v_exp_f32 v30, v30\n"
    "v_exp_f32 v31, v31\n"
    "s_waitcnt vmcnt(0) lgkmcnt(2)\n"
    "s_barrier\n"
    "v_exp_f32 v32, v32\n"
    "v_exp_f32 v33, v33\n"
    "s_waitcnt lgkmcnt(2)\n"
    "v_smfmac_f32_32x32x32_bf16 v[0:15], v[60:63], v[24:31], v64\n"
    "ds_read_b128 v[60:63], v67 offset:13728\n"
    "v_exp_f32 v34, v34\n"
    "v_exp_f32 v35, v35\n"
    "v_exp_f32 v36, v36\n"
    "v_exp_f32 v37, v37\n"
    "s_waitcnt lgkmcnt(2)\n"
    "v_mfma_f32_32x32x16_bf16 v[16:31], v[52:55], v[48:51], 0\n"
    "ds_read_b128 v[52:55], v65 offset:15360\n"
    "s_add_u32 m0, s86, 32768\n"
    "s_mov_b32 s0, 2129920\n"
    "buffer_load_dwordx4 v89, s[4:7], s0 offen sc1 lds\n"
    "v_exp_f32 v38, v38\n"
    "v_exp_f32 v39, v39\n"
    "v_exp_f32 v40, v40\n"
    "v_exp_f32 v41, v41\n"
    "s_waitcnt lgkmcnt(2)\n"
    "v_smfmac_f32_32x32x32_bf16 v[0:15], v[56:59], v[32:39], v64\n"
    "ds_read_b128 v[56:59], v66 offset:14784\n"
    "v_exp_f32 v42, v42\n"
    "v_exp_f32 v43, v43\n"
    "v_exp_f32 v44, v44\n"
    "s_add_u32 m0, s86, 32768\n"
    "s_mov_b32 s0, 2129920\n"
    "buffer_load_dwordx4 v89, s[4:7], s0 offen offset:1024 sc1 lds\n"
    "v_exp_f32 v45, v45\n"
    "v_exp_f32 v46, v46\n"
    "v_exp_f32 v47, v47\n"
    "v_exp_f32 v16, v16\n"
    "v_exp_f32 v17, v17\n"
    "s_waitcnt lgkmcnt(2)\n"
    "v_smfmac_f32_32x32x32_bf16 v[0:15], v[60:63], v[40:47], v64\n"
    "ds_read_b128 v[60:63], v67 offset:14784\n"
    "v_exp_f32 v18, v18\n"
    "v_exp_f32 v19, v19\n"
    "v_exp_f32 v20, v20\n"
    "v_exp_f32 v21, v21\n"
    "s_waitcnt lgkmcnt(2)\n"
    "v_mfma_f32_32x32x16_bf16 v[32:47], v[52:55], v[48:51], 0\n"
    "ds_read_b128 v[52:55], v65 offset:16384\n"
    "s_add_u32 m0, s85, 33792\n"
    "s_mov_b32 s0, 4227072\n"
    "buffer_load_dwordx4 v89, s[4:7], s0 offen sc1 lds\n"
    "v_exp_f32 v22, v22\n"
    "v_exp_f32 v23, v23\n"
    "v_exp_f32 v24, v24\n"
    "v_exp_f32 v25, v25\n"
    "s_waitcnt lgkmcnt(2)\n"
    "v_smfmac_f32_32x32x32_bf16 v[0:15], v[56:59], v[16:23], v64\n"
    "ds_read_b128 v[56:59], v66 offset:15840\n"
    "v_exp_f32 v26, v26\n"
    "v_exp_f32 v27, v27\n"
    "v_exp_f32 v28, v28\n"
    "s_add_u32 m0, s85, 34848\n"
    "s_mov_b32 s0, 4228096\n"
    "buffer_load_dwordx4 v89, s[4:7], s0 offen sc1 lds\n"
    "v_exp_f32 v29, v29\n"
    "v_exp_f32 v30, v30\n"
    "v_exp_f32 v31, v31\n"
    "v_exp_f32 v32, v32\n"
    "v_exp_f32 v33, v33\n"
    "s_waitcnt lgkmcnt(2)\n"
    "v_smfmac_f32_32x32x32_bf16 v[0:15], v[60:63], v[24:31], v64\n"
    "ds_read_b128 v[60:63], v67 offset:15840\n"
    "v_exp_f32 v34, v34\n"
    "v_exp_f32 v35, v35\n"
    "v_exp_f32 v36, v36\n"
    "v_exp_f32 v37, v37\n"
    "s_waitcnt lgkmcnt(2)\n"
    "v_mfma_f32_32x32x16_bf16 v[16:31], v[52:55], v[48:51], 0\n"
    "ds_read_b128 v[52:55], v65 offset:17408\n"
    "s_add_u32 m0, s81, 139264\n"
    "s_add_u32 s0, s50, 0\n"
    "buffer_load_dwordx4 v105, s[8:11], s0 offen lds\n"
    "v_exp_f32 v38, v38\n"
    "v_exp_f32 v39, v39\n"
    "v_exp_f32 v40, v40\n"
    "v_exp_f32 v41, v41\n"
    "s_waitcnt lgkmcnt(2)\n"
    "v_smfmac_f32_32x32x32_bf16 v[0:15], v[56:59], v[32:39], v64\n"
    "ds_read_b128 v[56:59], v66 offset:16896\n"
    "v_exp_f32 v42, v42\n"
    "v_exp_f32 v43, v43\n"
    "v_exp_f32 v44, v44\n"
    "s_add_u32 m0, s81, 147456\n"
    "s_add_u32 s0, s50, 57344\n"
    "buffer_load_dwordx4 v105, s[8:11], s0 offen lds\n"
    "v_exp_f32 v45, v45\n"
    "v_exp_f32 v46, v46\n"
    "v_exp_f32 v47, v47\n"
    "v_exp_f32 v16, v16\n"
    "v_exp_f32 v17, v17\n"
    "s_waitcnt lgkmcnt(2)\n"
    "v_smfmac_f32_32x32x32_bf16 v[0:15], v[60:63], v[40:47], v64\n"
    "ds_read_b128 v[60:63], v67 offset:16896\n"
    "v_exp_f32 v18, v18\n"
    "v_exp_f32 v19, v19\n"
    "v_exp_f32 v20, v20\n"
    "v_exp_f32 v21, v21\n"
    "s_waitcnt lgkmcnt(2)\n"
    "v_mfma_f32_32x32x16_bf16 v[32:47], v[52:55], v[48:51], 0\n"
    "ds_read_b128 v[52:55], v65 offset:18432\n"
    "s_add_u32 m0, s81, 155648\n"
    "s_add_u32 s0, s50, 114688\n"
    "buffer_load_dwordx4 v105, s[8:11], s0 offen lds\n"
    "v_exp_f32 v22, v22\n"
    "v_exp_f32 v23, v23\n"
    "v_exp_f32 v24, v24\n"
    "v_exp_f32 v25, v25\n"
    "s_waitcnt lgkmcnt(2)\n"
    "v_smfmac_f32_32x32x32_bf16 v[0:15], v[56:59], v[16:23], v64\n"
    "ds_read_b128 v[56:59], v66 offset:17952\n"
    "v_exp_f32 v26, v26\n"
    "v_exp_f32 v27, v27\n"
    "v_exp_f32 v28, v28\n"
    "s_add_u32 m0, s81, 0\n"
    "s_add_u32 s0, s50, 172032\n"
    "buffer_load_dwordx4 v105, s[8:11], s0 offen lds\n"
    "v_exp_f32 v29, v29\n"
    "v_exp_f32 v30, v30\n"
    "v_exp_f32 v31, v31\n"
    "v_exp_f32 v32, v32\n"
    "v_exp_f32 v33, v33\n"
    "s_waitcnt lgkmcnt(2)\n"
    "v_smfmac_f32_32x32x32_bf16 v[0:15], v[60:63], v[24:31], v64\n"
    "ds_read_b128 v[60:63], v67 offset:17952\n"
    "v_exp_f32 v34, v34\n"
    "v_exp_f32 v35, v35\n"
    "v_exp_f32 v36, v36\n"
    "v_exp_f32 v37, v37\n"
    "s_waitcnt lgkmcnt(2)\n"
    "v_mfma_f32_32x32x16_bf16 v[16:31], v[52:55], v[48:51], 0\n"
    "ds_read_b128 v[52:55], v65 offset:19456\n"
    "v_exp_f32 v38, v38\n"
    "v_exp_f32 v39, v39\n"
    "v_exp_f32 v40, v40\n"
    "v_exp_f32 v41, v41\n"
    "s_waitcnt lgkmcnt(2)\n"
    "v_smfmac_f32_32x32x32_bf16 v[0:15], v[56:59], v[32:39], v64\n"
    "ds_read_b128 v[56:59], v66 offset:19008\n"
    "v_exp_f32 v42, v42\n"
    "v_exp_f32 v43, v43\n"
    "v_exp_f32 v44, v44\n"
    "s_add_u32 m0, s81, 65536\n"
    "s_add_u32 s0, s50, 229376\n"
    "buffer_load_dwordx4 v105, s[8:11], s0 offen lds\n"
    "v_exp_f32 v45, v45\n"
    "v_exp_f32 v46, v46\n"
    "v_exp_f32 v47, v47\n"
    "v_exp_f32 v16, v16\n"
    "v_exp_f32 v17, v17\n"
    "s_waitcnt lgkmcnt(2)\n"
    "v_smfmac_f32_32x32x32_bf16 v[0:15], v[60:63], v[40:47], v64\n"
    "ds_read_b128 v[60:63], v67 offset:19008\n"
    "v_exp_f32 v18, v18\n"
    "v_exp_f32 v19, v19\n"
    "v_exp_f32 v20, v20\n"
    "v_exp_f32 v21, v21\n"
    "s_waitcnt lgkmcnt(2)\n"
    "v_mfma_f32_32x32x16_bf16 v[32:47], v[52:55], v[48:51], 0\n"
    "ds_read_b128 v[52:55], v65 offset:20480\n"
    "v_exp_f32 v22, v22\n"
    "v_exp_f32 v23, v23\n"
    "v_exp_f32 v24, v24\n"
    "v_exp_f32 v25, v25\n"
    "s_waitcnt lgkmcnt(2)\n"
    "v_smfmac_f32_32x32x32_bf16 v[0:15], v[56:59], v[16:23], v64\n"
    "ds_read_b128 v[56:59], v66 offset:20064\n"
    "v_exp_f32 v26, v26\n"
    "v_exp_f32 v27, v27\n"
    "v_exp_f32 v28, v28\n"
    "v_exp_f32 v29, v29\n"
    "v_exp_f32 v30, v30\n"
    "v_exp_f32 v31, v31\n"
    "v_exp_f32 v32, v32\n"
    "v_exp_f32 v33, v33\n"
    "s_waitcnt lgkmcnt(2)\n"
    "v_smfmac_f32_32x32x32_bf16 v[0:15], v[60:63], v[24:31], v64\n"
    "ds_read_b128 v[60:63], v67 offset:20064\n"
    "v_exp_f32 v34, v34\n"
    "v_exp_f32 v35, v35\n"
    "v_exp_f32 v36, v36\n"
    "v_exp_f32 v37, v37\n"
    "s_waitcnt lgkmcnt(2)\n"
    "v_mfma_f32_32x32x16_bf16 v[16:31], v[52:55], v[48:51], 0\n"
    "ds_read_b128 v[52:55], v65 offset:21504\n"
    "v_exp_f32 v38, v38\n"
    "v_exp_f32 v39, v39\n"
    "v_exp_f32 v40, v40\n"
    "v_exp_f32 v41, v41\n"
    "s_waitcnt lgkmcnt(2)\n"
    "v_smfmac_f32_32x32x32_bf16 v[0:15], v[56:59], v[32:39], v64\n"
    "ds_read_b128 v[56:59], v66 offset:21120\n"
    "v_exp_f32 v42, v42\n"
    "v_exp_f32 v43, v43\n"
    "v_exp_f32 v44, v44\n"
    "v_exp_f32 v45, v45\n"
    "v_exp_f32 v46, v46\n"
    "v_exp_f32 v47, v47\n"
    "v_exp_f32 v16, v16\n"
    "v_exp_f32 v17, v17\n"
    "s_waitcnt lgkmcnt(2)\n"
    "v_smfmac_f32_32x32x32_bf16 v[0:15], v[60:63], v[40:47], v64\n"
    "ds_read_b128 v[60:63], v67 offset:21120\n"
    "v_exp_f32 v18, v18\n"
    "v_exp_f32 v19, v19\n"
    "v_exp_f32 v20, v20\n"
    "v_exp_f32 v21, v21\n"
    "s_waitcnt lgkmcnt(2)\n"
    "v_mfma_f32_32x32x16_bf16 v[32:47], v[52:55], v[48:51], 0\n"
    "ds_read_b128 v[52:55], v65 offset:22528\n"
    "v_exp_f32 v22, v22\n"
    "v_exp_f32 v23, v23\n"
    "v_exp_f32 v24, v24\n"
    "v_exp_f32 v25, v25\n"
    "s_waitcnt lgkmcnt(2)\n"
    "v_smfmac_f32_32x32x32_bf16 v[0:15], v[56:59], v[16:23], v64\n"
    "ds_read_b128 v[56:59], v66 offset:22176\n"
    "v_exp_f32 v26, v26\n"
    "v_exp_f32 v27, v27\n"
    "v_exp_f32 v28, v28\n"
    "v_exp_f32 v29, v29\n"
    "v_exp_f32 v30, v30\n"
    "v_exp_f32 v31, v31\n"
    "s_waitcnt vmcnt(0) lgkmcnt(2)\n"
    "s_barrier\n"
    "v_exp_f32 v32, v32\n"
    "v_exp_f32 v33, v33\n"
    "s_waitcnt lgkmcnt(2)\n"
    "v_smfmac_f32_32x32x32_bf16 v[0:15], v[60:63], v[24:31], v64\n"
    "ds_read_b128 v[60:63], v67 offset:22176\n"
    "v_exp_f32 v34, v34\n"
    "v_exp_f32 v35, v35\n"
    "v_exp_f32 v36, v36\n"
    "v_exp_f32 v37, v37\n"
    "s_waitcnt lgkmcnt(2)\n"
    "v_mfma_f32_32x32x16_bf16 v[16:31], v[52:55], v[48:51], 0\n"
    "ds_read_b128 v[52:55], v65 offset:23552\n"
    "s_add_u32 m0, s86, 49152\n"
    "s_mov_b32 s0, 2146304\n"
    "buffer_load_dwordx4 v89, s[4:7], s0 offen sc1 lds\n"
    "v_exp_f32 v38, v38\n"
    "v_exp_f32 v39, v39\n"
    "v_exp_f32 v40, v40\n"
    "v_exp_f32 v41, v41\n"
    "s_waitcnt lgkmcnt(2)\n"
    "v_smfmac_f32_32x32x32_bf16 v[0:15], v[56:59], v[32:39], v64\n"
    "ds_read_b128 v[56:59], v66 offset:23232\n"
    "v_exp_f32 v42, v42\n"
    "v_exp_f32 v43, v43\n"
    "v_exp_f32 v44, v44\n"
    "s_add_u32 m0, s86, 49152\n"
    "s_mov_b32 s0, 2146304\n"
    "buffer_load_dwordx4 v89, s[4:7], s0 offen offset:1024 sc1 lds\n"
    "ds_read_b64_tr_b16 v[128:129], v98 offset:8192\n"
    "ds_read_b64_tr_b16 v[132:133], v99 offset:8192\n"
    "ds_read_b64_tr_b16 v[136:137], v100 offset:8192\n"
    "ds_read_b64_tr_b16 v[140:141], v101 offset:8192\n"
    "v_exp_f32 v45, v45\n"
    "v_exp_f32 v46, v46\n"
    "v_exp_f32 v47, v47\n"
    "v_exp_f32 v16, v16\n"
    "v_exp_f32 v17, v17\n"
    "s_waitcnt lgkmcnt(6)\n"
    "v_smfmac_f32_32x32x32_bf16 v[0:15], v[60:63], v[40:47], v64\n"
    "ds_read_b128 v[60:63], v67 offset:23232\n"
    "v_exp_f32 v18, v18\n"
    "v_exp_f32 v19, v19\n"
    "v_exp_f32 v20, v20\n"
    "v_exp_f32 v21, v21\n"
    "s_waitcnt lgkmcnt(6)\n"
    "v_mfma_f32_32x32x16_bf16 v[32:47], v[52:55], v[48:51], 0\n"
    "ds_read_b128 v[52:55], v65 offset:24576\n"
    "s_add_u32 m0, s85, 50688\n"
    "s_mov_b32 s0, 4243456\n"
    "buffer_load_dwordx4 v89, s[4:7], s0 offen sc1 lds\n"
    "v_exp_f32 v22, v22\n"
    "v_exp_f32 v23, v23\n"
    "v_exp_f32 v24, v24\n"
    "v_exp_f32 v25, v25\n"
    "s_waitcnt lgkmcnt(6)\n"
    "v_smfmac_f32_32x32x32_bf16 v[0:15], v[56:59], v[16:23], v64\n"
    "ds_read_b128 v[56:59], v66 offset:24288\n"
    "v_exp_f32 v26, v26\n"
    "v_exp_f32 v27, v27\n"
    "v_exp_f32 v28, v28\n"
    "s_add_u32 m0, s85, 51744\n"
    "s_mov_b32 s0, 4244480\n"
    "buffer_load_dwordx4 v89, s[4:7], s0 offen sc1 lds\n"
    "ds_read_b64_tr_b16 v[130:131], v98 offset:16384\n"
    "ds_read_b64_tr_b16 v[134:135], v99 offset:16384\n"
    "ds_read_b64_tr_b16 v[138:139], v100 offset:16384\n"
    "ds_read_b64_tr_b16 v[142:143], v101 offset:16384\n"
    "v_exp_f32 v29, v29\n"
    "v_exp_f32 v30, v30\n"
    "v_exp_f32 v31, v31\n"
    "v_exp_f32 v32, v32\n"
    "v_exp_f32 v33, v33\n"
    "s_waitcnt lgkmcnt(6)\n"
    "v_smfmac_f32_32x32x32_bf16 v[0:15], v[60:63], v[24:31], v64\n"
    "ds_read_b128 v[60:63], v67 offset:24288\n"
    "v_exp_f32 v34, v34\n"
    "v_exp_f32 v35, v35\n"
    "v_exp_f32 v36, v36\n"
    "v_exp_f32 v37, v37\n"
    "s_waitcnt lgkmcnt(6)\n"
    "v_mfma_f32_32x32x16_bf16 v[16:31], v[52:55], v[48:51], 0\n"
    "ds_read_b128 v[52:55], v65 offset:25600\n"
    "s_add_u32 m0, s81, 8192\n"
    "s_add_u32 s0, s50, 286720\n"
    "buffer_load_dwordx4 v105, s[8:11], s0 offen lds\n"
    "v_exp_f32 v38, v38\n"
    "v_exp_f32 v39, v39\n"
    "v_exp_f32 v40, v40\n"
    "v_exp_f32 v41, v41\n"
    "s_waitcnt lgkmcnt(6)\n"
    "v_smfmac_f32_32x32x32_bf16 v[0:15], v[56:59], v[32:39], v64\n"
    "ds_read_b128 v[56:59], v66 offset:25344\n"
    "v_exp_f32 v42, v42\n"
    "v_exp_f32 v43, v43\n"
    "v_exp_f32 v44, v44\n"
    "s_add_u32 m0, s81, 73728\n"
    "s_add_u32 s0, s50, 344064\n"
    "buffer_load_dwordx4 v105, s[8:11], s0 offen lds\n"
    "ds_read_b64_tr_b16 v[144:145], v98 offset:24576\n"
    "ds_read_b64_tr_b16 v[148:149], v99 offset:24576\n"
    "ds_read_b64_tr_b16 v[152:153], v100 offset:24576\n"
    "ds_read_b64_tr_b16 v[156:157], v101 offset:24576\n"
    "v_exp_f32 v45, v45\n"
    "v_exp_f32 v46, v46\n"
    "v_exp_f32 v47, v47\n"
    "v_exp_f32 v16, v16\n"
    "v_exp_f32 v17, v17\n"
    "s_waitcnt lgkmcnt(6)\n"
    "v_smfmac_f32_32x32x32_bf16 v[0:15], v[60:63], v[40:47], v64\n"
    "ds_read_b128 v[60:63], v67 offset:25344\n"
    "v_exp_f32 v18, v18\n"
    "v_exp_f32 v19, v19\n"
    "v_exp_f32 v20, v20\n"
    "v_exp_f32 v21, v21\n"
    "s_waitcnt lgkmcnt(6)\n"
    "v_mfma_f32_32x32x16_bf16 v[32:47], v[52:55], v[48:51], 0\n"
    "ds_read_b128 v[52:55], v65 offset:26624\n"
    "v_exp_f32 v22, v22\n"
    "v_exp_f32 v23, v23\n"
    "v_exp_f32 v24, v24\n"
    "v_exp_f32 v25, v25\n"
    "s_waitcnt lgkmcnt(6)\n"
    "v_smfmac_f32_32x32x32_bf16 v[0:15], v[56:59], v[16:23], v64\n"
    "ds_read_b128 v[56:59], v66 offset:26400\n"
    "v_exp_f32 v26, v26\n"
    "v_exp_f32 v27, v27\n"
    "v_exp_f32 v28, v28\n"
    "ds_read_b64_tr_b16 v[146:147], v90 offset:0\n"
    "ds_read_b64_tr_b16 v[150:151], v91 offset:0\n"
    "ds_read_b64_tr_b16 v[154:155], v92 offset:0\n"
    "ds_read_b64_tr_b16 v[158:159], v93 offset:0\n"
    "v_exp_f32 v29, v29\n"
    "v_exp_f32 v30, v30\n"
    "v_exp_f32 v31, v31\n"
    "v_exp_f32 v32, v32\n"
    "v_exp_f32 v33, v33\n"
    "s_waitcnt lgkmcnt(6)\n"
    "v_smfmac_f32_32x32x32_bf16 v[0:15], v[60:63], v[24:31], v64\n"
    "ds_read_b128 v[60:63], v67 offset:26400\n"
    "v_exp_f32 v34, v34\n"
    "v_exp_f32 v35, v35\n"
    "v_exp_f32 v36, v36\n"
    "v_exp_f32 v37, v37\n"
    "s_waitcnt lgkmcnt(6)\n"
    "v_mfma_f32_32x32x16_bf16 v[16:31], v[52:55], v[48:51], 0\n"
    "ds_read_b128 v[52:55], v65 offset:27648\n"
    "v_exp_f32 v38, v38\n"
    "v_exp_f32 v39, v39\n"
    "v_exp_f32 v40, v40\n"
    "v_exp_f32 v41, v41\n"
    "s_waitcnt lgkmcnt(6)\n"
    "v_smfmac_f32_32x32x32_bf16 v[0:15], v[56:59], v[32:39], v64\n"
    "ds_read_b128 v[56:59], v66 offset:27456\n"
    "v_exp_f32 v42, v42\n"
    "v_exp_f32 v43, v43\n"
    "v_exp_f32 v44, v44\n"
    "ds_read_b64_tr_b16 v[160:161], v94 offset:0\n"
    "ds_read_b64_tr_b16 v[164:165], v95 offset:0\n"
    "ds_read_b64_tr_b16 v[168:169], v96 offset:0\n"
    "ds_read_b64_tr_b16 v[172:173], v97 offset:0\n"
    "v_exp_f32 v45, v45\n"
    "v_exp_f32 v46, v46\n"
    "v_exp_f32 v47, v47\n"
    "v_exp_f32 v16, v16\n"
    "v_exp_f32 v17, v17\n"
    "s_waitcnt lgkmcnt(6)\n"
    "v_smfmac_f32_32x32x32_bf16 v[0:15], v[60:63], v[40:47], v64\n"
    "ds_read_b128 v[60:63], v67 offset:27456\n"
    "v_exp_f32 v18, v18\n"
    "v_exp_f32 v19, v19\n"
    "v_exp_f32 v20, v20\n"
    "v_exp_f32 v21, v21\n"
    "s_waitcnt lgkmcnt(6)\n"
    "v_mfma_f32_32x32x16_bf16 v[32:47], v[52:55], v[48:51], 0\n"
    "ds_read_b128 v[52:55], v65 offset:28672\n"
    "v_exp_f32 v22, v22\n"
    "v_exp_f32 v23, v23\n"
    "v_exp_f32 v24, v24\n"
    "v_exp_f32 v25, v25\n"
    "s_waitcnt lgkmcnt(6)\n"
    "v_smfmac_f32_32x32x32_bf16 v[0:15], v[56:59], v[16:23], v64\n"
    "ds_read_b128 v[56:59], v66 offset:28512\n"
    "v_exp_f32 v26, v26\n"
    "v_exp_f32 v27, v27\n"
    "v_exp_f32 v28, v28\n"
    "v_exp_f32 v29, v29\n"
    "v_exp_f32 v30, v30\n"
    "v_exp_f32 v31, v31\n"
    "v_exp_f32 v32, v32\n"
    "v_exp_f32 v33, v33\n"
    "s_waitcnt lgkmcnt(2)\n"
    "v_smfmac_f32_32x32x32_bf16 v[0:15], v[60:63], v[24:31], v64\n"
    "ds_read_b128 v[60:63], v67 offset:28512\n"
    "v_exp_f32 v34, v34\n"
    "v_exp_f32 v35, v35\n"
    "v_exp_f32 v36, v36\n"
    "v_exp_f32 v37, v37\n"
    "s_waitcnt lgkmcnt(2)\n"
    "v_mfma_f32_32x32x16_bf16 v[16:31], v[52:55], v[48:51], 0\n"
    "ds_read_b128 v[52:55], v65 offset:29696\n"
    "v_exp_f32 v38, v38\n"
    "v_exp_f32 v39, v39\n"
    "v_exp_f32 v40, v40\n"
    "v_exp_f32 v41, v41\n"
    "s_waitcnt lgkmcnt(2)\n"
    "v_smfmac_f32_32x32x32_bf16 v[0:15], v[56:59], v[32:39], v64\n"
    "ds_read_b128 v[56:59], v66 offset:29568\n"
    "v_exp_f32 v42, v42\n"
    "v_exp_f32 v43, v43\n"
    "v_exp_f32 v44, v44\n"
    "v_exp_f32 v45, v45\n"
    "v_exp_f32 v46, v46\n"
    "v_exp_f32 v47, v47\n"
    "v_exp_f32 v16, v16\n"
    "v_exp_f32 v17, v17\n"
    "s_waitcnt lgkmcnt(2)\n"
    "v_smfmac_f32_32x32x32_bf16 v[0:15], v[60:63], v[40:47], v64\n"
    "ds_read_b128 v[60:63], v67 offset:29568\n"
    "v_exp_f32 v18, v18\n"
    "v_exp_f32 v19, v19\n"
    "v_exp_f32 v20, v20\n"
    "v_exp_f32 v21, v21\n"
    "s_waitcnt lgkmcnt(2)\n"
    "v_mfma_f32_32x32x16_bf16 v[32:47], v[52:55], v[48:51], 0\n"
    "ds_read_b128 v[52:55], v65 offset:30720\n"
    "v_exp_f32 v22, v22\n"
    "v_exp_f32 v23, v23\n"
    "v_exp_f32 v24, v24\n"
    "v_exp_f32 v25, v25\n"
    "s_waitcnt lgkmcnt(2)\n"
    "v_smfmac_f32_32x32x32_bf16 v[0:15], v[56:59], v[16:23], v64\n"
    "ds_read_b128 v[56:59], v66 offset:30624\n"
    "v_exp_f32 v26, v26\n"
    "v_exp_f32 v27, v27\n"
    "v_exp_f32 v28, v28\n"
    "v_exp_f32 v29, v29\n"
    "v_exp_f32 v30, v30\n"
    "v_exp_f32 v31, v31\n"
    "s_waitcnt vmcnt(0) lgkmcnt(2)\n"
    "s_barrier\n"
    "v_exp_f32 v32, v32\n"
    "v_exp_f32 v33, v33\n"
    "s_waitcnt lgkmcnt(2)\n"
    "v_smfmac_f32_32x32x32_bf16 v[0:15], v[60:63], v[24:31], v64\n"
    "ds_read_b128 v[60:63], v67 offset:30624\n"
    "v_exp_f32 v34, v34\n"
    "v_exp_f32 v35, v35\n"
    "v_exp_f32 v36, v36\n"
    "v_exp_f32 v37, v37\n"
    "s_waitcnt lgkmcnt(2)\n"
    "v_mfma_f32_32x32x16_bf16 v[16:31], v[52:55], v[48:51], 0\n"
    "ds_read_b128 v[52:55], v65 offset:31744\n"
    "s_add_u32 m0, s81, 139264\n"
    "s_add_u32 s0, s50, 401408\n"
    "buffer_load_dwordx4 v105, s[8:11], s0 offen lds\n"
    "v_exp_f32 v38, v38\n"
    "v_exp_f32 v39, v39\n"
    "v_exp_f32 v40, v40\n"
    "v_exp_f32 v41, v41\n"
    "s_waitcnt lgkmcnt(2)\n"
    "v_smfmac_f32_32x32x32_bf16 v[0:15], v[56:59], v[32:39], v64\n"
    "ds_read_b128 v[56:59], v66 offset:31680\n"
    "v_exp_f32 v42, v42\n"
    "v_exp_f32 v43, v43\n"
    "v_exp_f32 v44, v44\n"
    "s_add_u32 m0, s81, 147456\n"
    "s_add_u32 s0, s50, 458752\n"
    "buffer_load_dwordx4 v105, s[8:11], s0 offen lds\n"
    "ds_read_b64_tr_b16 v[162:163], v90 offset:8192\n"
    "ds_read_b64_tr_b16 v[166:167], v91 offset:8192\n"
    "ds_read_b64_tr_b16 v[170:171], v92 offset:8192\n"
    "ds_read_b64_tr_b16 v[174:175], v93 offset:8192\n"
    "v_exp_f32 v45, v45\n"
    "v_exp_f32 v46, v46\n"
    "v_exp_f32 v47, v47\n"
    "v_exp_f32 v16, v16\n"
    "v_exp_f32 v17, v17\n"
    "s_waitcnt lgkmcnt(6)\n"
    "v_smfmac_f32_32x32x32_bf16 v[0:15], v[60:63], v[40:47], v64\n"
    "ds_read_b128 v[60:63], v67 offset:31680\n"
    "v_exp_f32 v18, v18\n"
    "v_exp_f32 v19, v19\n"
    "v_exp_f32 v20, v20\n"
    "v_exp_f32 v21, v21\n"
    "s_waitcnt lgkmcnt(6)\n"
    "v_mfma_f32_32x32x16_bf16 v[32:47], v[52:55], v[48:51], 0\n"
    "ds_read_b128 v[52:55], v65 offset:32768\n"
    "s_add_u32 m0, s81, 155648\n"
    "s_add_u32 s0, s50, 516096\n"
    "buffer_load_dwordx4 v105, s[8:11], s0 offen lds\n"
    "v_exp_f32 v22, v22\n"
    "v_exp_f32 v23, v23\n"
    "v_exp_f32 v24, v24\n"
    "v_exp_f32 v25, v25\n"
    "s_waitcnt lgkmcnt(6)\n"
    "v_smfmac_f32_32x32x32_bf16 v[0:15], v[56:59], v[16:23], v64\n"
    "ds_read_b128 v[56:59], v66 offset:32736\n"
    "v_exp_f32 v26, v26\n"
    "v_exp_f32 v27, v27\n"
    "v_exp_f32 v28, v28\n"
    "s_add_u32 m0, s81, 0\n"
    "s_add_u32 s0, s50, 573440\n"
    "buffer_load_dwordx4 v105, s[8:11], s0 offen lds\n"
    "ds_read_b64_tr_b16 v[176:177], v94 offset:8192\n"
    "ds_read_b64_tr_b16 v[180:181], v95 offset:8192\n"
    "ds_read_b64_tr_b16 v[184:185], v96 offset:8192\n"
    "ds_read_b64_tr_b16 v[188:189], v97 offset:8192\n"
    "v_exp_f32 v29, v29\n"
    "v_exp_f32 v30, v30\n"
    "v_exp_f32 v31, v31\n"
    "v_exp_f32 v32, v32\n"
    "v_exp_f32 v33, v33\n"
    "s_waitcnt lgkmcnt(6)\n"
    "v_smfmac_f32_32x32x32_bf16 v[0:15], v[60:63], v[24:31], v64\n"
    "ds_read_b128 v[60:63], v67 offset:32736\n"
    "v_exp_f32 v34, v34\n"
    "v_exp_f32 v35, v35\n"
    "v_exp_f32 v36, v36\n"
    "v_exp_f32 v37, v37\n"
    "s_waitcnt lgkmcnt(6)\n"
    "v_mfma_f32_32x32x16_bf16 v[16:31], v[52:55], v[48:51], 0\n"
    "ds_read_b128 v[52:55], v65 offset:33792\n"
    "s_add_u32 m0, s81, 65536\n"
    "s_add_u32 s0, s50, 630784\n"
    "buffer_load_dwordx4 v105, s[8:11], s0 offen lds\n"
    "v_exp_f32 v38, v38\n"
    "v_exp_f32 v39, v39\n"
    "v_exp_f32 v40, v40\n"
    "v_exp_f32 v41, v41\n"
    "s_waitcnt lgkmcnt(6)\n"
    "v_smfmac_f32_32x32x32_bf16 v[0:15], v[56:59], v[32:39], v64\n"
    "ds_read_b128 v[56:59], v66 offset:33792\n"
    "v_exp_f32 v42, v42\n"
    "v_exp_f32 v43, v43\n"
    "v_exp_f32 v44, v44\n"
    "s_add_u32 m0, s81, 16384\n"
    "s_add_u32 s0, s50, 688128\n"
    "buffer_load_dwordx4 v105, s[8:11], s0 offen lds\n"
    "v_exp_f32 v45, v45\n"
    "v_exp_f32 v46, v46\n"
    "v_exp_f32 v47, v47\n"
    "v_add_u32 v66, 33792, v66\n"
    "v_add_u32 v67, 33792, v67\n"
    "v_exp_f32 v16, v16\n"
    "v_exp_f32 v17, v17\n"
    "s_waitcnt lgkmcnt(2)\n"
    "v_smfmac_f32_32x32x32_bf16 v[0:15], v[60:63], v[40:47], v64\n"
    "ds_read_b128 v[60:63], v67 offset:0\n"
    "v_exp_f32 v18, v18\n"
    "v_exp_f32 v19, v19\n"
    "v_exp_f32 v20, v20\n"
    "v_exp_f32 v21, v21\n"
    "s_waitcnt lgkmcnt(2)\n"
    "v_mfma_f32_32x32x16_bf16 v[32:47], v[52:55], v[48:51], 0\n"
    "ds_read_b128 v[52:55], v65 offset:34816\n"
    "v_exp_f32 v22, v22\n"
    "v_exp_f32 v23, v23\n"
    "v_exp_f32 v24, v24\n"
    "v_exp_f32 v25, v25\n"
    "s_waitcnt lgkmcnt(2)\n"
    "v_smfmac_f32_32x32x32_bf16 v[0:15], v[56:59], v[16:23], v64\n"
    "ds_read_b128 v[56:59], v66 offset:1056\n"
    "v_exp_f32 v26, v26\n"
    "v_exp_f32 v27, v27\n"
    "v_exp_f32 v28, v28\n"
    "s_add_u32 m0, s81, 81920\n"
    "s_add_u32 s0, s50, 745472\n"
    "buffer_load_dwordx4 v105, s[8:11], s0 offen lds\n"
    "v_exp_f32 v29, v29\n"
    "v_exp_f32 v30, v30\n"
    "v_exp_f32 v31, v31\n"
    "v_exp_f32 v32, v32\n"
    "v_exp_f32 v33, v33\n"
    "s_waitcnt lgkmcnt(2)\n"
    "v_smfmac_f32_32x32x32_bf16 v[0:15], v[60:63], v[24:31], v64\n"
    "ds_read_b128 v[60:63], v67 offset:1056\n"
    "v_exp_f32 v34, v34\n"
    "v_exp_f32 v35, v35\n"
    "v_exp_f32 v36, v36\n"
    "v_exp_f32 v37, v37\n"
    "s_waitcnt lgkmcnt(2)\n"
    "v_mfma_f32_32x32x16_bf16 v[16:31], v[52:55], v[48:51], 0\n"
    "ds_read_b128 v[52:55], v65 offset:35840\n"
    "v_exp_f32 v38, v38\n"
    "v_exp_f32 v39, v39\n"
    "v_exp_f32 v40, v40\n"
    "v_exp_f32 v41, v41\n"
    "s_waitcnt lgkmcnt(2)\n"
    "v_smfmac_f32_32x32x32_bf16 v[0:15], v[56:59], v[32:39], v64\n"
    "ds_read_b128 v[56:59], v66 offset:2112\n"
    "v_exp_f32 v42, v42\n"
    "v_exp_f32 v43, v43\n"
    "v_exp_f32 v44, v44\n"
    "v_exp_f32 v45, v45\n"
    "v_exp_f32 v46, v46\n"
    "v_exp_f32 v47, v47\n"
    "v_exp_f32 v16, v16\n"
    "v_exp_f32 v17, v17\n"
    "s_waitcnt lgkmcnt(2)\n"
    "v_smfmac_f32_32x32x32_bf16 v[0:15], v[60:63], v[40:47], v64\n"
    "ds_read_b128 v[60:63], v67 offset:2112\n"
    "v_exp_f32 v18, v18\n"
    "v_exp_f32 v19, v19\n"
    "v_exp_f32 v20, v20\n"
    "v_exp_f32 v21, v21\n"
    "s_waitcnt lgkmcnt(2)\n"
    "v_mfma_f32_32x32x16_bf16 v[32:47], v[52:55], v[48:51], 0\n"
    "ds_read_b128 v[52:55], v65 offset:36864\n"
    "v_exp_f32 v22, v22\n"
    "v_exp_f32 v23, v23\n"
    "v_exp_f32 v24, v24\n"
    "v_exp_f32 v25, v25\n"
    "s_waitcnt lgkmcnt(2)\n"
    "v_smfmac_f32_32x32x32_bf16 v[0:15], v[56:59], v[16:23], v64\n"
    "ds_read_b128 v[56:59], v66 offset:3168\n"
    "v_exp_f32 v26, v26\n"
    "v_exp_f32 v27, v27\n"
    "v_exp_f32 v28, v28\n"
    "v_exp_f32 v29, v29\n"
    "v_exp_f32 v30, v30\n"
    "v_exp_f32 v31, v31\n"
    "v_exp_f32 v32, v32\n"
    "v_exp_f32 v33, v33\n"
    "s_waitcnt lgkmcnt(2)\n"
    "v_smfmac_f32_32x32x32_bf16 v[0:15], v[60:63], v[24:31], v64\n"
    "ds_read_b128 v[60:63], v67 offset:3168\n"
    "v_exp_f32 v34, v34\n"
    "v_exp_f32 v35, v35\n"
    "v_exp_f32 v36, v36\n"
    "v_exp_f32 v37, v37\n"
    "s_waitcnt lgkmcnt(2)\n"
    "v_mfma_f32_32x32x16_bf16 v[16:31], v[52:55], v[48:51], 0\n"
    "ds_read_b128 v[52:55], v65 offset:37888\n"
    "v_exp_f32 v38, v38\n"
    "v_exp_f32 v39, v39\n"
    "v_exp_f32 v40, v40\n"
    "v_exp_f32 v41, v41\n"
    "s_waitcnt lgkmcnt(2)\n"
    "v_smfmac_f32_32x32x32_bf16 v[0:15], v[56:59], v[32:39], v64\n"
    "ds_read_b128 v[56:59], v66 offset:4224\n"
    "v_exp_f32 v42, v42\n"
    "v_exp_f32 v43, v43\n"
    "v_exp_f32 v44, v44\n"
    "v_exp_f32 v45, v45\n"
    "v_exp_f32 v46, v46\n"
    "v_exp_f32 v47, v47\n"
    "v_exp_f32 v16, v16\n"
    "v_exp_f32 v17, v17\n"
    "s_waitcnt lgkmcnt(2)\n"
    "v_smfmac_f32_32x32x32_bf16 v[0:15], v[60:63], v[40:47], v64\n"
    "ds_read_b128 v[60:63], v67 offset:4224\n"
    "v_exp_f32 v18, v18\n"
    "v_exp_f32 v19, v19\n"
    "v_exp_f32 v20, v20\n"
    "v_exp_f32 v21, v21\n"
    "s_waitcnt lgkmcnt(2)\n"
    "v_mfma_f32_32x32x16_bf16 v[32:47], v[52:55], v[48:51], 0\n"
    "ds_read_b128 v[52:55], v65 offset:38912\n"
    "v_exp_f32 v22, v22\n"
    "v_exp_f32 v23, v23\n"
    "v_exp_f32 v24, v24\n"
    "v_exp_f32 v25, v25\n"
    "s_waitcnt lgkmcnt(2)\n"
    "v_smfmac_f32_32x32x32_bf16 v[0:15], v[56:59], v[16:23], v64\n"
    "ds_read_b128 v[56:59], v66 offset:5280\n"
    "v_exp_f32 v26, v26\n"
    "v_exp_f32 v27, v27\n"
    "v_exp_f32 v28, v28\n"
    "v_exp_f32 v29, v29\n"
    "v_exp_f32 v30, v30\n"
    "v_exp_f32 v31, v31\n"
    "s_waitcnt vmcnt(0) lgkmcnt(2)\n"
    "s_barrier\n"
    "v_exp_f32 v32, v32\n"
    "v_exp_f32 v33, v33\n"
    "s_waitcnt lgkmcnt(2)\n"
    "v_smfmac_f32_32x32x32_bf16 v[0:15], v[60:63], v[24:31], v64\n"
    "ds_read_b128 v[60:63], v67 offset:5280\n"
    "v_exp_f32 v34, v34\n"
    "v_exp_f32 v35, v35\n"
    "v_exp_f32 v36, v36\n"
    "v_exp_f32 v37, v37\n"
    "s_waitcnt lgkmcnt(2)\n"
    "v_mfma_f32_32x32x16_bf16 v[16:31], v[52:55], v[48:51], 0\n"
    "ds_read_b128 v[52:55], v65 offset:39936\n"
    "s_add_u32 m0, s81, 24576\n"
    "s_add_u32 s0, s50, 802816\n"
    "buffer_load_dwordx4 v105, s[8:11], s0 offen lds\n"
    "v_exp_f32 v38, v38\n"
    "v_exp_f32 v39, v39\n"
    "v_exp_f32 v40, v40\n"
    "v_exp_f32 v41, v41\n"
    "s_waitcnt lgkmcnt(2)\n"
    "v_smfmac_f32_32x32x32_bf16 v[0:15], v[56:59], v[32:39], v64\n"
    "ds_read_b128 v[56:59], v66 offset:6336\n"
    "v_exp_f32 v42, v42\n"
    "v_exp_f32 v43, v43\n"
    "v_exp_f32 v44, v44\n"
    "s_add_u32 m0, s81, 90112\n"
    "s_add_u32 s0, s50, 860160\n"
    "buffer_load_dwordx4 v105, s[8:11], s0 offen lds\n"
    "ds_read_b64_tr_b16 v[178:179], v98 offset:8192\n"
    "ds_read_b64_tr_b16 v[182:183], v99 offset:8192\n"
    "ds_read_b64_tr_b16 v[186:187], v100 offset:8192\n"
    "ds_read_b64_tr_b16 v[190:191], v101 offset:8192\n"
    "v_exp_f32 v45, v45\n"
    "v_exp_f32 v46, v46\n"
    "v_exp_f32 v47, v47\n"
    "v_exp_f32 v16, v16\n"
    "v_exp_f32 v17, v17\n"
    "s_waitcnt lgkmcnt(6)\n"
    "v_smfmac_f32_32x32x32_bf16 v[0:15], v[60:63], v[40:47], v64\n"
    "ds_read_b128 v[60:63], v67 offset:6336\n"
    "v_exp_f32 v18, v18\n"
    "v_exp_f32 v19, v19\n"
    "v_exp_f32 v20, v20\n"
    "v_exp_f32 v21, v21\n"
    "s_waitcnt lgkmcnt(6)\n"
    "v_mfma_f32_32x32x16_bf16 v[32:47], v[52:55], v[48:51], 0\n"
    "ds_read_b128 v[52:55], v65 offset:40960\n"
    "s_add_u32 m0, s37, 8192\n"
    "s_add_u32 s0, s52, 16384\n"
    "buffer_load_dwordx4 v107, s[16:19], s0 offen lds\n"
    "v_exp_f32 v22, v22\n"
    "v_exp_f32 v23, v23\n"
    "v_exp_f32 v24, v24\n"
    "v_exp_f32 v25, v25\n"
    "s_waitcnt lgkmcnt(6)\n"
    "v_smfmac_f32_32x32x32_bf16 v[0:15], v[56:59], v[16:23], v64\n"
    "ds_read_b128 v[56:59], v66 offset:7392\n"
    "v_exp_f32 v26, v26\n"
    "v_exp_f32 v27, v27\n"
    "v_exp_f32 v28, v28\n"
    "s_add_u32 m0, s81, 73728\n"
    "s_add_u32 s0, s50, 974848\n"
    "buffer_load_dwordx4 v105, s[8:11], s0 offen lds\n"
    "ds_read_b64_tr_b16 v[192:193], v98 offset:16384\n"
    "ds_read_b64_tr_b16 v[196:197], v99 offset:16384\n"
    "ds_read_b64_tr_b16 v[200:201], v100 offset:16384\n"
    "ds_read_b64_tr_b16 v[204:205], v101 offset:16384\n"
    "v_exp_f32 v29, v29\n"
    "v_exp_f32 v30, v30\n"
    "v_exp_f32 v31, v31\n"
    "v_exp_f32 v32, v32\n"
    "v_exp_f32 v33, v33\n"
    "s_waitcnt lgkmcnt(6)\n"
    "v_smfmac_f32_32x32x32_bf16 v[0:15], v[60:63], v[24:31], v64\n"
    "ds_read_b128 v[60:63], v67 offset:7392\n"
    "v_exp_f32 v34, v34\n"
    "v_exp_f32 v35, v35\n"
    "v_exp_f32 v36, v36\n"
    "v_exp_f32 v37, v37\n"
    "s_waitcnt lgkmcnt(6)\n"
    "v_mfma_f32_32x32x16_bf16 v[16:31], v[52:55], v[48:51], 0\n"
    "ds_read_b128 v[52:55], v65 offset:41984\n"
    "v_exp_f32 v38, v38\n"
    "v_exp_f32 v39, v39\n"
    "v_exp_f32 v40, v40\n"
    "v_exp_f32 v41, v41\n"
    "s_waitcnt lgkmcnt(6)\n"
    "v_smfmac_f32_32x32x32_bf16 v[0:15], v[56:59], v[32:39], v64\n"
    "ds_read_b128 v[56:59], v66 offset:8448\n"
    "v_exp_f32 v42, v42\n"
    "v_exp_f32 v43, v43\n"
    "v_exp_f32 v44, v44\n"
    "ds_read_b64_tr_b16 v[194:195], v98 offset:24576\n"
    "ds_read_b64_tr_b16 v[198:199], v99 offset:24576\n"
    "ds_read_b64_tr_b16 v[202:203], v100 offset:24576\n"
    "ds_read_b64_tr_b16 v[206:207], v101 offset:24576\n"
    "v_exp_f32 v45, v45\n"
    "v_exp_f32 v46, v46\n"
    "v_exp_f32 v47, v47\n"
    "v_exp_f32 v16, v16\n"
    "v_exp_f32 v17, v17\n"
    "s_waitcnt lgkmcnt(6)\n"
    "v_smfmac_f32_32x32x32_bf16 v[0:15], v[60:63], v[40:47], v64\n"
    "ds_read_b128 v[60:63], v67 offset:8448\n"
    "v_exp_f32 v18, v18\n"
    "v_exp_f32 v19, v19\n"
    "v_exp_f32 v20, v20\n"
    "v_exp_f32 v21, v21\n"
    "s_waitcnt lgkmcnt(6)\n"
    "v_mfma_f32_32x32x16_bf16 v[32:47], v[52:55], v[48:51], 0\n"
    "ds_read_b128 v[52:55], v65 offset:43008\n"
    "v_exp_f32 v22, v22\n"
    "v_exp_f32 v23, v23\n"
    "v_exp_f32 v24, v24\n"
    "v_exp_f32 v25, v25\n"
    "s_waitcnt lgkmcnt(6)\n"
    "v_smfmac_f32_32x32x32_bf16 v[0:15], v[56:59], v[16:23], v64\n"
    "ds_read_b128 v[56:59], v66 offset:9504\n"
    "v_exp_f32 v26, v26\n"
    "v_exp_f32 v27, v27\n"
    "v_exp_f32 v28, v28\n"
    "ds_read_b64_tr_b16 v[208:209], v90 offset:0\n"
    "ds_read_b64_tr_b16 v[212:213], v91 offset:0\n"
    "ds_read_b64_tr_b16 v[216:217], v92 offset:0\n"
    "ds_read_b64_tr_b16 v[220:221], v93 offset:0\n"
    "s_add_u32 s0, s65, 0\n"
    "buffer_load_dword v70, v106, s[12:15], s0 offen\n"
    "v_exp_f32 v29, v29\n"
    "v_exp_f32 v30, v30\n"
    "v_exp_f32 v31, v31\n"
    "v_exp_f32 v32, v32\n"
    "v_exp_f32 v33, v33\n"
    "s_waitcnt lgkmcnt(6)\n"
    "v_smfmac_f32_32x32x32_bf16 v[0:15], v[60:63], v[24:31], v64\n"
    "ds_read_b128 v[60:63], v67 offset:9504\n"
    "v_exp_f32 v34, v34\n"
    "v_exp_f32 v35, v35\n"
    "v_exp_f32 v36, v36\n"
    "v_exp_f32 v37, v37\n"
    "s_waitcnt lgkmcnt(6)\n"
    "v_mfma_f32_32x32x16_bf16 v[16:31], v[52:55], v[48:51], 0\n"
    "ds_read_b128 v[52:55], v65 offset:44032\n"
    "v_exp_f32 v38, v38\n"
    "v_exp_f32 v39, v39\n"
    "v_exp_f32 v40, v40\n"
    "v_exp_f32 v41, v41\n"
    "s_waitcnt lgkmcnt(6)\n"
    "v_smfmac_f32_32x32x32_bf16 v[0:15], v[56:59], v[32:39], v64\n"
    "ds_read_b128 v[56:59], v66 offset:10560\n"
    "v_exp_f32 v42, v42\n"
    "v_exp_f32 v43, v43\n"
    "v_exp_f32 v44, v44\n"
    "ds_read_b64_tr_b16 v[210:211], v94 offset:0\n"
    "ds_read_b64_tr_b16 v[214:215], v95 offset:0\n"
    "ds_read_b64_tr_b16 v[218:219], v96 offset:0\n"
    "ds_read_b64_tr_b16 v[222:223], v97 offset:0\n"
    "s_add_u32 s0, s65, 32768\n"
    "buffer_load_dword v70, v106, s[12:15], s0 offen\n"
    "v_exp_f32 v45, v45\n"
    "v_exp_f32 v46, v46\n"
    "v_exp_f32 v47, v47\n"
    "v_exp_f32 v16, v16\n"
    "v_exp_f32 v17, v17\n"
    "s_waitcnt lgkmcnt(6)\n"
    "v_smfmac_f32_32x32x32_bf16 v[0:15], v[60:63], v[40:47], v64\n"
    "ds_read_b128 v[60:63], v67 offset:10560\n"
    "v_exp_f32 v18, v18\n"
    "v_exp_f32 v19, v19\n"
    "v_exp_f32 v20, v20\n"
    "v_exp_f32 v21, v21\n"
    "s_waitcnt lgkmcnt(6)\n"
    "v_mfma_f32_32x32x16_bf16 v[32:47], v[52:55], v[48:51], 0\n"
    "ds_read_b128 v[52:55], v65 offset:45056\n"
    "v_exp_f32 v22, v22\n"
    "v_exp_f32 v23, v23\n"
    "v_exp_f32 v24, v24\n"
    "v_exp_f32 v25, v25\n"
    "s_waitcnt lgkmcnt(6)\n"
    "v_smfmac_f32_32x32x32_bf16 v[0:15], v[56:59], v[16:23], v64\n"
    "ds_read_b128 v[56:59], v66 offset:11616\n"
    "v_exp_f32 v26, v26\n"
    "v_exp_f32 v27, v27\n"
    "v_exp_f32 v28, v28\n"
    "ds_read_b64_tr_b16 v[224:225], v90 offset:16384\n"
    "ds_read_b64_tr_b16 v[228:229], v91 offset:16384\n"
    "ds_read_b64_tr_b16 v[232:233], v92 offset:16384\n"
    "ds_read_b64_tr_b16 v[236:237], v93 offset:16384\n"
    "s_add_u32 s0, s65, 65536\n"
    "buffer_load_dword v70, v106, s[12:15], s0 offen\n"
    "v_exp_f32 v29, v29\n"
    "v_exp_f32 v30, v30\n"
    "v_exp_f32 v31, v31\n"
    "v_exp_f32 v32, v32\n"
    "v_exp_f32 v33, v33\n"
    "s_waitcnt lgkmcnt(6)\n"
    "v_smfmac_f32_32x32x32_bf16 v[0:15], v[60:63], v[24:31], v64\n"
    "ds_read_b128 v[60:63], v67 offset:11616\n"
    "v_exp_f32 v34, v34\n"
    "v_exp_f32 v35, v35\n"
    "v_exp_f32 v36, v36\n"
    "v_exp_f32 v37, v37\n"
    "s_waitcnt lgkmcnt(6)\n"
    "v_mfma_f32_32x32x16_bf16 v[16:31], v[52:55], v[48:51], 0\n"
    "ds_read_b128 v[52:55], v65 offset:46080\n"
    "v_exp_f32 v38, v38\n"
    "v_exp_f32 v39, v39\n"
    "v_exp_f32 v40, v40\n"
    "v_exp_f32 v41, v41\n"
    "s_waitcnt lgkmcnt(6)\n"
    "v_smfmac_f32_32x32x32_bf16 v[0:15], v[56:59], v[32:39], v64\n"
    "ds_read_b128 v[56:59], v66 offset:12672\n"
    "v_exp_f32 v42, v42\n"
    "v_exp_f32 v43, v43\n"
    "v_exp_f32 v44, v44\n"
    "ds_read_b64_tr_b16 v[226:227], v94 offset:16384\n"
    "ds_read_b64_tr_b16 v[230:231], v95 offset:16384\n"
    "ds_read_b64_tr_b16 v[234:235], v96 offset:16384\n"
    "ds_read_b64_tr_b16 v[238:239], v97 offset:16384\n"
    "s_add_u32 s0, s65, 98304\n"
    "buffer_load_dword v70, v106, s[12:15], s0 offen\n"
    "v_exp_f32 v45, v45\n"
    "v_exp_f32 v46, v46\n"
    "v_exp_f32 v47, v47\n"
    "v_exp_f32 v16, v16\n"
    "v_exp_f32 v17, v17\n"
    "s_waitcnt lgkmcnt(6)\n"
    "v_smfmac_f32_32x32x32_bf16 v[0:15], v[60:63], v[40:47], v64\n"
    "ds_read_b128 v[60:63], v67 offset:12672\n"
    "v_exp_f32 v18, v18\n"
    "v_exp_f32 v19, v19\n"
    "v_exp_f32 v20, v20\n"
    "v_exp_f32 v21, v21\n"
    "s_waitcnt lgkmcnt(6)\n"
    "v_mfma_f32_32x32x16_bf16 v[32:47], v[52:55], v[48:51], 0\n"
    "ds_read_b128 v[52:55], v65 offset:47104\n"
    "v_exp_f32 v22, v22\n"
    "v_exp_f32 v23, v23\n"
    "v_exp_f32 v24, v24\n"
    "v_exp_f32 v25, v25\n"
    "s_waitcnt lgkmcnt(6)\n"
    "v_smfmac_f32_32x32x32_bf16 v[0:15], v[56:59], v[16:23], v64\n"
    "ds_read_b128 v[56:59], v66 offset:13728\n"
    "v_exp_f32 v26, v26\n"
    "v_exp_f32 v27, v27\n"
    "v_exp_f32 v28, v28\n"
    "v_exp_f32 v29, v29\n"
    "v_exp_f32 v30, v30\n"
    "v_exp_f32 v31, v31\n"
    "s_waitcnt vmcnt(6) lgkmcnt(2)\n"
    "s_barrier\n"
    "v_exp_f32 v32, v32\n"
    "v_exp_f32 v33, v33\n"
    "s_waitcnt lgkmcnt(2)\n"
    "v_smfmac_f32_32x32x32_bf16 v[0:15], v[60:63], v[24:31], v64\n"
    "ds_read_b128 v[60:63], v67 offset:13728\n"
    "v_exp_f32 v34, v34\n"
    "v_exp_f32 v35, v35\n"
    "v_exp_f32 v36, v36\n"
    "v_exp_f32 v37, v37\n"
    "s_waitcnt lgkmcnt(2)\n"
    "v_mfma_f32_32x32x16_bf16 v[16:31], v[52:55], v[48:51], 0\n"
    "ds_read_b128 v[52:55], v65 offset:48128\n"
    "s_add_u32 m0, s37, 0\n"
    "s_add_u32 s0, s52, 0\n"
    "buffer_load_dwordx4 v107, s[16:19], s0 offen lds\n"
    "v_exp_f32 v38, v38\n"
    "v_exp_f32 v39, v39\n"
    "v_exp_f32 v40, v40\n"
    "v_exp_f32 v41, v41\n"
    "s_waitcnt lgkmcnt(2)\n"
    "v_smfmac_f32_32x32x32_bf16 v[0:15], v[56:59], v[32:39], v64\n"
    "ds_read_b128 v[56:59], v66 offset:14784\n"
    "v_exp_f32 v42, v42\n"
    "v_exp_f32 v43, v43\n"
    "v_exp_f32 v44, v44\n"
    "s_add_u32 m0, s37, 16384\n"
    "s_add_u32 s0, s52, 32768\n"
    "buffer_load_dwordx4 v107, s[16:19], s0 offen lds\n"
    "ds_read_b64_tr_b16 v[240:241], v90 offset:24576\n"
    "ds_read_b64_tr_b16 v[244:245], v91 offset:24576\n"
    "ds_read_b64_tr_b16 v[248:249], v92 offset:24576\n"
    "ds_read_b64_tr_b16 v[252:253], v93 offset:24576\n"
    "v_exp_f32 v45, v45\n"
    "v_exp_f32 v46, v46\n"
    "v_exp_f32 v47, v47\n"
    "v_exp_f32 v16, v16\n"
    "v_exp_f32 v17, v17\n"
    "s_waitcnt lgkmcnt(6)\n"
    "v_smfmac_f32_32x32x32_bf16 v[0:15], v[60:63], v[40:47], v64\n"
    "ds_read_b128 v[60:63], v67 offset:14784\n"
    "v_exp_f32 v18, v18\n"
    "v_exp_f32 v19, v19\n"
    "v_exp_f32 v20, v20\n"
    "v_exp_f32 v21, v21\n"
    "s_waitcnt lgkmcnt(6)\n"
    "v_mfma_f32_32x32x16_bf16 v[32:47], v[52:55], v[48:51], 0\n"
    "ds_read_b128 v[52:55], v65 offset:49152\n"
    "s_add_u32 m0, s37, 32768\n"
    "s_add_u32 s0, s52, 65536\n"
    "buffer_load_dwordx4 v107, s[16:19], s0 offen lds\n"
    "v_exp_f32 v22, v22\n"
    "v_exp_f32 v23, v23\n"
    "v_exp_f32 v24, v24\n"
    "v_exp_f32 v25, v25\n"
    "s_waitcnt lgkmcnt(6)\n"
    "v_smfmac_f32_32x32x32_bf16 v[0:15], v[56:59], v[16:23], v64\n"
    "ds_read_b128 v[56:59], v66 offset:15840\n"
    "v_exp_f32 v26, v26\n"
    "v_exp_f32 v27, v27\n"
    "v_exp_f32 v28, v28\n"
    "s_add_u32 m0, s37, 147456\n"
    "s_add_u32 s0, s52, 98304\n"
    "buffer_load_dwordx4 v107, s[16:19], s0 offen lds\n"
    "ds_read_b64_tr_b16 v[242:243], v94 offset:24576\n"
    "ds_read_b64_tr_b16 v[246:247], v95 offset:24576\n"
    "ds_read_b64_tr_b16 v[250:251], v96 offset:24576\n"
    "ds_read_b64_tr_b16 v[254:255], v97 offset:24576\n"
    "v_exp_f32 v29, v29\n"
    "v_exp_f32 v30, v30\n"
    "v_exp_f32 v31, v31\n"
    "v_exp_f32 v32, v32\n"
    "v_exp_f32 v33, v33\n"
    "s_waitcnt lgkmcnt(6)\n"
    "v_smfmac_f32_32x32x32_bf16 v[0:15], v[60:63], v[24:31], v64\n"
    "ds_read_b128 v[60:63], v67 offset:15840\n"
    "v_exp_f32 v34, v34\n"
    "v_exp_f32 v35, v35\n"
    "v_exp_f32 v36, v36\n"
    "v_exp_f32 v37, v37\n"
    "s_waitcnt lgkmcnt(6)\n"
    "v_mfma_f32_32x32x16_bf16 v[16:31], v[52:55], v[48:51], 0\n"
    "ds_read_b128 v[52:55], v65 offset:50176\n"
    "s_add_u32 m0, s37, 155648\n"
    "s_add_u32 s0, s52, 114688\n"
    "buffer_load_dwordx4 v107, s[16:19], s0 offen lds\n"
    "v_exp_f32 v38, v38\n"
    "v_exp_f32 v39, v39\n"
    "v_exp_f32 v40, v40\n"
    "v_exp_f32 v41, v41\n"
    "s_waitcnt lgkmcnt(6)\n"
    "v_smfmac_f32_32x32x32_bf16 v[0:15], v[56:59], v[32:39], v64\n"
    "ds_read_b128 v[56:59], v66 offset:16896\n"
    "v_exp_f32 v42, v42\n"
    "v_exp_f32 v43, v43\n"
    "v_exp_f32 v44, v44\n"
    "s_add_u32 m0, s81, 65536\n"
    "s_add_u32 s0, s50, 917504\n"
    "buffer_load_dwordx4 v105, s[8:11], s0 offen lds\n"
    "v_exp_f32 v45, v45\n"
    "v_exp_f32 v46, v46\n"
    "v_exp_f32 v47, v47\n"
    "v_exp_f32 v16, v16\n"
    "v_exp_f32 v17, v17\n"
    "s_waitcnt lgkmcnt(2)\n"
    "v_smfmac_f32_32x32x32_bf16 v[0:15], v[60:63], v[40:47], v64\n"
    "ds_read_b128 v[60:63], v67 offset:16896\n"
    "v_exp_f32 v18, v18\n"
    "v_exp_f32 v19, v19\n"
    "v_exp_f32 v20, v20\n"
    "v_exp_f32 v21, v21\n"
    "s_waitcnt lgkmcnt(2)\n"
    "v_mfma_f32_32x32x16_bf16 v[32:47], v[52:55], v[48:51], 0\n"
    "ds_read_b128 v[52:55], v65 offset:51200\n"
    "s_add_u32 m0, s81, 81920\n"
    "s_add_u32 s0, s50, 1032192\n"
    "buffer_load_dwordx4 v105, s[8:11], s0 offen lds\n"
    "v_exp_f32 v22, v22\n"
    "v_exp_f32 v23, v23\n"
    "v_exp_f32 v24, v24\n"
    "v_exp_f32 v25, v25\n"
    "s_waitcnt lgkmcnt(2)\n"
    "v_smfmac_f32_32x32x32_bf16 v[0:15], v[56:59], v[16:23], v64\n"
    "ds_read_b128 v[56:59], v66 offset:17952\n"
    "v_exp_f32 v26, v26\n"
    "v_exp_f32 v27, v27\n"
    "v_exp_f32 v28, v28\n"
    "s_add_u32 m0, s81, 98304\n"
    "s_add_u32 s0, s50, 1146880\n"
    "buffer_load_dwordx4 v105, s[8:11], s0 offen lds\n"
    "v_exp_f32 v29, v29\n"
    "v_exp_f32 v30, v30\n"
    "v_exp_f32 v31, v31\n"
    "v_exp_f32 v32, v32\n"
    "v_exp_f32 v33, v33\n"
    "s_waitcnt lgkmcnt(2)\n"
    "v_smfmac_f32_32x32x32_bf16 v[0:15], v[60:63], v[24:31], v64\n"
    "ds_read_b128 v[60:63], v67 offset:17952\n"
    "v_exp_f32 v34, v34\n"
    "v_exp_f32 v35, v35\n"
    "v_exp_f32 v36, v36\n"
    "v_exp_f32 v37, v37\n"
    "s_waitcnt lgkmcnt(2)\n"
    "v_mfma_f32_32x32x16_bf16 v[16:31], v[52:55], v[48:51], 0\n"
    "ds_read_b128 v[52:55], v65 offset:52224\n"
    "v_exp_f32 v38, v38\n"
    "v_exp_f32 v39, v39\n"
    "v_exp_f32 v40, v40\n"
    "v_exp_f32 v41, v41\n"
    "s_waitcnt lgkmcnt(2)\n"
    "v_smfmac_f32_32x32x32_bf16 v[0:15], v[56:59], v[32:39], v64\n"
    "ds_read_b128 v[56:59], v66 offset:19008\n"
    "v_exp_f32 v42, v42\n"
    "v_exp_f32 v43, v43\n"
    "v_exp_f32 v44, v44\n"
    "v_exp_f32 v45, v45\n"
    "v_exp_f32 v46, v46\n"
    "v_exp_f32 v47, v47\n"
    "v_exp_f32 v16, v16\n"
    "v_exp_f32 v17, v17\n"
    "s_waitcnt lgkmcnt(2)\n"
    "v_smfmac_f32_32x32x32_bf16 v[0:15], v[60:63], v[40:47], v64\n"
    "ds_read_b128 v[60:63], v67 offset:19008\n"
    "v_exp_f32 v18, v18\n"
    "v_exp_f32 v19, v19\n"
    "v_exp_f32 v20, v20\n"
    "v_exp_f32 v21, v21\n"
    "s_waitcnt lgkmcnt(2)\n"
    "v_mfma_f32_32x32x16_bf16 v[32:47], v[52:55], v[48:51], 0\n"
    "ds_read_b128 v[52:55], v65 offset:53248\n"
    "v_exp_f32 v22, v22\n"
    "v_exp_f32 v23, v23\n"
    "v_exp_f32 v24, v24\n"
    "v_exp_f32 v25, v25\n"
    "s_waitcnt lgkmcnt(2)\n"
    "v_smfmac_f32_32x32x32_bf16 v[0:15], v[56:59], v[16:23], v64\n"
    "ds_read_b128 v[56:59], v66 offset:20064\n"
    "v_exp_f32 v26, v26\n"
    "v_exp_f32 v27, v27\n"
    "v_exp_f32 v28, v28\n"
    "v_exp_f32 v29, v29\n"
    "v_exp_f32 v30, v30\n"
    "v_exp_f32 v31, v31\n"
    "v_exp_f32 v32, v32\n"
    "v_exp_f32 v33, v33\n"
    "s_waitcnt lgkmcnt(2)\n"
    "v_smfmac_f32_32x32x32_bf16 v[0:15], v[60:63], v[24:31], v64\n"
    "ds_read_b128 v[60:63], v67 offset:20064\n"
    "v_exp_f32 v34, v34\n"
    "v_exp_f32 v35, v35\n"
    "v_exp_f32 v36, v36\n"
    "v_exp_f32 v37, v37\n"
    "s_waitcnt lgkmcnt(2)\n"
    "v_mfma_f32_32x32x16_bf16 v[16:31], v[52:55], v[48:51], 0\n"
    "ds_read_b128 v[52:55], v65 offset:54272\n"
    "v_exp_f32 v38, v38\n"
    "v_exp_f32 v39, v39\n"
    "v_exp_f32 v40, v40\n"
    "v_exp_f32 v41, v41\n"
    "s_waitcnt lgkmcnt(2)\n"
    "v_smfmac_f32_32x32x32_bf16 v[0:15], v[56:59], v[32:39], v64\n"
    "ds_read_b128 v[56:59], v66 offset:21120\n"
    "v_exp_f32 v42, v42\n"
    "v_exp_f32 v43, v43\n"
    "v_exp_f32 v44, v44\n"
    "v_exp_f32 v45, v45\n"
    "v_exp_f32 v46, v46\n"
    "v_exp_f32 v47, v47\n"
    "v_exp_f32 v16, v16\n"
    "v_exp_f32 v17, v17\n"
    "s_waitcnt lgkmcnt(2)\n"
    "v_smfmac_f32_32x32x32_bf16 v[0:15], v[60:63], v[40:47], v64\n"
    "ds_read_b128 v[60:63], v67 offset:21120\n"
    "v_exp_f32 v18, v18\n"
    "v_exp_f32 v19, v19\n"
    "v_exp_f32 v20, v20\n"
    "v_exp_f32 v21, v21\n"
    "s_waitcnt lgkmcnt(2)\n"
    "v_mfma_f32_32x32x16_bf16 v[32:47], v[52:55], v[48:51], 0\n"
    "ds_read_b128 v[52:55], v65 offset:55296\n"
    "v_exp_f32 v22, v22\n"
    "v_exp_f32 v23, v23\n"
    "v_exp_f32 v24, v24\n"
    "v_exp_f32 v25, v25\n"
    "s_waitcnt lgkmcnt(2)\n"
    "v_smfmac_f32_32x32x32_bf16 v[0:15], v[56:59], v[16:23], v64\n"
    "ds_read_b128 v[56:59], v66 offset:22176\n"
    "v_exp_f32 v26, v26\n"
    "v_exp_f32 v27, v27\n"
    "v_exp_f32 v28, v28\n"
    "v_exp_f32 v29, v29\n"
    "v_exp_f32 v30, v30\n"
    "v_exp_f32 v31, v31\n"
    "s_waitcnt lgkmcnt(2)\n"
    "s_barrier\n"
    "v_exp_f32 v32, v32\n"
    "v_exp_f32 v33, v33\n"
    "s_waitcnt lgkmcnt(2)\n"
    "v_smfmac_f32_32x32x32_bf16 v[0:15], v[60:63], v[24:31], v64\n"
    "ds_read_b128 v[60:63], v67 offset:22176\n"
    "v_exp_f32 v34, v34\n"
    "v_exp_f32 v35, v35\n"
    "v_exp_f32 v36, v36\n"
    "v_exp_f32 v37, v37\n"
    "s_waitcnt lgkmcnt(2)\n"
    "v_mfma_f32_32x32x16_bf16 v[16:31], v[52:55], v[48:51], 0\n"
    "ds_read_b128 v[52:55], v65 offset:56320\n"
    "s_add_u32 m0, s37, 24576\n"
    "s_add_u32 s0, s52, 49152\n"
    "buffer_load_dwordx4 v107, s[16:19], s0 offen lds\n"
    "v_exp_f32 v38, v38\n"
    "v_exp_f32 v39, v39\n"
    "v_exp_f32 v40, v40\n"
    "v_exp_f32 v41, v41\n"
    "s_waitcnt lgkmcnt(2)\n"
    "v_smfmac_f32_32x32x32_bf16 v[0:15], v[56:59], v[32:39], v64\n"
    "ds_read_b128 v[56:59], v66 offset:23232\n"
    "v_exp_f32 v42, v42\n"
    "v_exp_f32 v43, v43\n"
    "v_exp_f32 v44, v44\n"
    "s_add_u32 m0, s37, 40960\n"
    "s_add_u32 s0, s52, 81920\n"
    "buffer_load_dwordx4 v107, s[16:19], s0 offen lds\n"
    "v_exp_f32 v45, v45\n"
    "v_exp_f32 v46, v46\n"
    "v_exp_f32 v47, v47\n"
    "v_exp_f32 v16, v16\n"
    "v_exp_f32 v17, v17\n"
    "s_waitcnt lgkmcnt(2)\n"
    "v_smfmac_f32_32x32x32_bf16 v[0:15], v[60:63], v[40:47], v64\n"
    "ds_read_b128 v[60:63], v67 offset:23232\n"
    "v_exp_f32 v18, v18\n"
    "v_exp_f32 v19, v19\n"
    "v_exp_f32 v20, v20\n"
    "v_exp_f32 v21, v21\n"
    "s_waitcnt lgkmcnt(2)\n"
    "v_mfma_f32_32x32x16_bf16 v[32:47], v[52:55], v[48:51], 0\n"
    "ds_read_b128 v[52:55], v65 offset:57344\n"
    "s_add_u32 m0, s81, 90112\n"
    "s_add_u32 s0, s50, 1089536\n"
    "buffer_load_dwordx4 v105, s[8:11], s0 offen lds\n"
    "v_exp_f32 v22, v22\n"
    "v_exp_f32 v23, v23\n"
    "v_exp_f32 v24, v24\n"
    "v_exp_f32 v25, v25\n"
    "s_waitcnt lgkmcnt(2)\n"
    "v_smfmac_f32_32x32x32_bf16 v[0:15], v[56:59], v[16:23], v64\n"
    "ds_read_b128 v[56:59], v66 offset:24288\n"
    "v_exp_f32 v26, v26\n"
    "v_exp_f32 v27, v27\n"
    "v_exp_f32 v28, v28\n"
    "s_add_u32 m0, s81, 106496\n"
    "s_add_u32 s0, s50, 1204224\n"
    "buffer_load_dwordx4 v105, s[8:11], s0 offen lds\n"
    "v_exp_f32 v29, v29\n"
    "v_exp_f32 v30, v30\n"
    "v_exp_f32 v31, v31\n"
    "v_exp_f32 v32, v32\n"
    "v_exp_f32 v33, v33\n"
    "s_waitcnt lgkmcnt(2)\n"
    "v_smfmac_f32_32x32x32_bf16 v[0:15], v[60:63], v[24:31], v64\n"
    "ds_read_b128 v[60:63], v67 offset:24288\n"
    "v_exp_f32 v34, v34\n"
    "v_exp_f32 v35, v35\n"
    "v_exp_f32 v36, v36\n"
    "v_exp_f32 v37, v37\n"
    "s_waitcnt lgkmcnt(2)\n"
    "v_mfma_f32_32x32x16_bf16 v[16:31], v[52:55], v[48:51], 0\n"
    "ds_read_b128 v[52:55], v65 offset:58368\n"
    "v_exp_f32 v38, v38\n"
    "v_exp_f32 v39, v39\n"
    "v_exp_f32 v40, v40\n"
    "v_exp_f32 v41, v41\n"
    "s_waitcnt lgkmcnt(2)\n"
    "v_smfmac_f32_32x32x32_bf16 v[0:15], v[56:59], v[32:39], v64\n"
    "ds_read_b128 v[56:59], v66 offset:25344\n"
    "v_exp_f32 v42, v42\n"
    "v_exp_f32 v43, v43\n"
    "v_exp_f32 v44, v44\n"
    "v_exp_f32 v45, v45\n"
    "v_exp_f32 v46, v46\n"
    "v_exp_f32 v47, v47\n"
    "v_exp_f32 v16, v16\n"
    "v_exp_f32 v17, v17\n"
    "s_waitcnt lgkmcnt(2)\n"
    "v_smfmac_f32_32x32x32_bf16 v[0:15], v[60:63], v[40:47], v64\n"
    "ds_read_b128 v[60:63], v67 offset:25344\n"
    "v_exp_f32 v18, v18\n"
    "v_exp_f32 v19, v19\n"
    "v_exp_f32 v20, v20\n"
    "v_exp_f32 v21, v21\n"
    "s_waitcnt lgkmcnt(2)\n"
    "v_mfma_f32_32x32x16_bf16 v[32:47], v[52:55], v[48:51], 0\n"
    "ds_read_b128 v[52:55], v65 offset:59392\n"
    "v_exp_f32 v22, v22\n"
    "v_exp_f32 v23, v23\n"
    "v_exp_f32 v24, v24\n"
    "v_exp_f32 v25, v25\n"
    "s_waitcnt lgkmcnt(2)\n"
    "v_smfmac_f32_32x32x32_bf16 v[0:15], v[56:59], v[16:23], v64\n"
    "ds_read_b128 v[56:59], v66 offset:26400\n"
    "v_exp_f32 v26, v26\n"
    "v_exp_f32 v27, v27\n"
    "v_exp_f32 v28, v28\n"
    "v_exp_f32 v29, v29\n"
    "v_exp_f32 v30, v30\n"
    "v_exp_f32 v31, v31\n"
    "v_exp_f32 v32, v32\n"
    "v_exp_f32 v33, v33\n"
    "s_waitcnt lgkmcnt(2)\n"
    "v_smfmac_f32_32x32x32_bf16 v[0:15], v[60:63], v[24:31], v64\n"
    "ds_read_b128 v[60:63], v67 offset:26400\n"
    "v_exp_f32 v34, v34\n"
    "v_exp_f32 v35, v35\n"
    "v_exp_f32 v36, v36\n"
    "v_exp_f32 v37, v37\n"
    "s_waitcnt lgkmcnt(2)\n"
    "v_mfma_f32_32x32x16_bf16 v[16:31], v[52:55], v[48:51], 0\n"
    "ds_read_b128 v[52:55], v65 offset:60416\n"
    "v_exp_f32 v38, v38\n"
    "v_exp_f32 v39, v39\n"
    "v_exp_f32 v40, v40\n"
    "v_exp_f32 v41, v41\n"
    "s_waitcnt lgkmcnt(2)\n"
    "v_smfmac_f32_32x32x32_bf16 v[0:15], v[56:59], v[32:39], v64\n"
    "ds_read_b128 v[56:59], v66 offset:27456\n"
    "v_exp_f32 v42, v42\n"
    "v_exp_f32 v43, v43\n"
    "v_exp_f32 v44, v44\n"
    "v_exp_f32 v45, v45\n"
    "v_exp_f32 v46, v46\n"
    "v_exp_f32 v47, v47\n"
    "v_exp_f32 v16, v16\n"
    "v_exp_f32 v17, v17\n"
    "s_waitcnt lgkmcnt(2)\n"
    "v_smfmac_f32_32x32x32_bf16 v[0:15], v[60:63], v[40:47], v64\n"
    "ds_read_b128 v[60:63], v67 offset:27456\n"
    "v_exp_f32 v18, v18\n"
    "v_exp_f32 v19, v19\n"
    "v_exp_f32 v20, v20\n"
    "v_exp_f32 v21, v21\n"
    "s_waitcnt lgkmcnt(2)\n"
    "v_mfma_f32_32x32x16_bf16 v[32:47], v[52:55], v[48:51], 0\n"
    "ds_read_b128 v[52:55], v65 offset:61440\n"
    "v_exp_f32 v22, v22\n"
    "v_exp_f32 v23, v23\n"
    "v_exp_f32 v24, v24\n"
    "v_exp_f32 v25, v25\n"
    "s_waitcnt lgkmcnt(2)\n"
    "v_smfmac_f32_32x32x32_bf16 v[0:15], v[56:59], v[16:23], v64\n"
    "ds_read_b128 v[56:59], v66 offset:28512\n"
    "v_exp_f32 v26, v26\n"
    "v_exp_f32 v27, v27\n"
    "v_exp_f32 v28, v28\n"
    "v_exp_f32 v29, v29\n"
    "v_exp_f32 v30, v30\n"
    "v_exp_f32 v31, v31\n"
    "v_exp_f32 v32, v32\n"
    "v_exp_f32 v33, v33\n"
    "s_waitcnt lgkmcnt(2)\n"
    "v_smfmac_f32_32x32x32_bf16 v[0:15], v[60:63], v[24:31], v64\n"
    "ds_read_b128 v[60:63], v67 offset:28512\n"
    "v_exp_f32 v34, v34\n"
    "v_exp_f32 v35, v35\n"
    "v_exp_f32 v36, v36\n"
    "v_exp_f32 v37, v37\n"
    "s_waitcnt lgkmcnt(2)\n"
    "v_mfma_f32_32x32x16_bf16 v[16:31], v[52:55], v[48:51], 0\n"
    "ds_read_b128 v[52:55], v65 offset:62464\n"
    "v_exp_f32 v38, v38\n"
    "v_exp_f32 v39, v39\n"
    "v_exp_f32 v40, v40\n"
    "v_exp_f32 v41, v41\n"
    "s_waitcnt lgkmcnt(2)\n"
    "v_smfmac_f32_32x32x32_bf16 v[0:15], v[56:59], v[32:39], v64\n"
    "ds_read_b128 v[56:59], v66 offset:29568\n"
    "v_exp_f32 v42, v42\n"
    "v_exp_f32 v43, v43\n"
    "v_exp_f32 v44, v44\n"
    "v_exp_f32 v45, v45\n"
    "v_exp_f32 v46, v46\n"
    "v_exp_f32 v47, v47\n"
    "v_exp_f32 v16, v16\n"
    "v_exp_f32 v17, v17\n"
    "s_waitcnt lgkmcnt(2)\n"
    "v_smfmac_f32_32x32x32_bf16 v[0:15], v[60:63], v[40:47], v64\n"
    "ds_read_b128 v[60:63], v67 offset:29568\n"
    "v_exp_f32 v18, v18\n"
    "v_exp_f32 v19, v19\n"
    "v_exp_f32 v20, v20\n"
    "v_exp_f32 v21, v21\n"
    "s_waitcnt lgkmcnt(2)\n"
    "v_mfma_f32_32x32x16_bf16 v[32:47], v[52:55], v[48:51], 0\n"
    "ds_read_b128 v[52:55], v65 offset:63488\n"
    "v_exp_f32 v22, v22\n"
    "v_exp_f32 v23, v23\n"
    "v_exp_f32 v24, v24\n"
    "v_exp_f32 v25, v25\n"
    "s_waitcnt lgkmcnt(2)\n"
    "v_smfmac_f32_32x32x32_bf16 v[0:15], v[56:59], v[16:23], v64\n"
    "ds_read_b128 v[56:59], v66 offset:30624\n"
    "v_exp_f32 v26, v26\n"
    "v_exp_f32 v27, v27\n"
    "v_exp_f32 v28, v28\n"
    "v_exp_f32 v29, v29\n"
    "v_exp_f32 v30, v30\n"
    "v_exp_f32 v31, v31\n"
    "v_exp_f32 v32, v32\n"
    "v_exp_f32 v33, v33\n"
    "s_waitcnt lgkmcnt(2)\n"
    "v_smfmac_f32_32x32x32_bf16 v[0:15], v[60:63], v[24:31], v64\n"
    "ds_read_b128 v[60:63], v67 offset:30624\n"
    "v_exp_f32 v34, v34\n"
    "v_exp_f32 v35, v35\n"
    "v_exp_f32 v36, v36\n"
    "v_exp_f32 v37, v37\n"
    "s_waitcnt lgkmcnt(2)\n"
    "v_mfma_f32_32x32x16_bf16 v[16:31], v[52:55], v[48:51], 0\n"
    "ds_read_b128 v[52:55], v65 offset:64512\n"
    "v_exp_f32 v38, v38\n"
    "v_exp_f32 v39, v39\n"
    "v_exp_f32 v40, v40\n"
    "v_exp_f32 v41, v41\n"
    "s_waitcnt lgkmcnt(2)\n"
    "v_smfmac_f32_32x32x32_bf16 v[0:15], v[56:59], v[32:39], v64\n"
    "ds_read_b128 v[56:59], v66 offset:31680\n"
    "v_exp_f32 v42, v42\n"
    "v_exp_f32 v43, v43\n"
    "v_exp_f32 v44, v44\n"
    "v_exp_f32 v45, v45\n"
    "v_exp_f32 v46, v46\n"
    "v_exp_f32 v47, v47\n"
    "v_exp_f32 v16, v16\n"
    "v_exp_f32 v17, v17\n"
    "s_waitcnt lgkmcnt(2)\n"
    "v_smfmac_f32_32x32x32_bf16 v[0:15], v[60:63], v[40:47], v64\n"
    "ds_read_b128 v[60:63], v67 offset:31680\n"
    "v_exp_f32 v18, v18\n"
    "v_exp_f32 v19, v19\n"
    "v_exp_f32 v20, v20\n"
    "v_exp_f32 v21, v21\n"
    "s_waitcnt lgkmcnt(2)\n"
    "v_mfma_f32_32x32x16_bf16 v[32:47], v[52:55], v[48:51], 0\n"
    "v_exp_f32 v22, v22\n"
    "v_exp_f32 v23, v23\n"
    "v_exp_f32 v24, v24\n"
    "v_exp_f32 v25, v25\n"
    "s_waitcnt lgkmcnt(1)\n"
    "v_smfmac_f32_32x32x32_bf16 v[0:15], v[56:59], v[16:23], v64\n"
    "ds_read_b128 v[56:59], v66 offset:32736\n"
    "v_exp_f32 v26, v26\n"
    "v_exp_f32 v27, v27\n"
    "v_exp_f32 v28, v28\n"
    "v_exp_f32 v29, v29\n"
    "v_exp_f32 v30, v30\n"
    "v_exp_f32 v31, v31\n"
    "v_exp_f32 v32, v32\n"
    "v_exp_f32 v33, v33\n"
    "s_waitcnt lgkmcnt(1)\n"
    "v_smfmac_f32_32x32x32_bf16 v[0:15], v[60:63], v[24:31], v64\n"
    "ds_read_b128 v[60:63], v67 offset:32736\n"
    "v_exp_f32 v34, v34\n"
    "v_exp_f32 v35, v35\n"
    "v_exp_f32 v36, v36\n"
    "v_exp_f32 v37, v37\n"
    "v_exp_f32 v38, v38\n"
    "v_exp_f32 v39, v39\n"
    "v_exp_f32 v40, v40\n"
    "v_exp_f32 v41, v41\n"
    "s_waitcnt lgkmcnt(1)\n"
    "v_smfmac_f32_32x32x32_bf16 v[0:15], v[56:59], v[32:39], v64\n"
    "v_exp_f32 v42, v42\n"
    "v_exp_f32 v43, v43\n"
    "v_exp_f32 v44, v44\n"
    "v_exp_f32 v45, v45\n"
    "v_exp_f32 v46, v46\n"
    "v_exp_f32 v47, v47\n"
    "Late_66_%=:\n"
    "s_waitcnt vmcnt(0) lgkmcnt(0)\n"
    "s_nop 0\n"
    "v_smfmac_f32_32x32x32_bf16 v[0:15], v[60:63], v[40:47], v64\n"
    "s_add_u32 s3, s70, s40\n"
    "s_mov_b32 s0, 0x2b800000\n"
    "s_mov_b32 s1, 0x53800000\n"
    "s_nop 8\n"
    "v_cmp_nle_f32 vcc, s0, v8\n"
    "s_cbranch_vccnz Lfb_69_%=\n"
    "v_cmp_nge_f32 vcc, s1, v8\n"
    "s_cbranch_vccnz Lfb_69_%=\n"
    "Lnorm_70_%=:\n"
    "v_rcp_f32 v16, v8\n"
    "s_nop 0\n"
    "v_pk_mul_f32 v[0:1], v[0:1], v[16:17] op_sel_hi:[1,0]\n"
    "v_pk_mul_f32 v[2:3], v[2:3], v[16:17] op_sel_hi:[1,0]\n"
    "v_pk_mul_f32 v[4:5], v[4:5], v[16:17] op_sel_hi:[1,0]\n"
    "v_pk_mul_f32 v[6:7], v[6:7], v[16:17] op_sel_hi:[1,0]\n"
    "v_cvt_pk_bf16_f32 v20, v0, v1\n"
    "v_cvt_pk_bf16_f32 v21, v2, v3\n"
    "v_cvt_pk_bf16_f32 v22, v4, v5\n"
    "v_cvt_pk_bf16_f32 v23, v6, v7\n"
    "v_min_u32 v20, -2, v20\n"
    "v_min_u32 v21, -2, v21\n"
    "v_min_u32 v22, -2, v22\n"
    "v_min_u32 v23, -2, v23\n"
    "buffer_store_dwordx4 v[20:23], v113, s[4:7], s3 offen sc1\n"
    "s_barrier\n"
    "s_add_u32 s2, s70, s47\n"
    "v_add_u32 v32, s2, v104\n"
    "v_add_u32 v33, 16384, v32\n"
    "s_waitcnt vmcnt(0)\n"
    "s_sleep 8\n"
    "s_mov_b32 s84, 0\n"
    "buffer_load_dwordx4 v[40:43], v32, s[4:7], s84 offen offset:0 sc1\n"
    "buffer_load_dwordx4 v[44:47], v32, s[4:7], s84 offen offset:1024 sc1\n"
    "buffer_load_dwordx4 v[48:51], v33, s[4:7], s84 offen offset:0 sc1\n"
    "buffer_load_dwordx4 v[52:55], v33, s[4:7], s84 offen offset:1024 sc1\n"
    "s_lshl_b32 s0, s67, 11\n"
    "s_add_u32 s0, s0, 0\n"
    "v_add_u32 v72, s0, v110\n"
    "v_xor_b32 v73, 32, v72\n"
    "v_xor_b32 v74, 64, v72\n"
    "v_xor_b32 v75, 96, v72\n"
    "ds_read_b64_tr_b16 v[88:89], v72 offset:0\n"
    "ds_read_b64_tr_b16 v[90:91], v72 offset:1024\n"
    "ds_read_b64_tr_b16 v[92:93], v72 offset:16384\n"
    "ds_read_b64_tr_b16 v[94:95], v72 offset:17408\n"
    "s_lshl_b32 s0, s67, 11\n"
    "s_add_u32 s0, s0, 131072\n"
    "v_add_u32 v76, s0, v110\n"
    "v_xor_b32 v77, 32, v76\n"
    "v_xor_b32 v78, 64, v76\n"
    "v_xor_b32 v79, 96, v76\n"
    "s_getpc_b64 s[82:83]\n"
    "Lpc_77_%=:\n"
    "s_mul_i32 s0, s67, Lred_2_%=-Lred_1_%=\n"
    "s_add_u32 s0, s0, Lred_1_%=-Lpc_77_%=\n"
    "s_add_u32 s82, s82, s0\n"
    "s_addc_u32 s83, s83, 0\n"
    "s_lshl_b32 s1, s69, 13\n"
    "s_lshl_b32 s0, s67, 14\n"
    "s_add_u32 s0, s0, s1\n"
    "v_add_u32 v34, s0, v104\n"
    "s_lshl_b32 s0, s67, 11\n"
    "s_add_u32 s0, s0, s1\n"
    "v_add_u32 v35, s0, v104\n"
    "s_mul_i32 s0, s68, 4352\n"
    "s_bfe_u32 s1, s36, 0x10001\n"
    "s_lshl_b32 s1, s1, 7\n"
    "s_add_u32 s0, s0, s1\n"
    "s_add_u32 s0, s0, 131072\n"
    "v_mul_u32_u24 v36, 272, v114\n"
    "v_lshl_add_u32 v36, v115, 4, v36\n"
    "v_add_u32 v36, s0, v36\n"
    "s_mul_i32 s0, s69, 8704\n"
    "s_lshl_b32 s1, s67, 6\n"
    "s_add_u32 s0, s0, s1\n"
    "s_add_u32 s0, s0, 131072\n"
    "v_mul_u32_u24 v37, 1088, v115\n"
    "v_lshl_add_u32 v37, v114, 2, v37\n"
    "v_add_u32 v37, s0, v37\n"
    "s_lshl_b32 s0, s69, 7\n"
    "s_add_u32 s0, s0, 150528\n"
    "v_lshl_add_u32 v38, v115, 4, s0\n"
    "s_mov_b32 s85, 2048\n"
    "buffer_load_dwordx4 v[56:59], v32, s[4:7], s85 offen offset:0 sc1\n"
    "buffer_load_dwordx4 v[60:63], v32, s[4:7], s85 offen offset:1024 sc1\n"
    "buffer_load_dwordx4 v[64:67], v33, s[4:7], s85 offen offset:0 sc1\n"
    "buffer_load_dwordx4 v[68:71], v33, s[4:7], s85 offen offset:1024 sc1\n"
    "s_mov_b32 s86, 0\n"
    "s_mov_b32 s96, 0\n"
    "s_waitcnt vmcnt(4)\n"
    "v_max_u32 v124, v40, v41\n"
    "v_max3_u32 v124, v124, v42, v43\n"
    "v_max3_u32 v124, v124, v44, v45\n"
    "v_max3_u32 v124, v124, v46, v47\n"
    "v_max3_u32 v124, v124, v48, v49\n"
    "v_max3_u32 v124, v124, v50, v51\n"
    "v_max3_u32 v124, v124, v52, v53\n"
    "v_max3_u32 v124, v124, v54, v55\n"
    "v_cmp_eq_u32 vcc, -1, v124\n"
    "s_cbranch_vccnz Lno_78_%=\n"
    "Lok_79_%=:\n"
    "ds_read_b64_tr_b16 v[96:97], v73 offset:0\n"
    "ds_read_b64_tr_b16 v[98:99], v73 offset:1024\n"
    "ds_read_b64_tr_b16 v[100:101], v73 offset:16384\n"
    "ds_read_b64_tr_b16 v[102:103], v73 offset:17408\n"
    "s_waitcnt lgkmcnt(4)\n"
    "v_mfma_f32_16x16x32_bf16 v[0:3], v[88:91], v[40:43], 0\n"
    "v_mfma_f32_16x16x32_bf16 v[0:3], v[92:95], v[44:47], v[0:3]\n"
    "v_mfma_f32_16x16x32_bf16 v[8:11], v[88:91], v[48:51], 0\n"
    "v_mfma_f32_16x16x32_bf16 v[8:11], v[92:95], v[52:55], v[8:11]\n"
    "ds_read_b64_tr_b16 v[88:89], v74 offset:0\n"
    "ds_read_b64_tr_b16 v[90:91], v74 offset:1024\n"
    "ds_read_b64_tr_b16 v[92:93], v74 offset:16384\n"
    "ds_read_b64_tr_b16 v[94:95], v74 offset:17408\n"
    "s_waitcnt lgkmcnt(4)\n"
    "v_mfma_f32_16x16x32_bf16 v[4:7], v[96:99], v[40:43], 0\n"
    "v_mfma_f32_16x16x32_bf16 v[4:7], v[100:103], v[44:47], v[4:7]\n"
    "v_mfma_f32_16x16x32_bf16 v[12:15], v[96:99], v[48:51], 0\n"
    "v_mfma_f32_16x16x32_bf16 v[12:15], v[100:103], v[52:55], v[12:15]\n"
    "ds_read_b64_tr_b16 v[96:97], v75 offset:0\n"
    "ds_read_b64_tr_b16 v[98:99], v75 offset:1024\n"
    "ds_read_b64_tr_b16 v[100:101], v75 offset:16384\n"
    "ds_read_b64_tr_b16 v[102:103], v75 offset:17408\n"
    "s_waitcnt lgkmcnt(4)\n"
    "v_mfma_f32_16x16x32_bf16 v[16:19], v[88:91], v[40:43], 0\n"
    "v_mfma_f32_16x16x32_bf16 v[16:19], v[92:95], v[44:47], v[16:19]\n"
    "v_mfma_f32_16x16x32_bf16 v[24:27], v[88:91], v[48:51], 0\n"
    "v_mfma_f32_16x16x32_bf16 v[24:27], v[92:95], v[52:55], v[24:27]\n"
    "ds_read_b64_tr_b16 v[88:89], v72 offset:32768\n"
    "ds_read_b64_tr_b16 v[90:91], v72 offset:33792\n"
    "ds_read_b64_tr_b16 v[92:93], v76 offset:16384\n"
    "ds_read_b64_tr_b16 v[94:95], v76 offset:17408\n"
    "s_waitcnt lgkmcnt(4)\n"
    "v_mfma_f32_16x16x32_bf16 v[20:23], v[96:99], v[40:43], 0\n"
    "v_mfma_f32_16x16x32_bf16 v[20:23], v[100:103], v[44:47], v[20:23]\n"
    "v_mfma_f32_16x16x32_bf16 v[28:31], v[96:99], v[48:51], 0\n"
    "v_mfma_f32_16x16x32_bf16 v[28:31], v[100:103], v[52:55], v[28:31]\n"
    "s_waitcnt vmcnt(0)\n"
    "v_max_u32 v124, v56, v57\n"
    "v_max3_u32 v124, v124, v58, v59\n"
    "v_max3_u32 v124, v124, v60, v61\n"
    "v_max3_u32 v124, v124, v62, v63\n"
    "v_max3_u32 v124, v124, v64, v65\n"
    "v_max3_u32 v124, v124, v66, v67\n"
    "v_max3_u32 v124, v124, v68, v69\n"
    "v_max3_u32 v124, v124, v70, v71\n"
    "v_cmp_eq_u32 vcc, -1, v124\n"
    "s_cbranch_vccnz Lno_80_%=\n"
    "Lok_81_%=:\n"
    "ds_read_b64_tr_b16 v[96:97], v73 offset:32768\n"
    "ds_read_b64_tr_b16 v[98:99], v73 offset:33792\n"
    "ds_read_b64_tr_b16 v[100:101], v77 offset:16384\n"
    "ds_read_b64_tr_b16 v[102:103], v77 offset:17408\n"
    "s_waitcnt lgkmcnt(4)\n"
    "v_mfma_f32_16x16x32_bf16 v[0:3], v[88:91], v[56:59], v[0:3]\n"
    "v_mfma_f32_16x16x32_bf16 v[0:3], v[92:95], v[60:63], v[0:3]\n"
    "v_mfma_f32_16x16x32_bf16 v[8:11], v[88:91], v[64:67], v[8:11]\n"
    "v_mfma_f32_16x16x32_bf16 v[8:11], v[92:95], v[68:71], v[8:11]\n"
    "ds_read_b64_tr_b16 v[88:89], v74 offset:32768\n"
    "ds_read_b64_tr_b16 v[90:91], v74 offset:33792\n"
    "ds_read_b64_tr_b16 v[92:93], v78 offset:16384\n"
    "ds_read_b64_tr_b16 v[94:95], v78 offset:17408\n"
    "s_waitcnt lgkmcnt(4)\n"
    "v_mfma_f32_16x16x32_bf16 v[4:7], v[96:99], v[56:59], v[4:7]\n"
    "v_mfma_f32_16x16x32_bf16 v[4:7], v[100:103], v[60:63], v[4:7]\n"
    "v_mfma_f32_16x16x32_bf16 v[12:15], v[96:99], v[64:67], v[12:15]\n"
    "v_mfma_f32_16x16x32_bf16 v[12:15], v[100:103], v[68:71], v[12:15]\n"
    "ds_read_b64_tr_b16 v[96:97], v75 offset:32768\n"
    "ds_read_b64_tr_b16 v[98:99], v75 offset:33792\n"
    "ds_read_b64_tr_b16 v[100:101], v79 offset:16384\n"
    "ds_read_b64_tr_b16 v[102:103], v79 offset:17408\n"
    "s_waitcnt lgkmcnt(4)\n"
    "v_mfma_f32_16x16x32_bf16 v[16:19], v[88:91], v[56:59], v[16:19]\n"
    "v_mfma_f32_16x16x32_bf16 v[16:19], v[92:95], v[60:63], v[16:19]\n"
    "v_mfma_f32_16x16x32_bf16 v[24:27], v[88:91], v[64:67], v[24:27]\n"
    "v_mfma_f32_16x16x32_bf16 v[24:27], v[92:95], v[68:71], v[24:27]\n"
    "s_waitcnt lgkmcnt(0)\n"
    "v_mfma_f32_16x16x32_bf16 v[20:23], v[96:99], v[56:59], v[20:23]\n"
    "v_mfma_f32_16x16x32_bf16 v[20:23], v[100:103], v[60:63], v[20:23]\n"
    "v_mfma_f32_16x16x32_bf16 v[28:31], v[96:99], v[64:67], v[28:31]\n"
    "v_mfma_f32_16x16x32_bf16 v[28:31], v[100:103], v[68:71], v[28:31]\n"
    "s_barrier\n"
    "s_nop 4\n"
    "s_cmp_eq_u32 s67, 0\n"
    "s_cbranch_scc1 Lrs_82_%=\n"
    "ds_write_b128 v34, v[0:3] offset:0\n"
    "ds_write_b128 v34, v[4:7] offset:1024\n"
    "Lrs_82_%=:\n"
    "s_cmp_eq_u32 s67, 1\n"
    "s_cbranch_scc1 Lrs_83_%=\n"
    "ds_write_b128 v34, v[8:11] offset:2048\n"
    "ds_write_b128 v34, v[12:15] offset:3072\n"
    "Lrs_83_%=:\n"
    "s_cmp_eq_u32 s67, 2\n"
    "s_cbranch_scc1 Lrs_84_%=\n"
    "ds_write_b128 v34, v[16:19] offset:4096\n"
    "ds_write_b128 v34, v[20:23] offset:5120\n"
    "Lrs_84_%=:\n"
    "s_cmp_eq_u32 s67, 3\n"
    "s_cbranch_scc1 Lrs_85_%=\n"
    "ds_write_b128 v34, v[24:27] offset:6144\n"
    "ds_write_b128 v34, v[28:31] offset:7168\n"
    "Lrs_85_%=:\n"
    "s_swappc_b64 s[98:99], s[82:83]\n"
    "v_pk_add_f32 v[116:117], v[116:117], v[88:89]\n"
    "v_pk_add_f32 v[118:119], v[118:119], v[90:91]\n"
    "v_pk_add_f32 v[120:121], v[120:121], v[92:93]\n"
    "v_pk_add_f32 v[122:123], v[122:123], v[94:95]\n"
    "ds_write_b128 v36, v[116:119]\n"
    "ds_write_b128 v36, v[120:123] offset:64\n"
    "s_lshl_b32 s0, s68, 8\n"
    "s_bfe_u32 s1, s36, 0x10001\n"
    "s_lshl_b32 s1, s1, 3\n"
    "s_add_u32 s0, s0, s1\n"
    "s_add_u32 s0, s0, 148480\n"
    "v_pk_add_f32 v[4:5], v[116:117], v[118:119]\n"
    "v_pk_add_f32 v[6:7], v[120:121], v[122:123]\n"
    "v_pk_add_f32 v[4:5], v[4:5], v[6:7]\n"
    "v_add_f32 v4, v4, v5\n"
    "v_lshl_add_u32 v16, v114, 4, s0\n"
    "v_mov_b32 v5, v4\n"
    "s_nop 1\n"
    "v_permlane16_swap_b32 v4, v5\n"
    "v_add_f32 v4, v4, v5\n"
    "v_mov_b32 v5, v4\n"
    "s_nop 1\n"
    "v_permlane32_swap_b32 v4, v5\n"
    "v_add_f32 v4, v4, v5\n"
    "v_mul_f32 v4, 0x3d000000, v4\n"
    "v_pk_add_f32 v[8:9], v[116:117], v[4:5] op_sel_hi:[1,0] neg_lo:[0,1] neg_hi:[0,1]\n"
    "v_pk_add_f32 v[10:11], v[118:119], v[4:5] op_sel_hi:[1,0] neg_lo:[0,1] neg_hi:[0,1]\n"
    "v_pk_add_f32 v[12:13], v[120:121], v[4:5] op_sel_hi:[1,0] neg_lo:[0,1] neg_hi:[0,1]\n"
    "v_pk_add_f32 v[14:15], v[122:123], v[4:5] op_sel_hi:[1,0] neg_lo:[0,1] neg_hi:[0,1]\n"
    "v_pk_mul_f32 v[8:9], v[8:9], v[8:9]\n"
    "v_pk_mul_f32 v[12:13], v[12:13], v[12:13]\n"
    "v_pk_fma_f32 v[8:9], v[10:11], v[10:11], v[8:9]\n"
    "v_pk_fma_f32 v[12:13], v[14:15], v[14:15], v[12:13]\n"
    "v_pk_add_f32 v[8:9], v[8:9], v[12:13]\n"
    "v_add_f32 v5, v8, v9\n"
    "v_mov_b32 v6, v5\n"
    "s_nop 1\n"
    "v_permlane16_swap_b32 v5, v6\n"
    "v_add_f32 v5, v5, v6\n"
    "v_mov_b32 v6, v5\n"
    "s_nop 1\n"
    "v_permlane32_swap_b32 v5, v6\n"
    "v_add_f32 v5, v5, v6\n"
    "ds_write_b64 v16, v[4:5]\n"
    "s_waitcnt lgkmcnt(0)\n"
    "s_barrier\n"
    "ds_read_b32 v40, v37 offset:0\n"
    "ds_read_b32 v41, v37 offset:272\n"
    "ds_read_b32 v42, v37 offset:544\n"
    "ds_read_b32 v43, v37 offset:816\n"
    "ds_read_b32 v44, v37 offset:4352\n"
    "ds_read_b32 v45, v37 offset:4624\n"
    "ds_read_b32 v46, v37 offset:4896\n"
    "ds_read_b32 v47, v37 offset:5168\n"
    "s_cmp_eq_u32 s36, 0\n"
    "s_cbranch_scc0 Lw_91_%=\n"
    "s_lshl_b32 s2, s71, 17\n"
    "s_add_u32 s3, s2, s60\n"
    "s_add_u32 s2, s2, s61\n"
    "v_add_u32 v16, 148480, v104\n"
    "ds_read_b128 v[4:7], v16\n"
    "v_lshrrev_b32 v17, 1, v104\n"
    "s_waitcnt lgkmcnt(0)\n"
    "v_pk_add_f32 v[12:13], v[4:5], v[6:7]\n"
    "v_sub_f32 v4, v4, v6\n"
    "v_mul_f32 v12, 0.5, v12\n"
    "v_mul_f32 v4, v4, v4\n"
    "v_fmac_f32 v13, 0x41800000, v4\n"
    "v_min_u32 v12, -2, v12\n"
    "v_min_u32 v13, -2, v13\n"
    "buffer_store_dwordx2 v[12:13], v17, s[4:7], s3 offen sc1\n"
    "s_waitcnt vmcnt(0)\n"
    "s_mov_b32 s96, 0\n"
    "buffer_load_dwordx2 v[48:49], v17, s[4:7], s2 offen offset:0 sc1\n"
    "buffer_load_dwordx2 v[50:51], v17, s[4:7], s2 offen offset:512 sc1\n"
    "buffer_load_dwordx2 v[52:53], v17, s[4:7], s2 offen offset:1024 sc1\n"
    "buffer_load_dwordx2 v[54:55], v17, s[4:7], s2 offen offset:1536 sc1\n"
    "buffer_load_dwordx2 v[56:57], v17, s[4:7], s2 offen offset:2048 sc1\n"
    "buffer_load_dwordx2 v[58:59], v17, s[4:7], s2 offen offset:2560 sc1\n"
    "buffer_load_dwordx2 v[60:61], v17, s[4:7], s2 offen offset:3072 sc1\n"
    "buffer_load_dwordx2 v[62:63], v17, s[4:7], s2 offen offset:3584 sc1\n"
    "s_waitcnt vmcnt(0)\n"
    "Lrt_88_%=:\n"
    "v_max_u32 v4, v48, v49\n"
    "v_max3_u32 v4, v4, v50, v51\n"
    "v_max3_u32 v4, v4, v52, v53\n"
    "v_max3_u32 v4, v4, v54, v55\n"
    "v_max3_u32 v4, v4, v56, v57\n"
    "v_max3_u32 v4, v4, v58, v59\n"
    "v_max3_u32 v4, v4, v60, v61\n"
    "v_max3_u32 v4, v4, v62, v63\n"
    "v_cmp_eq_u32 vcc, -1, v4\n"
    "s_cbranch_vccz Lrk_89_%=\n"
    "buffer_load_dwordx2 v[48:49], v17, s[4:7], s2 offen offset:0 sc1\n"
    "buffer_load_dwordx2 v[50:51], v17, s[4:7], s2 offen offset:512 sc1\n"
    "buffer_load_dwordx2 v[52:53], v17, s[4:7], s2 offen offset:1024 sc1\n"
    "buffer_load_dwordx2 v[54:55], v17, s[4:7], s2 offen offset:1536 sc1\n"
    "buffer_load_dwordx2 v[56:57], v17, s[4:7], s2 offen offset:2048 sc1\n"
    "buffer_load_dwordx2 v[58:59], v17, s[4:7], s2 offen offset:2560 sc1\n"
    "buffer_load_dwordx2 v[60:61], v17, s[4:7], s2 offen offset:3072 sc1\n"
    "buffer_load_dwordx2 v[62:63], v17, s[4:7], s2 offen offset:3584 sc1\n"
    "s_add_u32 s96, s96, 1\n"
    "s_cmp_lt_u32 s96, 65536\n"
    "s_waitcnt vmcnt(0)\n"
    "s_cbranch_scc1 Lrt_88_%=\n"
    "Lrk_89_%=:\n"
    "v_pk_add_f32 v[4:5], v[48:49], v[50:51]\n"
    "v_pk_add_f32 v[6:7], v[52:53], v[54:55]\n"
    "v_pk_add_f32 v[8:9], v[56:57], v[58:59]\n"
    "v_pk_add_f32 v[10:11], v[60:61], v[62:63]\n"
    "v_pk_add_f32 v[4:5], v[4:5], v[6:7]\n"
    "v_pk_add_f32 v[8:9], v[8:9], v[10:11]\n"
    "v_pk_add_f32 v[4:5], v[4:5], v[8:9]\n"
    "v_mul_f32 v4, 0x3e000000, v4\n"
    "v_sub_f32 v48, v48, v4\n"
    "v_sub_f32 v50, v50, v4\n"
    "v_sub_f32 v52, v52, v4\n"
    "v_sub_f32 v54, v54, v4\n"
    "v_sub_f32 v56, v56, v4\n"
    "v_sub_f32 v58, v58, v4\n"
    "v_sub_f32 v60, v60, v4\n"
    "v_sub_f32 v62, v62, v4\n"
    "v_mul_f32 v6, v48, v48\n"
    "v_mul_f32 v7, v50, v50\n"
    "v_fmac_f32 v6, v52, v52\n"
    "v_fmac_f32 v7, v54, v54\n"
    "v_fmac_f32 v6, v56, v56\n"
    "v_fmac_f32 v7, v58, v58\n"
    "v_fmac_f32 v6, v60, v60\n"
    "v_fmac_f32 v7, v62, v62\n"
    "v_add_f32 v6, v6, v7\n"
    "v_fmac_f32 v5, 0x42800000, v6\n"
    "v_mul_f32 v5, 0x3b000000, v5\n"
    "v_add_f32 v6, 0x3727c5ac, v5\n"
    "v_rsq_f32 v6, v6\n"
    "v_lshrrev_b32 v17, 1, v17\n"
    "v_mul_f32 v5, v5, v6\n"
    "v_mov_b32 v7, 0x3727c5ac\n"
    "v_fmac_f32 v7, v5, v6\n"
    "v_rsq_f32 v7, v7\n"
    "v_add_u32 v17, 150528, v17\n"
    "v_mul_f32 v5, v6, v7\n"
    "ds_write_b32 v17, v4\n"
    "ds_write_b32 v17, v5 offset:256\n"
    "Lw_91_%=:\n"
    "s_cmp_eq_u32 s36, 0\n"
    "s_cbranch_scc1 Lw_93_%=\n"
    "s_cmp_eq_u32 s66, 0\n"
    "s_cbranch_scc1 Lc_92_%=\n"
    "s_sub_u32 s2, s36, 1\n"
    "Ld7_90_%=:\n"
    "s_cmp_lt_u32 s2, 64\n"
    "s_cselect_b32 s0, 0, 48\n"
    "s_cselect_b32 s3, 24, 14\n"
    "s_add_u32 s0, s0, s2\n"
    "s_lshl_b32 m0, s0, 10\n"
    "s_bfe_u32 s0, s2, 0x20001\n"
    "s_mul_i32 s0, s0, 1835008\n"
    "s_and_b32 s1, s2, 1\n"
    "s_mul_i32 s1, s1, 28672\n"
    "s_add_u32 s0, s0, s1\n"
    "s_lshr_b32 s1, s2, 3\n"
    "s_add_u32 s1, s1, s3\n"
    "s_mul_i32 s1, s1, 57344\n"
    "s_add_u32 s0, s0, s1\n"
    "buffer_load_dwordx4 v105, s[8:11], s0 offen lds\n"
    "s_add_u32 s2, s2, 7\n"
    "s_cmp_lt_u32 s2, 80\n"
    "s_cbranch_scc1 Ld7_90_%=\n"
    "Lc_92_%=:\n"
    "Lw_93_%=:\n"
    "s_add_u32 s3, s70, s41\n"
    "s_waitcnt lgkmcnt(0)\n"
    "s_barrier\n"
    "ds_read_b128 v[48:51], v38 offset:0\n"
    "ds_read_b128 v[52:55], v38 offset:64\n"
    "ds_read_b128 v[56:59], v38 offset:256\n"
    "ds_read_b128 v[60:63], v38 offset:320\n"
    "s_waitcnt lgkmcnt(2)\n"
    "v_pk_add_f32 v[40:41], v[40:41], v[48:49] neg_lo:[0,1] neg_hi:[0,1]\n"
    "v_pk_add_f32 v[42:43], v[42:43], v[50:51] neg_lo:[0,1] neg_hi:[0,1]\n"
    "v_pk_add_f32 v[44:45], v[44:45], v[52:53] neg_lo:[0,1] neg_hi:[0,1]\n"
    "v_pk_add_f32 v[46:47], v[46:47], v[54:55] neg_lo:[0,1] neg_hi:[0,1]\n"
    "s_waitcnt lgkmcnt(0)\n"
    "v_pk_mul_f32 v[40:41], v[40:41], v[56:57]\n"
    "v_pk_mul_f32 v[42:43], v[42:43], v[58:59]\n"
    "v_pk_mul_f32 v[44:45], v[44:45], v[60:61]\n"
    "v_pk_mul_f32 v[46:47], v[46:47], v[62:63]\n"
    "v_cvt_pk_bf16_f32 v4, v40, v41\n"
    "v_cvt_pk_bf16_f32 v5, v42, v43\n"
    "v_cvt_pk_bf16_f32 v6, v44, v45\n"
    "v_cvt_pk_bf16_f32 v7, v46, v47\n"
    "v_min_u32 v4, -2, v4\n"
    "v_min_u32 v5, -2, v5\n"
    "v_min_u32 v6, -2, v6\n"
    "v_min_u32 v7, -2, v7\n"
    "buffer_store_dwordx4 v[4:7], v104, s[4:7], s3 offen sc1\n"
    "s_barrier\n"
    "s_cmp_eq_u32 s66, 0\n"
    "s_cbranch_scc1 Lidle_94_%=\n"
    "v_lshlrev_b32 v38, 3, v115\n"
    "buffer_load_dwordx2 v[96:97], v38, s[24:27], s79 offen\n"
    "buffer_load_dwordx2 v[98:99], v38, s[24:27], s79 offen offset:32\n"
    "s_add_u32 m0, s37, 131072\n"
    "s_add_u32 s0, s51, 524288\n"
    "buffer_load_dwordx4 v106, s[12:15], s0 offen lds\n"
    "s_add_u32 m0, s37, 139264\n"
    "s_add_u32 s0, s51, 589824\n"
    "buffer_load_dwordx4 v106, s[12:15], s0 offen lds\n"
    "s_add_u32 m0, s37, 147456\n"
    "s_add_u32 s0, s51, 655360\n"
    "buffer_load_dwordx4 v106, s[12:15], s0 offen lds\n"
    "s_add_u32 m0, s37, 155648\n"
    "s_add_u32 s0, s51, 720896\n"
    "buffer_load_dwordx4 v106, s[12:15], s0 offen lds\n"
    "s_add_u32 s2, s70, s48\n"
    "v_add_u32 v32, s2, v104\n"
    "v_add_u32 v33, 65536, v32\n"
    "s_waitcnt vmcnt(6)\n"
    "s_sleep 9\n"
    "s_mov_b32 s84, 8192\n"
    "buffer_load_dwordx4 v[40:43], v32, s[4:7], s84 offen offset:0 sc1\n"
    "buffer_load_dwordx4 v[44:47], v32, s[4:7], s84 offen offset:1024 sc1\n"
    "buffer_load_dwordx4 v[48:51], v33, s[4:7], s84 offen offset:0 sc1\n"
    "buffer_load_dwordx4 v[52:55], v33, s[4:7], s84 offen offset:1024 sc1\n"
    "s_lshl_b32 s0, s67, 11\n"
    "s_add_u32 s0, s0, 65536\n"
    "v_add_u32 v34, s0, v109\n"
    "v_xor_b32 v35, 32, v34\n"
    "v_xor_b32 v36, 64, v34\n"
    "v_xor_b32 v37, 96, v34\n"
    "ds_read_b64_tr_b16 v[88:89], v34 offset:0\n"
    "ds_read_b64_tr_b16 v[90:91], v34 offset:8192\n"
    "ds_read_b64_tr_b16 v[92:93], v34 offset:16384\n"
    "ds_read_b64_tr_b16 v[94:95], v34 offset:24576\n"
    "v_mov_b32 v0, 0\n"
    "v_mov_b32 v1, 0\n"
    "v_mov_b32 v2, 0\n"
    "v_mov_b32 v3, 0\n"
    "v_mov_b32 v4, 0\n"
    "v_mov_b32 v5, 0\n"
    "v_mov_b32 v6, 0\n"
    "v_mov_b32 v7, 0\n"
    "v_mov_b32 v8, 0\n"
    "v_mov_b32 v9, 0\n"
    "v_mov_b32 v10, 0\n"
    "v_mov_b32 v11, 0\n"
    "v_mov_b32 v12, 0\n"
    "v_mov_b32 v13, 0\n"
    "v_mov_b32 v14, 0\n"
    "v_mov_b32 v15, 0\n"
    "v_mov_b32 v16, 0\n"
    "v_mov_b32 v17, 0\n"
    "v_mov_b32 v18, 0\n"
    "v_mov_b32 v19, 0\n"
    "v_mov_b32 v20, 0\n"
    "v_mov_b32 v21, 0\n"
    "v_mov_b32 v22, 0\n"
    "v_mov_b32 v23, 0\n"
    "v_mov_b32 v24, 0\n"
    "v_mov_b32 v25, 0\n"
    "v_mov_b32 v26, 0\n"
    "v_mov_b32 v27, 0\n"
    "v_mov_b32 v28, 0\n"
    "v_mov_b32 v29, 0\n"
    "v_mov_b32 v30, 0\n"
    "v_mov_b32 v31, 0\n"
    "s_waitcnt vmcnt(8)\n"
    "s_cmp_eq_u32 s67, 0\n"
    "s_cbranch_scc0 Lb_102_%=\n"
    "v_lshlrev_b32 v0, 16, v96\n"
    "v_and_b32 v1, 0xffff0000, v96\n"
    "v_lshlrev_b32 v2, 16, v97\n"
    "v_and_b32 v3, 0xffff0000, v97\n"
    "v_lshlrev_b32 v4, 16, v98\n"
    "v_and_b32 v5, 0xffff0000, v98\n"
    "v_lshlrev_b32 v6, 16, v99\n"
    "v_and_b32 v7, 0xffff0000, v99\n"
    "Lb_102_%=:\n"
    "s_cmp_eq_u32 s67, 1\n"
    "s_cbranch_scc0 Lb_103_%=\n"
    "v_lshlrev_b32 v8, 16, v96\n"
    "v_and_b32 v9, 0xffff0000, v96\n"
    "v_lshlrev_b32 v10, 16, v97\n"
    "v_and_b32 v11, 0xffff0000, v97\n"
    "v_lshlrev_b32 v12, 16, v98\n"
    "v_and_b32 v13, 0xffff0000, v98\n"
    "v_lshlrev_b32 v14, 16, v99\n"
    "v_and_b32 v15, 0xffff0000, v99\n"
    "Lb_103_%=:\n"
    "s_cmp_eq_u32 s67, 2\n"
    "s_cbranch_scc0 Lb_104_%=\n"
    "v_lshlrev_b32 v16, 16, v96\n"
    "v_and_b32 v17, 0xffff0000, v96\n"
    "v_lshlrev_b32 v18, 16, v97\n"
    "v_and_b32 v19, 0xffff0000, v97\n"
    "v_lshlrev_b32 v20, 16, v98\n"
    "v_and_b32 v21, 0xffff0000, v98\n"
    "v_lshlrev_b32 v22, 16, v99\n"
    "v_and_b32 v23, 0xffff0000, v99\n"
    "Lb_104_%=:\n"
    "s_cmp_eq_u32 s67, 3\n"
    "s_cbranch_scc0 Lb_105_%=\n"
    "v_lshlrev_b32 v24, 16, v96\n"
    "v_and_b32 v25, 0xffff0000, v96\n"
    "v_lshlrev_b32 v26, 16, v97\n"
    "v_and_b32 v27, 0xffff0000, v97\n"
    "v_lshlrev_b32 v28, 16, v98\n"
    "v_and_b32 v29, 0xffff0000, v98\n"
    "v_lshlrev_b32 v30, 16, v99\n"
    "v_and_b32 v31, 0xffff0000, v99\n"
    "Lb_105_%=:\n"
    "s_mov_b32 s85, 10240\n"
    "buffer_load_dwordx4 v[56:59], v32, s[4:7], s85 offen offset:0 sc1\n"
    "buffer_load_dwordx4 v[60:63], v32, s[4:7], s85 offen offset:1024 sc1\n"
    "buffer_load_dwordx4 v[64:67], v33, s[4:7], s85 offen offset:0 sc1\n"
    "buffer_load_dwordx4 v[68:71], v33, s[4:7], s85 offen offset:1024 sc1\n"
    "s_mov_b32 s86, 0\n"
    "s_mov_b32 s96, 0\n"
    "s_waitcnt vmcnt(4)\n"
    "v_max_u32 v124, v40, v41\n"
    "v_max3_u32 v124, v124, v42, v43\n"
    "v_max3_u32 v124, v124, v44, v45\n"
    "v_max3_u32 v124, v124, v46, v47\n"
    "v_max3_u32 v124, v124, v48, v49\n"
    "v_max3_u32 v124, v124, v50, v51\n"
    "v_max3_u32 v124, v124, v52, v53\n"
    "v_max3_u32 v124, v124, v54, v55\n"
    "v_cmp_eq_u32 vcc, -1, v124\n"
    "s_cbranch_vccnz Lno_106_%=\n"
    "Lok_107_%=:\n"
    "ds_read_b64_tr_b16 v[96:97], v35 offset:0\n"
    "ds_read_b64_tr_b16 v[98:99], v35 offset:8192\n"
    "ds_read_b64_tr_b16 v[100:101], v35 offset:16384\n"
    "ds_read_b64_tr_b16 v[102:103], v35 offset:24576\n"
    "s_waitcnt lgkmcnt(4)\n"
    "v_mfma_f32_16x16x32_bf16 v[0:3], v[88:91], v[40:43], v[0:3]\n"
    "v_mfma_f32_16x16x32_bf16 v[0:3], v[92:95], v[44:47], v[0:3]\n"
    "v_mfma_f32_16x16x32_bf16 v[8:11], v[88:91], v[48:51], v[8:11]\n"
    "v_mfma_f32_16x16x32_bf16 v[8:11], v[92:95], v[52:55], v[8:11]\n"
    "s_mov_b32 s86, 12288\n"
    "buffer_load_dwordx4 v[72:75], v32, s[4:7], s86 offen offset:0 sc1\n"
    "ds_read_b64_tr_b16 v[88:89], v36 offset:0\n"
    "ds_read_b64_tr_b16 v[90:91], v36 offset:8192\n"
    "ds_read_b64_tr_b16 v[92:93], v36 offset:16384\n"
    "ds_read_b64_tr_b16 v[94:95], v36 offset:24576\n"
    "s_waitcnt lgkmcnt(4)\n"
    "v_mfma_f32_16x16x32_bf16 v[4:7], v[96:99], v[40:43], v[4:7]\n"
    "v_mfma_f32_16x16x32_bf16 v[4:7], v[100:103], v[44:47], v[4:7]\n"
    "v_mfma_f32_16x16x32_bf16 v[12:15], v[96:99], v[48:51], v[12:15]\n"
    "v_mfma_f32_16x16x32_bf16 v[12:15], v[100:103], v[52:55], v[12:15]\n"
    "buffer_load_dwordx4 v[76:79], v32, s[4:7], s86 offen offset:1024 sc1\n"
    "ds_read_b64_tr_b16 v[96:97], v37 offset:0\n"
    "ds_read_b64_tr_b16 v[98:99], v37 offset:8192\n"
    "ds_read_b64_tr_b16 v[100:101], v37 offset:16384\n"
    "ds_read_b64_tr_b16 v[102:103], v37 offset:24576\n"
    "s_waitcnt lgkmcnt(4)\n"
    "v_mfma_f32_16x16x32_bf16 v[16:19], v[88:91], v[40:43], v[16:19]\n"
    "v_mfma_f32_16x16x32_bf16 v[16:19], v[92:95], v[44:47], v[16:19]\n"
    "v_mfma_f32_16x16x32_bf16 v[24:27], v[88:91], v[48:51], v[24:27]\n"
    "v_mfma_f32_16x16x32_bf16 v[24:27], v[92:95], v[52:55], v[24:27]\n"
    "buffer_load_dwordx4 v[80:83], v33, s[4:7], s86 offen offset:0 sc1\n"
    "s_barrier\n"
    "ds_read_b64_tr_b16 v[88:89], v34 offset:32768\n"
    "ds_read_b64_tr_b16 v[90:91], v34 offset:40960\n"
    "ds_read_b64_tr_b16 v[92:93], v34 offset:49152\n"
    "ds_read_b64_tr_b16 v[94:95], v34 offset:57344\n"
    "s_waitcnt lgkmcnt(4)\n"
    "v_mfma_f32_16x16x32_bf16 v[20:23], v[96:99], v[40:43], v[20:23]\n"
    "v_mfma_f32_16x16x32_bf16 v[20:23], v[100:103], v[44:47], v[20:23]\n"
    "v_mfma_f32_16x16x32_bf16 v[28:31], v[96:99], v[48:51], v[28:31]\n"
    "v_mfma_f32_16x16x32_bf16 v[28:31], v[100:103], v[52:55], v[28:31]\n"
    "buffer_load_dwordx4 v[84:87], v33, s[4:7], s86 offen offset:1024 sc1\n"
    "s_waitcnt vmcnt(4)\n"
    "v_max_u32 v124, v56, v57\n"
    "v_max3_u32 v124, v124, v58, v59\n"
    "v_max3_u32 v124, v124, v60, v61\n"
    "v_max3_u32 v124, v124, v62, v63\n"
    "v_max3_u32 v124, v124, v64, v65\n"
    "v_max3_u32 v124, v124, v66, v67\n"
    "v_max3_u32 v124, v124, v68, v69\n"
    "v_max3_u32 v124, v124, v70, v71\n"
    "v_cmp_eq_u32 vcc, -1, v124\n"
    "s_cbranch_vccnz Lno_108_%=\n"
    "Lok_109_%=:\n"
    "ds_read_b64_tr_b16 v[96:97], v35 offset:32768\n"
    "ds_read_b64_tr_b16 v[98:99], v35 offset:40960\n"
    "ds_read_b64_tr_b16 v[100:101], v35 offset:49152\n"
    "ds_read_b64_tr_b16 v[102:103], v35 offset:57344\n"
    "s_waitcnt lgkmcnt(4)\n"
    "v_mfma_f32_16x16x32_bf16 v[0:3], v[88:91], v[56:59], v[0:3]\n"
    "v_mfma_f32_16x16x32_bf16 v[0:3], v[92:95], v[60:63], v[0:3]\n"
    "v_mfma_f32_16x16x32_bf16 v[8:11], v[88:91], v[64:67], v[8:11]\n"
    "v_mfma_f32_16x16x32_bf16 v[8:11], v[92:95], v[68:71], v[8:11]\n"
    "s_mov_b32 s84, 14336\n"
    "buffer_load_dwordx4 v[40:43], v32, s[4:7], s84 offen offset:0 sc1\n"
    "ds_read_b64_tr_b16 v[88:89], v36 offset:32768\n"
    "ds_read_b64_tr_b16 v[90:91], v36 offset:40960\n"
    "ds_read_b64_tr_b16 v[92:93], v36 offset:49152\n"
    "ds_read_b64_tr_b16 v[94:95], v36 offset:57344\n"
    "s_waitcnt lgkmcnt(4)\n"
    "v_mfma_f32_16x16x32_bf16 v[4:7], v[96:99], v[56:59], v[4:7]\n"
    "v_mfma_f32_16x16x32_bf16 v[4:7], v[100:103], v[60:63], v[4:7]\n"
    "v_mfma_f32_16x16x32_bf16 v[12:15], v[96:99], v[64:67], v[12:15]\n"
    "v_mfma_f32_16x16x32_bf16 v[12:15], v[100:103], v[68:71], v[12:15]\n"
    "buffer_load_dwordx4 v[44:47], v32, s[4:7], s84 offen offset:1024 sc1\n"
    "ds_read_b64_tr_b16 v[96:97], v37 offset:32768\n"
    "ds_read_b64_tr_b16 v[98:99], v37 offset:40960\n"
    "ds_read_b64_tr_b16 v[100:101], v37 offset:49152\n"
    "ds_read_b64_tr_b16 v[102:103], v37 offset:57344\n"
    "s_waitcnt lgkmcnt(4)\n"
    "v_mfma_f32_16x16x32_bf16 v[16:19], v[88:91], v[56:59], v[16:19]\n"
    "v_mfma_f32_16x16x32_bf16 v[16:19], v[92:95], v[60:63], v[16:19]\n"
    "v_mfma_f32_16x16x32_bf16 v[24:27], v[88:91], v[64:67], v[24:27]\n"
    "v_mfma_f32_16x16x32_bf16 v[24:27], v[92:95], v[68:71], v[24:27]\n"
    "buffer_load_dwordx4 v[48:51], v33, s[4:7], s84 offen offset:0 sc1\n"
    "v_subrev_u32 v34, 65536, v34\n"
    "v_subrev_u32 v35, 65536, v35\n"
    "v_subrev_u32 v36, 65536, v36\n"
    "v_subrev_u32 v37, 65536, v37\n"
    "ds_read_b64_tr_b16 v[88:89], v34 offset:0\n"
    "ds_read_b64_tr_b16 v[90:91], v34 offset:8192\n"
    "ds_read_b64_tr_b16 v[92:93], v34 offset:16384\n"
    "ds_read_b64_tr_b16 v[94:95], v34 offset:24576\n"
    "s_waitcnt lgkmcnt(4)\n"
    "v_mfma_f32_16x16x32_bf16 v[20:23], v[96:99], v[56:59], v[20:23]\n"
    "v_mfma_f32_16x16x32_bf16 v[20:23], v[100:103], v[60:63], v[20:23]\n"
    "v_mfma_f32_16x16x32_bf16 v[28:31], v[96:99], v[64:67], v[28:31]\n"
    "v_mfma_f32_16x16x32_bf16 v[28:31], v[100:103], v[68:71], v[28:31]\n"
    "buffer_load_dwordx4 v[52:55], v33, s[4:7], s84 offen offset:1024 sc1\n"
    "s_waitcnt vmcnt(4)\n"
    "v_max_u32 v124, v72, v73\n"
    "v_max3_u32 v124, v124, v74, v75\n"
    "v_max3_u32 v124, v124, v76, v77\n"
    "v_max3_u32 v124, v124, v78, v79\n"
    "v_max3_u32 v124, v124, v80, v81\n"
    "v_max3_u32 v124, v124, v82, v83\n"
    "v_max3_u32 v124, v124, v84, v85\n"
    "v_max3_u32 v124, v124, v86, v87\n"
    "v_cmp_eq_u32 vcc, -1, v124\n"
    "s_cbranch_vccnz Lno_110_%=\n"
    "Lok_111_%=:\n"
    "ds_read_b64_tr_b16 v[96:97], v35 offset:0\n"
    "ds_read_b64_tr_b16 v[98:99], v35 offset:8192\n"
    "ds_read_b64_tr_b16 v[100:101], v35 offset:16384\n"
    "ds_read_b64_tr_b16 v[102:103], v35 offset:24576\n"
    "s_waitcnt lgkmcnt(4)\n"
    "v_mfma_f32_16x16x32_bf16 v[0:3], v[88:91], v[72:75], v[0:3]\n"
    "v_mfma_f32_16x16x32_bf16 v[0:3], v[92:95], v[76:79], v[0:3]\n"
    "v_mfma_f32_16x16x32_bf16 v[8:11], v[88:91], v[80:83], v[8:11]\n"
    "v_mfma_f32_16x16x32_bf16 v[8:11], v[92:95], v[84:87], v[8:11]\n"
    "s_mov_b32 s85, 0\n"
    "buffer_load_dwordx4 v[56:59], v32, s[4:7], s85 offen offset:0 sc1\n"
    "ds_read_b64_tr_b16 v[88:89], v36 offset:0\n"
    "ds_read_b64_tr_b16 v[90:91], v36 offset:8192\n"
    "ds_read_b64_tr_b16 v[92:93], v36 offset:16384\n"
    "ds_read_b64_tr_b16 v[94:95], v36 offset:24576\n"
    "s_waitcnt lgkmcnt(4)\n"
    "v_mfma_f32_16x16x32_bf16 v[4:7], v[96:99], v[72:75], v[4:7]\n"
    "v_mfma_f32_16x16x32_bf16 v[4:7], v[100:103], v[76:79], v[4:7]\n"
    "v_mfma_f32_16x16x32_bf16 v[12:15], v[96:99], v[80:83], v[12:15]\n"
    "v_mfma_f32_16x16x32_bf16 v[12:15], v[100:103], v[84:87], v[12:15]\n"
    "buffer_load_dwordx4 v[60:63], v32, s[4:7], s85 offen offset:1024 sc1\n"
    "ds_read_b64_tr_b16 v[96:97], v37 offset:0\n"
    "ds_read_b64_tr_b16 v[98:99], v37 offset:8192\n"
    "ds_read_b64_tr_b16 v[100:101], v37 offset:16384\n"
    "ds_read_b64_tr_b16 v[102:103], v37 offset:24576\n"
    "s_waitcnt lgkmcnt(4)\n"
    "v_mfma_f32_16x16x32_bf16 v[16:19], v[88:91], v[72:75], v[16:19]\n"
    "v_mfma_f32_16x16x32_bf16 v[16:19], v[92:95], v[76:79], v[16:19]\n"
    "v_mfma_f32_16x16x32_bf16 v[24:27], v[88:91], v[80:83], v[24:27]\n"
    "v_mfma_f32_16x16x32_bf16 v[24:27], v[92:95], v[84:87], v[24:27]\n"
    "buffer_load_dwordx4 v[64:67], v33, s[4:7], s85 offen offset:0 sc1\n"
    "ds_read_b64_tr_b16 v[88:89], v34 offset:32768\n"
    "ds_read_b64_tr_b16 v[90:91], v34 offset:40960\n"
    "ds_read_b64_tr_b16 v[92:93], v34 offset:49152\n"
    "ds_read_b64_tr_b16 v[94:95], v34 offset:57344\n"
    "s_waitcnt lgkmcnt(4)\n"
    "v_mfma_f32_16x16x32_bf16 v[20:23], v[96:99], v[72:75], v[20:23]\n"
    "v_mfma_f32_16x16x32_bf16 v[20:23], v[100:103], v[76:79], v[20:23]\n"
    "v_mfma_f32_16x16x32_bf16 v[28:31], v[96:99], v[80:83], v[28:31]\n"
    "v_mfma_f32_16x16x32_bf16 v[28:31], v[100:103], v[84:87], v[28:31]\n"
    "buffer_load_dwordx4 v[68:71], v33, s[4:7], s85 offen offset:1024 sc1\n"
    "s_waitcnt vmcnt(4)\n"
    "v_max_u32 v124, v40, v41\n"
    "v_max3_u32 v124, v124, v42, v43\n"
    "v_max3_u32 v124, v124, v44, v45\n"
    "v_max3_u32 v124, v124, v46, v47\n"
    "v_max3_u32 v124, v124, v48, v49\n"
    "v_max3_u32 v124, v124, v50, v51\n"
    "v_max3_u32 v124, v124, v52, v53\n"
    "v_max3_u32 v124, v124, v54, v55\n"
    "v_cmp_eq_u32 vcc, -1, v124\n"
    "s_cbranch_vccnz Lno_112_%=\n"
    "Lok_113_%=:\n"
    "ds_read_b64_tr_b16 v[96:97], v35 offset:32768\n"
    "ds_read_b64_tr_b16 v[98:99], v35 offset:40960\n"
    "ds_read_b64_tr_b16 v[100:101], v35 offset:49152\n"
    "ds_read_b64_tr_b16 v[102:103], v35 offset:57344\n"
    "s_waitcnt lgkmcnt(4)\n"
    "v_mfma_f32_16x16x32_bf16 v[0:3], v[88:91], v[40:43], v[0:3]\n"
    "v_mfma_f32_16x16x32_bf16 v[0:3], v[92:95], v[44:47], v[0:3]\n"
    "v_mfma_f32_16x16x32_bf16 v[8:11], v[88:91], v[48:51], v[8:11]\n"
    "v_mfma_f32_16x16x32_bf16 v[8:11], v[92:95], v[52:55], v[8:11]\n"
    "s_mov_b32 s86, 2048\n"
    "buffer_load_dwordx4 v[72:75], v32, s[4:7], s86 offen offset:0 sc1\n"
    "ds_read_b64_tr_b16 v[88:89], v36 offset:32768\n"
    "ds_read_b64_tr_b16 v[90:91], v36 offset:40960\n"
    "ds_read_b64_tr_b16 v[92:93], v36 offset:49152\n"
    "ds_read_b64_tr_b16 v[94:95], v36 offset:57344\n"
    "s_waitcnt lgkmcnt(4)\n"
    "v_mfma_f32_16x16x32_bf16 v[4:7], v[96:99], v[40:43], v[4:7]\n"
    "v_mfma_f32_16x16x32_bf16 v[4:7], v[100:103], v[44:47], v[4:7]\n"
    "v_mfma_f32_16x16x32_bf16 v[12:15], v[96:99], v[48:51], v[12:15]\n"
    "v_mfma_f32_16x16x32_bf16 v[12:15], v[100:103], v[52:55], v[12:15]\n"
    "buffer_load_dwordx4 v[76:79], v32, s[4:7], s86 offen offset:1024 sc1\n"
    "ds_read_b64_tr_b16 v[96:97], v37 offset:32768\n"
    "ds_read_b64_tr_b16 v[98:99], v37 offset:40960\n"
    "ds_read_b64_tr_b16 v[100:101], v37 offset:49152\n"
    "ds_read_b64_tr_b16 v[102:103], v37 offset:57344\n"
    "s_waitcnt lgkmcnt(4)\n"
    "v_mfma_f32_16x16x32_bf16 v[16:19], v[88:91], v[40:43], v[16:19]\n"
    "v_mfma_f32_16x16x32_bf16 v[16:19], v[92:95], v[44:47], v[16:19]\n"
    "v_mfma_f32_16x16x32_bf16 v[24:27], v[88:91], v[48:51], v[24:27]\n"
    "v_mfma_f32_16x16x32_bf16 v[24:27], v[92:95], v[52:55], v[24:27]\n"
    "buffer_load_dwordx4 v[80:83], v33, s[4:7], s86 offen offset:0 sc1\n"
    "s_waitcnt lgkmcnt(0)\n"
    "v_mfma_f32_16x16x32_bf16 v[20:23], v[96:99], v[40:43], v[20:23]\n"
    "v_mfma_f32_16x16x32_bf16 v[20:23], v[100:103], v[44:47], v[20:23]\n"
    "v_mfma_f32_16x16x32_bf16 v[28:31], v[96:99], v[48:51], v[28:31]\n"
    "v_mfma_f32_16x16x32_bf16 v[28:31], v[100:103], v[52:55], v[28:31]\n"
    "buffer_load_dwordx4 v[84:87], v33, s[4:7], s86 offen offset:1024 sc1\n"
    "s_waitcnt vmcnt(4)\n"
    "v_max_u32 v124, v56, v57\n"
    "v_max3_u32 v124, v124, v58, v59\n"
    "v_max3_u32 v124, v124, v60, v61\n"
    "v_max3_u32 v124, v124, v62, v63\n"
    "v_max3_u32 v124, v124, v64, v65\n"
    "v_max3_u32 v124, v124, v66, v67\n"
    "v_max3_u32 v124, v124, v68, v69\n"
    "v_max3_u32 v124, v124, v70, v71\n"
    "v_cmp_eq_u32 vcc, -1, v124\n"
    "s_cbranch_vccnz Lno_114_%=\n"
    "Lok_115_%=:\n"
    "s_barrier\n"
    "v_mfma_f32_16x16x32_bf16 v[0:3], v[128:131], v[56:59], v[0:3]\n"
    "v_mfma_f32_16x16x32_bf16 v[0:3], v[144:147], v[60:63], v[0:3]\n"
    "v_mfma_f32_16x16x32_bf16 v[8:11], v[128:131], v[64:67], v[8:11]\n"
    "v_mfma_f32_16x16x32_bf16 v[8:11], v[144:147], v[68:71], v[8:11]\n"
    "s_mov_b32 s84, 4096\n"
    "buffer_load_dwordx4 v[40:43], v32, s[4:7], s84 offen offset:0 sc1\n"
    "v_mfma_f32_16x16x32_bf16 v[4:7], v[132:135], v[56:59], v[4:7]\n"
    "v_mfma_f32_16x16x32_bf16 v[4:7], v[148:151], v[60:63], v[4:7]\n"
    "v_mfma_f32_16x16x32_bf16 v[12:15], v[132:135], v[64:67], v[12:15]\n"
    "v_mfma_f32_16x16x32_bf16 v[12:15], v[148:151], v[68:71], v[12:15]\n"
    "s_lshl_b32 s1, s69, 13\n"
    "s_lshl_b32 s0, s67, 14\n"
    "s_add_u32 s0, s0, s1\n"
    "v_add_u32 v34, s0, v104\n"
    "s_lshl_b32 s0, s67, 11\n"
    "s_add_u32 s0, s0, s1\n"
    "v_add_u32 v35, s0, v104\n"
    "buffer_load_dwordx4 v[44:47], v32, s[4:7], s84 offen offset:1024 sc1\n"
    "v_mfma_f32_16x16x32_bf16 v[16:19], v[136:139], v[56:59], v[16:19]\n"
    "v_mfma_f32_16x16x32_bf16 v[16:19], v[152:155], v[60:63], v[16:19]\n"
    "v_mfma_f32_16x16x32_bf16 v[24:27], v[136:139], v[64:67], v[24:27]\n"
    "v_mfma_f32_16x16x32_bf16 v[24:27], v[152:155], v[68:71], v[24:27]\n"
    "v_mov_b32 v36, 0xbdd2d3e8\n"
    "v_mov_b32 v37, 0xc0135761\n"
    "v_mov_b32 v38, 1.0\n"
    "buffer_load_dwordx4 v[48:51], v33, s[4:7], s84 offen offset:0 sc1\n"
    "v_mfma_f32_16x16x32_bf16 v[20:23], v[140:143], v[56:59], v[20:23]\n"
    "v_mfma_f32_16x16x32_bf16 v[20:23], v[156:159], v[60:63], v[20:23]\n"
    "v_mfma_f32_16x16x32_bf16 v[28:31], v[140:143], v[64:67], v[28:31]\n"
    "v_mfma_f32_16x16x32_bf16 v[28:31], v[156:159], v[68:71], v[28:31]\n"
    "buffer_load_dwordx4 v[52:55], v33, s[4:7], s84 offen offset:1024 sc1\n"
    "s_waitcnt vmcnt(4)\n"
    "v_max_u32 v124, v72, v73\n"
    "v_max3_u32 v124, v124, v74, v75\n"
    "v_max3_u32 v124, v124, v76, v77\n"
    "v_max3_u32 v124, v124, v78, v79\n"
    "v_max3_u32 v124, v124, v80, v81\n"
    "v_max3_u32 v124, v124, v82, v83\n"
    "v_max3_u32 v124, v124, v84, v85\n"
    "v_max3_u32 v124, v124, v86, v87\n"
    "v_cmp_eq_u32 vcc, -1, v124\n"
    "s_cbranch_vccnz Lno_116_%=\n"
    "Lok_117_%=:\n"
    "v_mfma_f32_16x16x32_bf16 v[0:3], v[160:163], v[72:75], v[0:3]\n"
    "v_mfma_f32_16x16x32_bf16 v[0:3], v[176:179], v[76:79], v[0:3]\n"
    "v_mfma_f32_16x16x32_bf16 v[8:11], v[160:163], v[80:83], v[8:11]\n"
    "v_mfma_f32_16x16x32_bf16 v[8:11], v[176:179], v[84:87], v[8:11]\n"
    "s_mov_b32 s85, 6144\n"
    "buffer_load_dwordx4 v[56:59], v32, s[4:7], s85 offen offset:0 sc1\n"
    "v_mfma_f32_16x16x32_bf16 v[4:7], v[164:167], v[72:75], v[4:7]\n"
    "v_mfma_f32_16x16x32_bf16 v[4:7], v[180:183], v[76:79], v[4:7]\n"
    "v_mfma_f32_16x16x32_bf16 v[12:15], v[164:167], v[80:83], v[12:15]\n"
    "v_mfma_f32_16x16x32_bf16 v[12:15], v[180:183], v[84:87], v[12:15]\n"
    "buffer_load_dwordx4 v[60:63], v32, s[4:7], s85 offen offset:1024 sc1\n"
    "v_mfma_f32_16x16x32_bf16 v[16:19], v[168:171], v[72:75], v[16:19]\n"
    "v_mfma_f32_16x16x32_bf16 v[16:19], v[184:187], v[76:79], v[16:19]\n"
    "v_mfma_f32_16x16x32_bf16 v[24:27], v[168:171], v[80:83], v[24:27]\n"
    "v_mfma_f32_16x16x32_bf16 v[24:27], v[184:187], v[84:87], v[24:27]\n"
    "buffer_load_dwordx4 v[64:67], v33, s[4:7], s85 offen offset:0 sc1\n"
    "v_mfma_f32_16x16x32_bf16 v[20:23], v[172:175], v[72:75], v[20:23]\n"
    "v_mfma_f32_16x16x32_bf16 v[20:23], v[188:191], v[76:79], v[20:23]\n"
    "v_mfma_f32_16x16x32_bf16 v[28:31], v[172:175], v[80:83], v[28:31]\n"
    "v_mfma_f32_16x16x32_bf16 v[28:31], v[188:191], v[84:87], v[28:31]\n"
    "buffer_load_dwordx4 v[68:71], v33, s[4:7], s85 offen offset:1024 sc1\n"
    "s_waitcnt vmcnt(4)\n"
    "v_max_u32 v124, v40, v41\n"
    "v_max3_u32 v124, v124, v42, v43\n"
    "v_max3_u32 v124, v124, v44, v45\n"
    "v_max3_u32 v124, v124, v46, v47\n"
    "v_max3_u32 v124, v124, v48, v49\n"
    "v_max3_u32 v124, v124, v50, v51\n"
    "v_max3_u32 v124, v124, v52, v53\n"
    "v_max3_u32 v124, v124, v54, v55\n"
    "v_cmp_eq_u32 vcc, -1, v124\n"
    "s_cbranch_vccnz Lno_118_%=\n"
    "Lok_119_%=:\n"
    "v_mfma_f32_16x16x32_bf16 v[0:3], v[192:195], v[40:43], v[0:3]\n"
    "v_mfma_f32_16x16x32_bf16 v[0:3], v[208:211], v[44:47], v[0:3]\n"
    "v_mfma_f32_16x16x32_bf16 v[8:11], v[192:195], v[48:51], v[8:11]\n"
    "v_mfma_f32_16x16x32_bf16 v[8:11], v[208:211], v[52:55], v[8:11]\n"
    "s_add_u32 m0, s37, 65536\n"
    "s_add_u32 s0, s51, 1310720\n"
    "buffer_load_dwordx4 v106, s[12:15], s0 offen lds\n"
    "v_mfma_f32_16x16x32_bf16 v[4:7], v[196:199], v[40:43], v[4:7]\n"
    "v_mfma_f32_16x16x32_bf16 v[4:7], v[212:215], v[44:47], v[4:7]\n"
    "v_mfma_f32_16x16x32_bf16 v[12:15], v[196:199], v[48:51], v[12:15]\n"
    "v_mfma_f32_16x16x32_bf16 v[12:15], v[212:215], v[52:55], v[12:15]\n"
    "s_add_u32 m0, s37, 73728\n"
    "s_add_u32 s0, s51, 1376256\n"
    "buffer_load_dwordx4 v106, s[12:15], s0 offen lds\n"
    "v_mfma_f32_16x16x32_bf16 v[16:19], v[200:203], v[40:43], v[16:19]\n"
    "v_mfma_f32_16x16x32_bf16 v[16:19], v[216:219], v[44:47], v[16:19]\n"
    "v_mfma_f32_16x16x32_bf16 v[24:27], v[200:203], v[48:51], v[24:27]\n"
    "v_mfma_f32_16x16x32_bf16 v[24:27], v[216:219], v[52:55], v[24:27]\n"
    "s_add_u32 m0, s37, 81920\n"
    "s_add_u32 s0, s51, 1441792\n"
    "buffer_load_dwordx4 v106, s[12:15], s0 offen lds\n"
    "v_mfma_f32_16x16x32_bf16 v[20:23], v[204:207], v[40:43], v[20:23]\n"
    "v_mfma_f32_16x16x32_bf16 v[20:23], v[220:223], v[44:47], v[20:23]\n"
    "v_mfma_f32_16x16x32_bf16 v[28:31], v[204:207], v[48:51], v[28:31]\n"
    "v_mfma_f32_16x16x32_bf16 v[28:31], v[220:223], v[52:55], v[28:31]\n"
    "s_add_u32 m0, s37, 90112\n"
    "s_add_u32 s0, s51, 1507328\n"
    "buffer_load_dwordx4 v106, s[12:15], s0 offen lds\n"
    "s_waitcnt vmcnt(4)\n"
    "v_max_u32 v124, v56, v57\n"
    "v_max3_u32 v124, v124, v58, v59\n"
    "v_max3_u32 v124, v124, v60, v61\n"
    "v_max3_u32 v124, v124, v62, v63\n"
    "v_max3_u32 v124, v124, v64, v65\n"
    "v_max3_u32 v124, v124, v66, v67\n"
    "v_max3_u32 v124, v124, v68, v69\n"
    "v_max3_u32 v124, v124, v70, v71\n"
    "v_cmp_eq_u32 vcc, -1, v124\n"
    "s_cbranch_vccnz Lno_120_%=\n"
    "Lok_121_%=:\n"
    "v_mfma_f32_16x16x32_bf16 v[0:3], v[224:227], v[56:59], v[0:3]\n"
    "v_mfma_f32_16x16x32_bf16 v[0:3], v[240:243], v[60:63], v[0:3]\n"
    "v_mfma_f32_16x16x32_bf16 v[8:11], v[224:227], v[64:67], v[8:11]\n"
    "v_mfma_f32_16x16x32_bf16 v[8:11], v[240:243], v[68:71], v[8:11]\n"
    "s_add_u32 m0, s37, 98304\n"
    "s_add_u32 s0, s51, 1572864\n"
    "buffer_load_dwordx4 v106, s[12:15], s0 offen lds\n"
    "v_mfma_f32_16x16x32_bf16 v[4:7], v[228:231], v[56:59], v[4:7]\n"
    "v_mfma_f32_16x16x32_bf16 v[4:7], v[244:247], v[60:63], v[4:7]\n"
    "v_mfma_f32_16x16x32_bf16 v[12:15], v[228:231], v[64:67], v[12:15]\n"
    "v_mfma_f32_16x16x32_bf16 v[12:15], v[244:247], v[68:71], v[12:15]\n"
    "s_add_u32 m0, s37, 106496\n"
    "s_add_u32 s0, s51, 1638400\n"
    "buffer_load_dwordx4 v106, s[12:15], s0 offen lds\n"
    "v_mfma_f32_16x16x32_bf16 v[16:19], v[232:235], v[56:59], v[16:19]\n"
    "v_mfma_f32_16x16x32_bf16 v[16:19], v[248:251], v[60:63], v[16:19]\n"
    "v_mfma_f32_16x16x32_bf16 v[24:27], v[232:235], v[64:67], v[24:27]\n"
    "v_mfma_f32_16x16x32_bf16 v[24:27], v[248:251], v[68:71], v[24:27]\n"
    "s_cmp_eq_u32 s67, 0\n"
    "s_cbranch_scc1 Lrs_122_%=\n"
    "ds_write_b128 v34, v[0:3] offset:0\n"
    "ds_write_b128 v34, v[4:7] offset:1024\n"
    "Lrs_122_%=:\n"
    "s_add_u32 m0, s37, 114688\n"
    "s_add_u32 s0, s51, 1703936\n"
    "buffer_load_dwordx4 v106, s[12:15], s0 offen lds\n"
    "v_mfma_f32_16x16x32_bf16 v[20:23], v[236:239], v[56:59], v[20:23]\n"
    "v_mfma_f32_16x16x32_bf16 v[20:23], v[252:255], v[60:63], v[20:23]\n"
    "v_mfma_f32_16x16x32_bf16 v[28:31], v[236:239], v[64:67], v[28:31]\n"
    "v_mfma_f32_16x16x32_bf16 v[28:31], v[252:255], v[68:71], v[28:31]\n"
    "s_cmp_eq_u32 s67, 1\n"
    "s_cbranch_scc1 Lrs_123_%=\n"
    "ds_write_b128 v34, v[8:11] offset:2048\n"
    "ds_write_b128 v34, v[12:15] offset:3072\n"
    "Lrs_123_%=:\n"
    "s_add_u32 m0, s37, 122880\n"
    "s_add_u32 s0, s51, 1769472\n"
    "buffer_load_dwordx4 v106, s[12:15], s0 offen lds\n"
    "s_add_u32 s3, s70, s42\n"
    "s_nop 4\n"
    "s_cmp_eq_u32 s67, 2\n"
    "s_cbranch_scc1 Lrs_124_%=\n"
    "ds_write_b128 v34, v[16:19] offset:4096\n"
    "ds_write_b128 v34, v[20:23] offset:5120\n"
    "Lrs_124_%=:\n"
    "s_cmp_eq_u32 s67, 3\n"
    "s_cbranch_scc1 Lrs_125_%=\n"
    "ds_write_b128 v34, v[24:27] offset:6144\n"
    "ds_write_b128 v34, v[28:31] offset:7168\n"
    "Lrs_125_%=:\n"
    "s_swappc_b64 s[98:99], s[82:83]\n"
    "v_pk_mul_f32 v[8:9], v[88:89], v[88:89]\n"
    "v_pk_mul_f32 v[10:11], v[90:91], v[90:91]\n"
    "v_pk_mul_f32 v[12:13], v[92:93], v[92:93]\n"
    "v_pk_mul_f32 v[14:15], v[94:95], v[94:95]\n"
    "v_pk_fma_f32 v[8:9], v[8:9], v[36:37], v[36:37] op_sel:[0,0,1] op_sel_hi:[1,0,1]\n"
    "v_pk_fma_f32 v[10:11], v[10:11], v[36:37], v[36:37] op_sel:[0,0,1] op_sel_hi:[1,0,1]\n"
    "v_pk_fma_f32 v[12:13], v[12:13], v[36:37], v[36:37] op_sel:[0,0,1] op_sel_hi:[1,0,1]\n"
    "v_pk_fma_f32 v[14:15], v[14:15], v[36:37], v[36:37] op_sel:[0,0,1] op_sel_hi:[1,0,1]\n"
    "v_pk_mul_f32 v[8:9], v[8:9], v[88:89]\n"
    "v_pk_mul_f32 v[10:11], v[10:11], v[90:91]\n"
    "v_pk_mul_f32 v[12:13], v[12:13], v[92:93]\n"
    "v_pk_mul_f32 v[14:15], v[14:15], v[94:95]\n"
    "v_exp_f32 v8, v8\n"
    "v_exp_f32 v9, v9\n"
    "v_exp_f32 v10, v10\n"
    "v_exp_f32 v11, v11\n"
    "v_exp_f32 v12, v12\n"
    "v_exp_f32 v13, v13\n"
    "v_exp_f32 v14, v14\n"
    "v_exp_f32 v15, v15\n"
    "v_pk_add_f32 v[8:9], v[8:9], v[38:39] op_sel_hi:[1,0]\n"
    "v_pk_add_f32 v[10:11], v[10:11], v[38:39] op_sel_hi:[1,0]\n"
    "v_pk_add_f32 v[12:13], v[12:13], v[38:39] op_sel_hi:[1,0]\n"
    "v_pk_add_f32 v[14:15], v[14:15], v[38:39] op_sel_hi:[1,0]\n"
    "v_rcp_f32 v8, v8\n"
    "v_rcp_f32 v9, v9\n"
    "v_rcp_f32 v10, v10\n"
    "v_rcp_f32 v11, v11\n"
    "v_rcp_f32 v12, v12\n"
    "v_rcp_f32 v13, v13\n"
    "v_rcp_f32 v14, v14\n"
    "v_rcp_f32 v15, v15\n"
    "v_pk_mul_f32 v[0:1], v[88:89], v[8:9]\n"
    "v_pk_mul_f32 v[2:3], v[90:91], v[10:11]\n"
    "v_pk_mul_f32 v[4:5], v[92:93], v[12:13]\n"
    "v_pk_mul_f32 v[6:7], v[94:95], v[14:15]\n"
    "v_cvt_pk_bf16_f32 v16, v0, v1\n"
    "v_cvt_pk_bf16_f32 v17, v2, v3\n"
    "v_cvt_pk_bf16_f32 v18, v4, v5\n"
    "v_cvt_pk_bf16_f32 v19, v6, v7\n"
    "v_min_u32 v16, -2, v16\n"
    "v_min_u32 v17, -2, v17\n"
    "v_min_u32 v18, -2, v18\n"
    "v_min_u32 v19, -2, v19\n"
    "buffer_store_dwordx4 v[16:19], v104, s[4:7], s3 offen sc1\n"
    "s_waitcnt vmcnt(1) lgkmcnt(0)\n"
    "s_barrier\n"
    "s_branch Lpre2_95_%=\n"
    "Lidle_94_%=:\n"
    "s_add_u32 m0, s37, 65536\n"
    "s_add_u32 s0, s51, 1310720\n"
    "buffer_load_dwordx4 v106, s[12:15], s0 offen lds\n"
    "s_add_u32 m0, s37, 73728\n"
    "s_add_u32 s0, s51, 1376256\n"
    "buffer_load_dwordx4 v106, s[12:15], s0 offen lds\n"
    "s_add_u32 m0, s37, 81920\n"
    "s_add_u32 s0, s51, 1441792\n"
    "buffer_load_dwordx4 v106, s[12:15], s0 offen lds\n"
    "s_add_u32 m0, s37, 90112\n"
    "s_add_u32 s0, s51, 1507328\n"
    "buffer_load_dwordx4 v106, s[12:15], s0 offen lds\n"
    "s_add_u32 m0, s37, 98304\n"
    "s_add_u32 s0, s51, 1572864\n"
    "buffer_load_dwordx4 v106, s[12:15], s0 offen lds\n"
    "s_add_u32 m0, s37, 106496\n"
    "s_add_u32 s0, s51, 1638400\n"
    "buffer_load_dwordx4 v106, s[12:15], s0 offen lds\n"
    "s_add_u32 m0, s37, 114688\n"
    "s_add_u32 s0, s51, 1703936\n"
    "buffer_load_dwordx4 v106, s[12:15], s0 offen lds\n"
    "s_add_u32 m0, s37, 122880\n"
    "s_add_u32 s0, s51, 1769472\n"
    "buffer_load_dwordx4 v106, s[12:15], s0 offen lds\n"
    "s_add_u32 m0, s37, 131072\n"
    "s_add_u32 s0, s51, 524288\n"
    "buffer_load_dwordx4 v106, s[12:15], s0 offen lds\n"
    "s_add_u32 m0, s37, 139264\n"
    "s_add_u32 s0, s51, 589824\n"
    "buffer_load_dwordx4 v106, s[12:15], s0 offen lds\n"
    "s_add_u32 m0, s37, 147456\n"
    "s_add_u32 s0, s51, 655360\n"
    "buffer_load_dwordx4 v106, s[12:15], s0 offen lds\n"
    "s_add_u32 m0, s37, 155648\n"
    "s_add_u32 s0, s51, 720896\n"
    "buffer_load_dwordx4 v106, s[12:15], s0 offen lds\n"
    "s_waitcnt vmcnt(0) lgkmcnt(0)\n"
    "s_barrier\n"
    "Lpre2_95_%=:\n"
    "v_lshlrev_b32 v38, 1, v114\n"
    "buffer_load_ushort v39, v38, s[28:31], s80 offen\n"
    "s_add_u32 m0, s37, 0\n"
    "s_add_u32 s0, s51, 0\n"
    "buffer_load_dwordx4 v106, s[12:15], s0 offen lds\n"
    "s_add_u32 m0, s37, 8192\n"
    "s_add_u32 s0, s51, 65536\n"
    "buffer_load_dwordx4 v106, s[12:15], s0 offen lds\n"
    "s_add_u32 m0, s37, 16384\n"
    "s_add_u32 s0, s51, 131072\n"
    "buffer_load_dwordx4 v106, s[12:15], s0 offen lds\n"
    "s_add_u32 m0, s37, 24576\n"
    "s_add_u32 s0, s51, 196608\n"
    "buffer_load_dwordx4 v106, s[12:15], s0 offen lds\n"
    "s_add_u32 m0, s37, 32768\n"
    "s_add_u32 s0, s51, 262144\n"
    "buffer_load_dwordx4 v106, s[12:15], s0 offen lds\n"
    "s_add_u32 m0, s37, 40960\n"
    "s_add_u32 s0, s51, 327680\n"
    "buffer_load_dwordx4 v106, s[12:15], s0 offen lds\n"
    "s_add_u32 m0, s37, 49152\n"
    "s_add_u32 s0, s51, 393216\n"
    "buffer_load_dwordx4 v106, s[12:15], s0 offen lds\n"
    "s_add_u32 m0, s37, 57344\n"
    "s_add_u32 s0, s51, 458752\n"
    "buffer_load_dwordx4 v106, s[12:15], s0 offen lds\n"
    "s_lshl_b32 s0, s68, 11\n"
    "s_add_u32 s0, s0, 65536\n"
    "v_add_u32 v34, s0, v109\n"
    "v_xor_b32 v35, 32, v34\n"
    "v_xor_b32 v36, 64, v34\n"
    "v_xor_b32 v37, 96, v34\n"
    "ds_read_b64_tr_b16 v[128:129], v34 offset:0\n"
    "ds_read_b64_tr_b16 v[132:133], v35 offset:0\n"
    "ds_read_b64_tr_b16 v[136:137], v36 offset:0\n"
    "ds_read_b64_tr_b16 v[140:141], v37 offset:0\n"
    "ds_read_b64_tr_b16 v[130:131], v34 offset:8192\n"
    "ds_read_b64_tr_b16 v[134:135], v35 offset:8192\n"
    "ds_read_b64_tr_b16 v[138:139], v36 offset:8192\n"
    "ds_read_b64_tr_b16 v[142:143], v37 offset:8192\n"
    "s_waitcnt lgkmcnt(6)\n"
    "ds_read_b64_tr_b16 v[144:145], v34 offset:16384\n"
    "ds_read_b64_tr_b16 v[148:149], v35 offset:16384\n"
    "ds_read_b64_tr_b16 v[152:153], v36 offset:16384\n"
    "ds_read_b64_tr_b16 v[156:157], v37 offset:16384\n"
    "ds_read_b64_tr_b16 v[146:147], v34 offset:24576\n"
    "ds_read_b64_tr_b16 v[150:151], v35 offset:24576\n"
    "ds_read_b64_tr_b16 v[154:155], v36 offset:24576\n"
    "ds_read_b64_tr_b16 v[158:159], v37 offset:24576\n"
    "s_waitcnt lgkmcnt(6)\n"
    "ds_read_b64_tr_b16 v[160:161], v34 offset:32768\n"
    "ds_read_b64_tr_b16 v[164:165], v35 offset:32768\n"
    "ds_read_b64_tr_b16 v[168:169], v36 offset:32768\n"
    "ds_read_b64_tr_b16 v[172:173], v37 offset:32768\n"
    "ds_read_b64_tr_b16 v[162:163], v34 offset:40960\n"
    "ds_read_b64_tr_b16 v[166:167], v35 offset:40960\n"
    "ds_read_b64_tr_b16 v[170:171], v36 offset:40960\n"
    "ds_read_b64_tr_b16 v[174:175], v37 offset:40960\n"
    "s_waitcnt lgkmcnt(6)\n"
    "ds_read_b64_tr_b16 v[176:177], v34 offset:49152\n"
    "ds_read_b64_tr_b16 v[180:181], v35 offset:49152\n"
    "ds_read_b64_tr_b16 v[184:185], v36 offset:49152\n"
    "ds_read_b64_tr_b16 v[188:189], v37 offset:49152\n"
    "ds_read_b64_tr_b16 v[178:179], v34 offset:57344\n"
    "ds_read_b64_tr_b16 v[182:183], v35 offset:57344\n"
    "ds_read_b64_tr_b16 v[186:187], v36 offset:57344\n"
    "ds_read_b64_tr_b16 v[190:191], v37 offset:57344\n"
    "s_add_u32 s2, s70, s49\n"
    "v_add_u32 v32, s2, v104\n"
    "v_add_u32 v33, 57344, v32\n"
    "s_getpc_b64 s[82:83]\n"
    "Lpc_132_%=:\n"
    "s_mul_i32 s0, s68, Lred_2_%=-Lred_1_%=\n"
    "s_add_u32 s0, s0, Lred_1_%=-Lpc_132_%=\n"
    "s_add_u32 s82, s82, s0\n"
    "s_addc_u32 s83, s83, 0\n"
    "s_mov_b32 s96, 0\n"
    "v_subrev_u32 v34, 65536, v34\n"
    "v_subrev_u32 v35, 65536, v35\n"
    "v_subrev_u32 v36, 65536, v36\n"
    "v_subrev_u32 v37, 65536, v37\n"
    "s_waitcnt lgkmcnt(0)\n"
    "s_barrier\n"
    "s_add_u32 m0, s37, 65536\n"
    "s_add_u32 s0, s51, 786432\n"
    "buffer_load_dwordx4 v106, s[12:15], s0 offen lds\n"
    "s_add_u32 m0, s37, 73728\n"
    "s_add_u32 s0, s51, 851968\n"
    "buffer_load_dwordx4 v106, s[12:15], s0 offen lds\n"
    "s_add_u32 m0, s37, 81920\n"
    "s_add_u32 s0, s51, 917504\n"
    "buffer_load_dwordx4 v106, s[12:15], s0 offen lds\n"
    "s_add_u32 m0, s37, 90112\n"
    "s_add_u32 s0, s51, 983040\n"
    "buffer_load_dwordx4 v106, s[12:15], s0 offen lds\n"
    "s_add_u32 m0, s37, 98304\n"
    "s_add_u32 s0, s51, 1048576\n"
    "buffer_load_dwordx4 v106, s[12:15], s0 offen lds\n"
    "s_add_u32 m0, s37, 106496\n"
    "s_add_u32 s0, s51, 1114112\n"
    "buffer_load_dwordx4 v106, s[12:15], s0 offen lds\n"
    "s_add_u32 m0, s37, 114688\n"
    "s_add_u32 s0, s51, 1179648\n"
    "buffer_load_dwordx4 v106, s[12:15], s0 offen lds\n"
    "s_add_u32 m0, s37, 122880\n"
    "s_add_u32 s0, s51, 1245184\n"
    "buffer_load_dwordx4 v106, s[12:15], s0 offen lds\n"
    "s_cmp_eq_u32 s66, 0\n"
    "s_cbranch_scc0 Lc_137_%=\n"
    "s_and_b32 s0, s48, 0xfffc0000\n"
    "s_add_u32 s0, s0, s70\n"
    "v_bfe_u32 v77, v104, 4, 5\n"
    "v_lshlrev_b32 v77, 11, v77\n"
    "Lh0_135_%=:\n"
    "buffer_load_dword v76, v77, s[4:7], s0 offen sc1\n"
    "s_add_u32 s96, s96, 1\n"
    "s_waitcnt vmcnt(0)\n"
    "v_cmp_eq_u32 vcc, -1, v76\n"
    "s_cbranch_vccz Lh1_136_%=\n"
    "s_sleep 8\n"
    "s_cmp_lt_u32 s96, 65536\n"
    "s_cbranch_scc1 Lh0_135_%=\n"
    "Lh1_136_%=:\n"
    "s_sleep 64\n"
    "Li0_133_%=:\n"
    "buffer_load_dword v76, off, s[4:7], s2 sc1\n"
    "s_add_u32 s96, s96, 1\n"
    "s_waitcnt vmcnt(0)\n"
    "v_cmp_eq_u32 vcc, -1, v76\n"
    "s_cbranch_vccz Li1_134_%=\n"
    "s_sleep 4\n"
    "s_cmp_lt_u32 s96, 65536\n"
    "s_cbranch_scc1 Li0_133_%=\n"
    "Li1_134_%=:\n"
    "s_mov_b32 s96, 0\n"
    "Lc_137_%=:\n"
    "s_mov_b32 s84, 0\n"
    "buffer_load_dwordx4 v[40:43], v32, s[4:7], s84 offen offset:0 sc1\n"
    "buffer_load_dwordx4 v[44:47], v32, s[4:7], s84 offen offset:1024 sc1\n"
    "buffer_load_dwordx4 v[48:51], v33, s[4:7], s84 offen offset:0 sc1\n"
    "buffer_load_dwordx4 v[52:55], v33, s[4:7], s84 offen offset:1024 sc1\n"
    "s_waitcnt vmcnt(4)\n"
    "s_barrier\n"
    "s_mov_b32 s85, 2048\n"
    "buffer_load_dwordx4 v[56:59], v32, s[4:7], s85 offen offset:0 sc1\n"
    "buffer_load_dwordx4 v[60:63], v32, s[4:7], s85 offen offset:1024 sc1\n"
    "buffer_load_dwordx4 v[64:67], v33, s[4:7], s85 offen offset:0 sc1\n"
    "buffer_load_dwordx4 v[68:71], v33, s[4:7], s85 offen offset:1024 sc1\n"
    "s_mov_b32 s86, 0\n"
    "s_mov_b32 s96, 0\n"
    "ds_read_b64_tr_b16 v[88:89], v34 offset:0\n"
    "ds_read_b64_tr_b16 v[90:91], v34 offset:8192\n"
    "ds_read_b64_tr_b16 v[92:93], v34 offset:16384\n"
    "ds_read_b64_tr_b16 v[94:95], v34 offset:24576\n"
    "v_lshlrev_b32 v38, 16, v39\n"
    "v_pk_add_f32 v[116:117], v[116:117], v[38:39] op_sel_hi:[1,0]\n"
    "v_pk_add_f32 v[118:119], v[118:119], v[38:39] op_sel_hi:[1,0]\n"
    "v_pk_add_f32 v[120:121], v[120:121], v[38:39] op_sel_hi:[1,0]\n"
    "v_pk_add_f32 v[122:123], v[122:123], v[38:39] op_sel_hi:[1,0]\n"
    "s_waitcnt vmcnt(4)\n"
    "v_max_u32 v124, v40, v41\n"
    "v_max3_u32 v124, v124, v42, v43\n"
    "v_max3_u32 v124, v124, v44, v45\n"
    "v_max3_u32 v124, v124, v46, v47\n"
    "v_max3_u32 v124, v124, v48, v49\n"
    "v_max3_u32 v124, v124, v50, v51\n"
    "v_max3_u32 v124, v124, v52, v53\n"
    "v_max3_u32 v124, v124, v54, v55\n"
    "v_cmp_eq_u32 vcc, -1, v124\n"
    "s_cbranch_vccnz Lno_138_%=\n"
    "Lok_139_%=:\n"
    "ds_read_b64_tr_b16 v[96:97], v35 offset:0\n"
    "ds_read_b64_tr_b16 v[98:99], v35 offset:8192\n"
    "ds_read_b64_tr_b16 v[100:101], v35 offset:16384\n"
    "ds_read_b64_tr_b16 v[102:103], v35 offset:24576\n"
    "s_waitcnt lgkmcnt(4)\n"
    "v_mfma_f32_16x16x32_bf16 v[0:3], v[40:43], v[88:91], 0\n"
    "v_mfma_f32_16x16x32_bf16 v[0:3], v[44:47], v[92:95], v[0:3]\n"
    "v_mfma_f32_16x16x32_bf16 v[4:7], v[48:51], v[88:91], 0\n"
    "v_mfma_f32_16x16x32_bf16 v[4:7], v[52:55], v[92:95], v[4:7]\n"
    "s_mov_b32 s86, 4096\n"
    "buffer_load_dwordx4 v[72:75], v32, s[4:7], s86 offen offset:0 sc1\n"
    "ds_read_b64_tr_b16 v[88:89], v36 offset:0\n"
    "ds_read_b64_tr_b16 v[90:91], v36 offset:8192\n"
    "ds_read_b64_tr_b16 v[92:93], v36 offset:16384\n"
    "ds_read_b64_tr_b16 v[94:95], v36 offset:24576\n"
    "s_waitcnt lgkmcnt(4)\n"
    "v_mfma_f32_16x16x32_bf16 v[8:11], v[40:43], v[96:99], 0\n"
    "v_mfma_f32_16x16x32_bf16 v[8:11], v[44:47], v[100:103], v[8:11]\n"
    "v_mfma_f32_16x16x32_bf16 v[12:15], v[48:51], v[96:99], 0\n"
    "v_mfma_f32_16x16x32_bf16 v[12:15], v[52:55], v[100:103], v[12:15]\n"
    "buffer_load_dwordx4 v[76:79], v32, s[4:7], s86 offen offset:1024 sc1\n"
    "ds_read_b64_tr_b16 v[96:97], v37 offset:0\n"
    "ds_read_b64_tr_b16 v[98:99], v37 offset:8192\n"
    "ds_read_b64_tr_b16 v[100:101], v37 offset:16384\n"
    "ds_read_b64_tr_b16 v[102:103], v37 offset:24576\n"
    "s_waitcnt lgkmcnt(4)\n"
    "v_mfma_f32_16x16x32_bf16 v[16:19], v[40:43], v[88:91], 0\n"
    "v_mfma_f32_16x16x32_bf16 v[16:19], v[44:47], v[92:95], v[16:19]\n"
    "v_mfma_f32_16x16x32_bf16 v[20:23], v[48:51], v[88:91], 0\n"
    "v_mfma_f32_16x16x32_bf16 v[20:23], v[52:55], v[92:95], v[20:23]\n"
    "buffer_load_dwordx4 v[80:83], v33, s[4:7], s86 offen offset:0 sc1\n"
    "ds_read_b64_tr_b16 v[88:89], v34 offset:32768\n"
    "ds_read_b64_tr_b16 v[90:91], v34 offset:40960\n"
    "ds_read_b64_tr_b16 v[92:93], v34 offset:49152\n"
    "ds_read_b64_tr_b16 v[94:95], v34 offset:57344\n"
    "s_waitcnt lgkmcnt(4)\n"
    "v_mfma_f32_16x16x32_bf16 v[24:27], v[40:43], v[96:99], 0\n"
    "v_mfma_f32_16x16x32_bf16 v[24:27], v[44:47], v[100:103], v[24:27]\n"
    "v_mfma_f32_16x16x32_bf16 v[28:31], v[48:51], v[96:99], 0\n"
    "v_mfma_f32_16x16x32_bf16 v[28:31], v[52:55], v[100:103], v[28:31]\n"
    "buffer_load_dwordx4 v[84:87], v33, s[4:7], s86 offen offset:1024 sc1\n"
    "s_waitcnt vmcnt(4)\n"
    "v_max_u32 v124, v56, v57\n"
    "v_max3_u32 v124, v124, v58, v59\n"
    "v_max3_u32 v124, v124, v60, v61\n"
    "v_max3_u32 v124, v124, v62, v63\n"
    "v_max3_u32 v124, v124, v64, v65\n"
    "v_max3_u32 v124, v124, v66, v67\n"
    "v_max3_u32 v124, v124, v68, v69\n"
    "v_max3_u32 v124, v124, v70, v71\n"
    "v_cmp_eq_u32 vcc, -1, v124\n"
    "s_cbranch_vccnz Lno_140_%=\n"
    "Lok_141_%=:\n"
    "ds_read_b64_tr_b16 v[96:97], v35 offset:32768\n"
    "ds_read_b64_tr_b16 v[98:99], v35 offset:40960\n"
    "ds_read_b64_tr_b16 v[100:101], v35 offset:49152\n"
    "ds_read_b64_tr_b16 v[102:103], v35 offset:57344\n"
    "s_waitcnt lgkmcnt(4)\n"
    "v_mfma_f32_16x16x32_bf16 v[0:3], v[56:59], v[88:91], v[0:3]\n"
    "v_mfma_f32_16x16x32_bf16 v[0:3], v[60:63], v[92:95], v[0:3]\n"
    "v_mfma_f32_16x16x32_bf16 v[4:7], v[64:67], v[88:91], v[4:7]\n"
    "v_mfma_f32_16x16x32_bf16 v[4:7], v[68:71], v[92:95], v[4:7]\n"
    "s_mov_b32 s84, 6144\n"
    "buffer_load_dwordx4 v[40:43], v32, s[4:7], s84 offen offset:0 sc1\n"
    "ds_read_b64_tr_b16 v[88:89], v36 offset:32768\n"
    "ds_read_b64_tr_b16 v[90:91], v36 offset:40960\n"
    "ds_read_b64_tr_b16 v[92:93], v36 offset:49152\n"
    "ds_read_b64_tr_b16 v[94:95], v36 offset:57344\n"
    "s_waitcnt lgkmcnt(4)\n"
    "v_mfma_f32_16x16x32_bf16 v[8:11], v[56:59], v[96:99], v[8:11]\n"
    "v_mfma_f32_16x16x32_bf16 v[8:11], v[60:63], v[100:103], v[8:11]\n"
    "v_mfma_f32_16x16x32_bf16 v[12:15], v[64:67], v[96:99], v[12:15]\n"
    "v_mfma_f32_16x16x32_bf16 v[12:15], v[68:71], v[100:103], v[12:15]\n"
    "buffer_load_dwordx4 v[44:47], v32, s[4:7], s84 offen offset:1024 sc1\n"
    "ds_read_b64_tr_b16 v[96:97], v37 offset:32768\n"
    "ds_read_b64_tr_b16 v[98:99], v37 offset:40960\n"
    "ds_read_b64_tr_b16 v[100:101], v37 offset:49152\n"
    "ds_read_b64_tr_b16 v[102:103], v37 offset:57344\n"
    "s_waitcnt lgkmcnt(4)\n"
    "v_mfma_f32_16x16x32_bf16 v[16:19], v[56:59], v[88:91], v[16:19]\n"
    "v_mfma_f32_16x16x32_bf16 v[16:19], v[60:63], v[92:95], v[16:19]\n"
    "v_mfma_f32_16x16x32_bf16 v[20:23], v[64:67], v[88:91], v[20:23]\n"
    "v_mfma_f32_16x16x32_bf16 v[20:23], v[68:71], v[92:95], v[20:23]\n"
    "buffer_load_dwordx4 v[48:51], v33, s[4:7], s84 offen offset:0 sc1\n"
    "v_add_u32 v34, 131072, v34\n"
    "v_add_u32 v35, 131072, v35\n"
    "v_add_u32 v36, 131072, v36\n"
    "v_add_u32 v37, 131072, v37\n"
    "ds_read_b64_tr_b16 v[88:89], v34 offset:0\n"
    "ds_read_b64_tr_b16 v[90:91], v34 offset:8192\n"
    "ds_read_b64_tr_b16 v[92:93], v34 offset:16384\n"
    "ds_read_b64_tr_b16 v[94:95], v34 offset:24576\n"
    "s_waitcnt lgkmcnt(4)\n"
    "v_mfma_f32_16x16x32_bf16 v[24:27], v[56:59], v[96:99], v[24:27]\n"
    "v_mfma_f32_16x16x32_bf16 v[24:27], v[60:63], v[100:103], v[24:27]\n"
    "v_mfma_f32_16x16x32_bf16 v[28:31], v[64:67], v[96:99], v[28:31]\n"
    "v_mfma_f32_16x16x32_bf16 v[28:31], v[68:71], v[100:103], v[28:31]\n"
    "buffer_load_dwordx4 v[52:55], v33, s[4:7], s84 offen offset:1024 sc1\n"
    "s_waitcnt vmcnt(4)\n"
    "v_max_u32 v124, v72, v73\n"
    "v_max3_u32 v124, v124, v74, v75\n"
    "v_max3_u32 v124, v124, v76, v77\n"
    "v_max3_u32 v124, v124, v78, v79\n"
    "v_max3_u32 v124, v124, v80, v81\n"
    "v_max3_u32 v124, v124, v82, v83\n"
    "v_max3_u32 v124, v124, v84, v85\n"
    "v_max3_u32 v124, v124, v86, v87\n"
    "v_cmp_eq_u32 vcc, -1, v124\n"
    "s_cbranch_vccnz Lno_142_%=\n"
    "Lok_143_%=:\n"
    "ds_read_b64_tr_b16 v[96:97], v35 offset:0\n"
    "ds_read_b64_tr_b16 v[98:99], v35 offset:8192\n"
    "ds_read_b64_tr_b16 v[100:101], v35 offset:16384\n"
    "ds_read_b64_tr_b16 v[102:103], v35 offset:24576\n"
    "s_waitcnt lgkmcnt(4)\n"
    "v_mfma_f32_16x16x32_bf16 v[0:3], v[72:75], v[88:91], v[0:3]\n"
    "v_mfma_f32_16x16x32_bf16 v[0:3], v[76:79], v[92:95], v[0:3]\n"
    "v_mfma_f32_16x16x32_bf16 v[4:7], v[80:83], v[88:91], v[4:7]\n"
    "v_mfma_f32_16x16x32_bf16 v[4:7], v[84:87], v[92:95], v[4:7]\n"
    "s_mov_b32 s85, 8192\n"
    "buffer_load_dwordx4 v[56:59], v32, s[4:7], s85 offen offset:0 sc1\n"
    "ds_read_b64_tr_b16 v[88:89], v36 offset:0\n"
    "ds_read_b64_tr_b16 v[90:91], v36 offset:8192\n"
    "ds_read_b64_tr_b16 v[92:93], v36 offset:16384\n"
    "ds_read_b64_tr_b16 v[94:95], v36 offset:24576\n"
    "s_waitcnt lgkmcnt(4)\n"
    "v_mfma_f32_16x16x32_bf16 v[8:11], v[72:75], v[96:99], v[8:11]\n"
    "v_mfma_f32_16x16x32_bf16 v[8:11], v[76:79], v[100:103], v[8:11]\n"
    "v_mfma_f32_16x16x32_bf16 v[12:15], v[80:83], v[96:99], v[12:15]\n"
    "v_mfma_f32_16x16x32_bf16 v[12:15], v[84:87], v[100:103], v[12:15]\n"
    "buffer_load_dwordx4 v[60:63], v32, s[4:7], s85 offen offset:1024 sc1\n"
    "ds_read_b64_tr_b16 v[96:97], v37 offset:0\n"
    "ds_read_b64_tr_b16 v[98:99], v37 offset:8192\n"
    "ds_read_b64_tr_b16 v[100:101], v37 offset:16384\n"
    "ds_read_b64_tr_b16 v[102:103], v37 offset:24576\n"
    "s_waitcnt lgkmcnt(4)\n"
    "v_mfma_f32_16x16x32_bf16 v[16:19], v[72:75], v[88:91], v[16:19]\n"
    "v_mfma_f32_16x16x32_bf16 v[16:19], v[76:79], v[92:95], v[16:19]\n"
    "v_mfma_f32_16x16x32_bf16 v[20:23], v[80:83], v[88:91], v[20:23]\n"
    "v_mfma_f32_16x16x32_bf16 v[20:23], v[84:87], v[92:95], v[20:23]\n"
    "buffer_load_dwordx4 v[64:67], v33, s[4:7], s85 offen offset:0 sc1\n"
    "v_subrev_u32 v34, 65536, v34\n"
    "v_subrev_u32 v35, 65536, v35\n"
    "v_subrev_u32 v36, 65536, v36\n"
    "v_subrev_u32 v37, 65536, v37\n"
    "ds_read_b64_tr_b16 v[88:89], v34 offset:0\n"
    "ds_read_b64_tr_b16 v[90:91], v34 offset:8192\n"
    "ds_read_b64_tr_b16 v[92:93], v34 offset:16384\n"
    "ds_read_b64_tr_b16 v[94:95], v34 offset:24576\n"
    "s_waitcnt lgkmcnt(4)\n"
    "v_mfma_f32_16x16x32_bf16 v[24:27], v[72:75], v[96:99], v[24:27]\n"
    "v_mfma_f32_16x16x32_bf16 v[24:27], v[76:79], v[100:103], v[24:27]\n"
    "v_mfma_f32_16x16x32_bf16 v[28:31], v[80:83], v[96:99], v[28:31]\n"
    "v_mfma_f32_16x16x32_bf16 v[28:31], v[84:87], v[100:103], v[28:31]\n"
    "buffer_load_dwordx4 v[68:71], v33, s[4:7], s85 offen offset:1024 sc1\n"
    "s_waitcnt vmcnt(4)\n"
    "v_max_u32 v124, v40, v41\n"
    "v_max3_u32 v124, v124, v42, v43\n"
    "v_max3_u32 v124, v124, v44, v45\n"
    "v_max3_u32 v124, v124, v46, v47\n"
    "v_max3_u32 v124, v124, v48, v49\n"
    "v_max3_u32 v124, v124, v50, v51\n"
    "v_max3_u32 v124, v124, v52, v53\n"
    "v_max3_u32 v124, v124, v54, v55\n"
    "v_cmp_eq_u32 vcc, -1, v124\n"
    "s_cbranch_vccnz Lno_144_%=\n"
    "Lok_145_%=:\n"
    "ds_read_b64_tr_b16 v[96:97], v35 offset:0\n"
    "ds_read_b64_tr_b16 v[98:99], v35 offset:8192\n"
    "ds_read_b64_tr_b16 v[100:101], v35 offset:16384\n"
    "ds_read_b64_tr_b16 v[102:103], v35 offset:24576\n"
    "s_waitcnt lgkmcnt(4)\n"
    "v_mfma_f32_16x16x32_bf16 v[0:3], v[40:43], v[88:91], v[0:3]\n"
    "v_mfma_f32_16x16x32_bf16 v[0:3], v[44:47], v[92:95], v[0:3]\n"
    "v_mfma_f32_16x16x32_bf16 v[4:7], v[48:51], v[88:91], v[4:7]\n"
    "v_mfma_f32_16x16x32_bf16 v[4:7], v[52:55], v[92:95], v[4:7]\n"
    "s_mov_b32 s86, 10240\n"
    "buffer_load_dwordx4 v[72:75], v32, s[4:7], s86 offen offset:0 sc1\n"
    "ds_read_b64_tr_b16 v[88:89], v36 offset:0\n"
    "ds_read_b64_tr_b16 v[90:91], v36 offset:8192\n"
    "ds_read_b64_tr_b16 v[92:93], v36 offset:16384\n"
    "ds_read_b64_tr_b16 v[94:95], v36 offset:24576\n"
    "s_waitcnt lgkmcnt(4)\n"
    "v_mfma_f32_16x16x32_bf16 v[8:11], v[40:43], v[96:99], v[8:11]\n"
    "v_mfma_f32_16x16x32_bf16 v[8:11], v[44:47], v[100:103], v[8:11]\n"
    "v_mfma_f32_16x16x32_bf16 v[12:15], v[48:51], v[96:99], v[12:15]\n"
    "v_mfma_f32_16x16x32_bf16 v[12:15], v[52:55], v[100:103], v[12:15]\n"
    "buffer_load_dwordx4 v[76:79], v32, s[4:7], s86 offen offset:1024 sc1\n"
    "ds_read_b64_tr_b16 v[96:97], v37 offset:0\n"
    "ds_read_b64_tr_b16 v[98:99], v37 offset:8192\n"
    "ds_read_b64_tr_b16 v[100:101], v37 offset:16384\n"
    "ds_read_b64_tr_b16 v[102:103], v37 offset:24576\n"
    "s_waitcnt lgkmcnt(4)\n"
    "v_mfma_f32_16x16x32_bf16 v[16:19], v[40:43], v[88:91], v[16:19]\n"
    "v_mfma_f32_16x16x32_bf16 v[16:19], v[44:47], v[92:95], v[16:19]\n"
    "v_mfma_f32_16x16x32_bf16 v[20:23], v[48:51], v[88:91], v[20:23]\n"
    "v_mfma_f32_16x16x32_bf16 v[20:23], v[52:55], v[92:95], v[20:23]\n"
    "buffer_load_dwordx4 v[80:83], v33, s[4:7], s86 offen offset:0 sc1\n"
    "ds_read_b64_tr_b16 v[88:89], v34 offset:32768\n"
    "ds_read_b64_tr_b16 v[90:91], v34 offset:40960\n"
    "ds_read_b64_tr_b16 v[92:93], v34 offset:49152\n"
    "ds_read_b64_tr_b16 v[94:95], v34 offset:57344\n"
    "s_waitcnt lgkmcnt(4)\n"
    "v_mfma_f32_16x16x32_bf16 v[24:27], v[40:43], v[96:99], v[24:27]\n"
    "v_mfma_f32_16x16x32_bf16 v[24:27], v[44:47], v[100:103], v[24:27]\n"
    "v_mfma_f32_16x16x32_bf16 v[28:31], v[48:51], v[96:99], v[28:31]\n"
    "v_mfma_f32_16x16x32_bf16 v[28:31], v[52:55], v[100:103], v[28:31]\n"
    "buffer_load_dwordx4 v[84:87], v33, s[4:7], s86 offen offset:1024 sc1\n"
    "s_waitcnt vmcnt(4)\n"
    "v_max_u32 v124, v56, v57\n"
    "v_max3_u32 v124, v124, v58, v59\n"
    "v_max3_u32 v124, v124, v60, v61\n"
    "v_max3_u32 v124, v124, v62, v63\n"
    "v_max3_u32 v124, v124, v64, v65\n"
    "v_max3_u32 v124, v124, v66, v67\n"
    "v_max3_u32 v124, v124, v68, v69\n"
    "v_max3_u32 v124, v124, v70, v71\n"
    "v_cmp_eq_u32 vcc, -1, v124\n"
    "s_cbranch_vccnz Lno_146_%=\n"
    "Lok_147_%=:\n"
    "ds_read_b64_tr_b16 v[96:97], v35 offset:32768\n"
    "ds_read_b64_tr_b16 v[98:99], v35 offset:40960\n"
    "ds_read_b64_tr_b16 v[100:101], v35 offset:49152\n"
    "ds_read_b64_tr_b16 v[102:103], v35 offset:57344\n"
    "s_waitcnt lgkmcnt(4)\n"
    "v_mfma_f32_16x16x32_bf16 v[0:3], v[56:59], v[88:91], v[0:3]\n"
    "v_mfma_f32_16x16x32_bf16 v[0:3], v[60:63], v[92:95], v[0:3]\n"
    "v_mfma_f32_16x16x32_bf16 v[4:7], v[64:67], v[88:91], v[4:7]\n"
    "v_mfma_f32_16x16x32_bf16 v[4:7], v[68:71], v[92:95], v[4:7]\n"
    "s_mov_b32 s84, 12288\n"
    "buffer_load_dwordx4 v[40:43], v32, s[4:7], s84 offen offset:0 sc1\n"
    "ds_read_b64_tr_b16 v[88:89], v36 offset:32768\n"
    "ds_read_b64_tr_b16 v[90:91], v36 offset:40960\n"
    "ds_read_b64_tr_b16 v[92:93], v36 offset:49152\n"
    "ds_read_b64_tr_b16 v[94:95], v36 offset:57344\n"
    "s_waitcnt lgkmcnt(4)\n"
    "v_mfma_f32_16x16x32_bf16 v[8:11], v[56:59], v[96:99], v[8:11]\n"
    "v_mfma_f32_16x16x32_bf16 v[8:11], v[60:63], v[100:103], v[8:11]\n"
    "v_mfma_f32_16x16x32_bf16 v[12:15], v[64:67], v[96:99], v[12:15]\n"
    "v_mfma_f32_16x16x32_bf16 v[12:15], v[68:71], v[100:103], v[12:15]\n"
    "buffer_load_dwordx4 v[44:47], v32, s[4:7], s84 offen offset:1024 sc1\n"
    "ds_read_b64_tr_b16 v[96:97], v37 offset:32768\n"
    "ds_read_b64_tr_b16 v[98:99], v37 offset:40960\n"
    "ds_read_b64_tr_b16 v[100:101], v37 offset:49152\n"
    "ds_read_b64_tr_b16 v[102:103], v37 offset:57344\n"
    "s_waitcnt lgkmcnt(4)\n"
    "v_mfma_f32_16x16x32_bf16 v[16:19], v[56:59], v[88:91], v[16:19]\n"
    "v_mfma_f32_16x16x32_bf16 v[16:19], v[60:63], v[92:95], v[16:19]\n"
    "v_mfma_f32_16x16x32_bf16 v[20:23], v[64:67], v[88:91], v[20:23]\n"
    "v_mfma_f32_16x16x32_bf16 v[20:23], v[68:71], v[92:95], v[20:23]\n"
    "buffer_load_dwordx4 v[48:51], v33, s[4:7], s84 offen offset:0 sc1\n"
    "s_waitcnt lgkmcnt(0)\n"
    "v_mfma_f32_16x16x32_bf16 v[24:27], v[56:59], v[96:99], v[24:27]\n"
    "v_mfma_f32_16x16x32_bf16 v[24:27], v[60:63], v[100:103], v[24:27]\n"
    "v_mfma_f32_16x16x32_bf16 v[28:31], v[64:67], v[96:99], v[28:31]\n"
    "v_mfma_f32_16x16x32_bf16 v[28:31], v[68:71], v[100:103], v[28:31]\n"
    "buffer_load_dwordx4 v[52:55], v33, s[4:7], s84 offen offset:1024 sc1\n"
    "s_waitcnt vmcnt(4)\n"
    "v_max_u32 v124, v72, v73\n"
    "v_max3_u32 v124, v124, v74, v75\n"
    "v_max3_u32 v124, v124, v76, v77\n"
    "v_max3_u32 v124, v124, v78, v79\n"
    "v_max3_u32 v124, v124, v80, v81\n"
    "v_max3_u32 v124, v124, v82, v83\n"
    "v_max3_u32 v124, v124, v84, v85\n"
    "v_max3_u32 v124, v124, v86, v87\n"
    "v_cmp_eq_u32 vcc, -1, v124\n"
    "s_cbranch_vccnz Lno_148_%=\n"
    "Lok_149_%=:\n"
    "s_barrier\n"
    "s_lshl_b32 s1, s37, 2\n"
    "s_and_b32 s1, s1, 0x2000\n"
    "s_lshl_b32 s0, s68, 14\n"
    "s_add_u32 s0, s0, s1\n"
    "v_add_u32 v34, s0, v104\n"
    "s_lshl_b32 s0, s68, 11\n"
    "s_add_u32 s0, s0, s1\n"
    "v_add_u32 v35, s0, v104\n"
    "s_cmp_eq_u32 s71, 0\n"
    "s_cbranch_scc0 Lt1_150_%=\n"
    "v_mfma_f32_16x16x32_bf16 v[0:3], v[72:75], v[128:131], v[0:3]\n"
    "v_mfma_f32_16x16x32_bf16 v[0:3], v[76:79], v[144:147], v[0:3]\n"
    "v_mfma_f32_16x16x32_bf16 v[4:7], v[80:83], v[128:131], v[4:7]\n"
    "v_mfma_f32_16x16x32_bf16 v[4:7], v[84:87], v[144:147], v[4:7]\n"
    "s_lshl_b32 s1, s37, 2\n"
    "s_add_u32 m0, s1, 131072\n"
    "s_add_u32 s0, s54, 65536\n"
    "buffer_load_dwordx4 v104, s[4:7], s0 offen offset:0 sc1 lds\n"
    "v_mfma_f32_16x16x32_bf16 v[8:11], v[72:75], v[132:135], v[8:11]\n"
    "v_mfma_f32_16x16x32_bf16 v[8:11], v[76:79], v[148:151], v[8:11]\n"
    "v_mfma_f32_16x16x32_bf16 v[12:15], v[80:83], v[132:135], v[12:15]\n"
    "v_mfma_f32_16x16x32_bf16 v[12:15], v[84:87], v[148:151], v[12:15]\n"
    "buffer_load_dwordx4 v104, s[4:7], s0 offen offset:1024 sc1 lds\n"
    "v_mfma_f32_16x16x32_bf16 v[16:19], v[72:75], v[136:139], v[16:19]\n"
    "v_mfma_f32_16x16x32_bf16 v[16:19], v[76:79], v[152:155], v[16:19]\n"
    "v_mfma_f32_16x16x32_bf16 v[20:23], v[80:83], v[136:139], v[20:23]\n"
    "v_mfma_f32_16x16x32_bf16 v[20:23], v[84:87], v[152:155], v[20:23]\n"
    "buffer_load_dwordx4 v104, s[4:7], s0 offen offset:2048 sc1 lds\n"
    "v_mfma_f32_16x16x32_bf16 v[24:27], v[72:75], v[140:143], v[24:27]\n"
    "v_mfma_f32_16x16x32_bf16 v[24:27], v[76:79], v[156:159], v[24:27]\n"
    "v_mfma_f32_16x16x32_bf16 v[28:31], v[80:83], v[140:143], v[28:31]\n"
    "v_mfma_f32_16x16x32_bf16 v[28:31], v[84:87], v[156:159], v[28:31]\n"
    "buffer_load_dwordx4 v104, s[4:7], s0 offen offset:3072 sc1 lds\n"
    "s_waitcnt vmcnt(4)\n"
    "v_max_u32 v124, v40, v41\n"
    "v_max3_u32 v124, v124, v42, v43\n"
    "v_max3_u32 v124, v124, v44, v45\n"
    "v_max3_u32 v124, v124, v46, v47\n"
    "v_max3_u32 v124, v124, v48, v49\n"
    "v_max3_u32 v124, v124, v50, v51\n"
    "v_max3_u32 v124, v124, v52, v53\n"
    "v_max3_u32 v124, v124, v54, v55\n"
    "v_cmp_eq_u32 vcc, -1, v124\n"
    "s_cbranch_vccnz Lno_152_%=\n"
    "Lok_153_%=:\n"
    "v_mfma_f32_16x16x32_bf16 v[0:3], v[40:43], v[160:163], v[0:3]\n"
    "v_mfma_f32_16x16x32_bf16 v[0:3], v[44:47], v[176:179], v[0:3]\n"
    "v_mfma_f32_16x16x32_bf16 v[4:7], v[48:51], v[160:163], v[4:7]\n"
    "v_mfma_f32_16x16x32_bf16 v[4:7], v[52:55], v[176:179], v[4:7]\n"
    "s_lshl_b32 s1, s37, 2\n"
    "s_add_u32 m0, s1, 98304\n"
    "s_add_u32 s0, s54, 32768\n"
    "buffer_load_dwordx4 v104, s[4:7], s0 offen offset:0 sc1 lds\n"
    "v_mfma_f32_16x16x32_bf16 v[8:11], v[40:43], v[164:167], v[8:11]\n"
    "v_mfma_f32_16x16x32_bf16 v[8:11], v[44:47], v[180:183], v[8:11]\n"
    "v_mfma_f32_16x16x32_bf16 v[12:15], v[48:51], v[164:167], v[12:15]\n"
    "v_mfma_f32_16x16x32_bf16 v[12:15], v[52:55], v[180:183], v[12:15]\n"
    "s_cmp_eq_u32 s68, 0\n"
    "s_cbranch_scc1 Lrs_154_%=\n"
    "ds_write_b128 v34, v[0:3] offset:0\n"
    "ds_write_b128 v34, v[4:7] offset:1024\n"
    "Lrs_154_%=:\n"
    "buffer_load_dwordx4 v104, s[4:7], s0 offen offset:1024 sc1 lds\n"
    "v_mfma_f32_16x16x32_bf16 v[16:19], v[40:43], v[168:171], v[16:19]\n"
    "v_mfma_f32_16x16x32_bf16 v[16:19], v[44:47], v[184:187], v[16:19]\n"
    "v_mfma_f32_16x16x32_bf16 v[20:23], v[48:51], v[168:171], v[20:23]\n"
    "v_mfma_f32_16x16x32_bf16 v[20:23], v[52:55], v[184:187], v[20:23]\n"
    "s_cmp_eq_u32 s68, 1\n"
    "s_cbranch_scc1 Lrs_155_%=\n"
    "ds_write_b128 v34, v[8:11] offset:2048\n"
    "ds_write_b128 v34, v[12:15] offset:3072\n"
    "Lrs_155_%=:\n"
    "buffer_load_dwordx4 v104, s[4:7], s0 offen offset:2048 sc1 lds\n"
    "v_mfma_f32_16x16x32_bf16 v[24:27], v[40:43], v[172:175], v[24:27]\n"
    "v_mfma_f32_16x16x32_bf16 v[24:27], v[44:47], v[188:191], v[24:27]\n"
    "v_mfma_f32_16x16x32_bf16 v[28:31], v[48:51], v[172:175], v[28:31]\n"
    "v_mfma_f32_16x16x32_bf16 v[28:31], v[52:55], v[188:191], v[28:31]\n"
    "s_cmp_eq_u32 s68, 2\n"
    "s_cbranch_scc1 Lrs_156_%=\n"
    "ds_write_b128 v34, v[16:19] offset:4096\n"
    "ds_write_b128 v34, v[20:23] offset:5120\n"
    "Lrs_156_%=:\n"
    "buffer_load_dwordx4 v104, s[4:7], s0 offen offset:3072 sc1 lds\n"
    "s_cmp_eq_u32 s36, 7\n"
    "s_cbranch_scc0 Lw_157_%=\n"
    "s_mov_b32 m0, 55296\n"
    "s_nop 0\n"
    "buffer_load_dwordx4 v104, s[4:7], s63 offen sc1 lds\n"
    "Lw_157_%=:\n"
    "s_branch Lt2_151_%=\n"
    "Lt1_150_%=:\n"
    "v_mfma_f32_16x16x32_bf16 v[0:3], v[72:75], v[128:131], v[0:3]\n"
    "v_mfma_f32_16x16x32_bf16 v[0:3], v[76:79], v[144:147], v[0:3]\n"
    "v_mfma_f32_16x16x32_bf16 v[4:7], v[80:83], v[128:131], v[4:7]\n"
    "v_mfma_f32_16x16x32_bf16 v[4:7], v[84:87], v[144:147], v[4:7]\n"
    "v_mfma_f32_16x16x32_bf16 v[8:11], v[72:75], v[132:135], v[8:11]\n"
    "v_mfma_f32_16x16x32_bf16 v[8:11], v[76:79], v[148:151], v[8:11]\n"
    "v_mfma_f32_16x16x32_bf16 v[12:15], v[80:83], v[132:135], v[12:15]\n"
    "v_mfma_f32_16x16x32_bf16 v[12:15], v[84:87], v[148:151], v[12:15]\n"
    "v_mfma_f32_16x16x32_bf16 v[16:19], v[72:75], v[136:139], v[16:19]\n"
    "v_mfma_f32_16x16x32_bf16 v[16:19], v[76:79], v[152:155], v[16:19]\n"
    "v_mfma_f32_16x16x32_bf16 v[20:23], v[80:83], v[136:139], v[20:23]\n"
    "v_mfma_f32_16x16x32_bf16 v[20:23], v[84:87], v[152:155], v[20:23]\n"
    "v_mfma_f32_16x16x32_bf16 v[24:27], v[72:75], v[140:143], v[24:27]\n"
    "v_mfma_f32_16x16x32_bf16 v[24:27], v[76:79], v[156:159], v[24:27]\n"
    "v_mfma_f32_16x16x32_bf16 v[28:31], v[80:83], v[140:143], v[28:31]\n"
    "v_mfma_f32_16x16x32_bf16 v[28:31], v[84:87], v[156:159], v[28:31]\n"
    "s_waitcnt vmcnt(0)\n"
    "v_max_u32 v124, v40, v41\n"
    "v_max3_u32 v124, v124, v42, v43\n"
    "v_max3_u32 v124, v124, v44, v45\n"
    "v_max3_u32 v124, v124, v46, v47\n"
    "v_max3_u32 v124, v124, v48, v49\n"
    "v_max3_u32 v124, v124, v50, v51\n"
    "v_max3_u32 v124, v124, v52, v53\n"
    "v_max3_u32 v124, v124, v54, v55\n"
    "v_cmp_eq_u32 vcc, -1, v124\n"
    "s_cbranch_vccnz Lno_158_%=\n"
    "Lok_159_%=:\n"
    "v_mfma_f32_16x16x32_bf16 v[0:3], v[40:43], v[160:163], v[0:3]\n"
    "v_mfma_f32_16x16x32_bf16 v[0:3], v[44:47], v[176:179], v[0:3]\n"
    "v_mfma_f32_16x16x32_bf16 v[4:7], v[48:51], v[160:163], v[4:7]\n"
    "v_mfma_f32_16x16x32_bf16 v[4:7], v[52:55], v[176:179], v[4:7]\n"
    "v_mfma_f32_16x16x32_bf16 v[8:11], v[40:43], v[164:167], v[8:11]\n"
    "v_mfma_f32_16x16x32_bf16 v[8:11], v[44:47], v[180:183], v[8:11]\n"
    "v_mfma_f32_16x16x32_bf16 v[12:15], v[48:51], v[164:167], v[12:15]\n"
    "v_mfma_f32_16x16x32_bf16 v[12:15], v[52:55], v[180:183], v[12:15]\n"
    "s_cmp_eq_u32 s68, 0\n"
    "s_cbranch_scc1 Lrs_160_%=\n"
    "ds_write_b128 v34, v[0:3] offset:0\n"
    "s_nop 0\n"
    "ds_write_b128 v34, v[4:7] offset:1024\n"
    "Lrs_160_%=:\n"
    "v_mfma_f32_16x16x32_bf16 v[16:19], v[40:43], v[168:171], v[16:19]\n"
    "v_mfma_f32_16x16x32_bf16 v[16:19], v[44:47], v[184:187], v[16:19]\n"
    "v_mfma_f32_16x16x32_bf16 v[20:23], v[48:51], v[168:171], v[20:23]\n"
    "v_mfma_f32_16x16x32_bf16 v[20:23], v[52:55], v[184:187], v[20:23]\n"
    "s_cmp_eq_u32 s68, 1\n"
    "s_cbranch_scc1 Lrs_161_%=\n"
    "ds_write_b128 v34, v[8:11] offset:2048\n"
    "ds_write_b128 v34, v[12:15] offset:3072\n"
    "Lrs_161_%=:\n"
    "v_mfma_f32_16x16x32_bf16 v[24:27], v[40:43], v[172:175], v[24:27]\n"
    "v_mfma_f32_16x16x32_bf16 v[24:27], v[44:47], v[188:191], v[24:27]\n"
    "v_mfma_f32_16x16x32_bf16 v[28:31], v[48:51], v[172:175], v[28:31]\n"
    "v_mfma_f32_16x16x32_bf16 v[28:31], v[52:55], v[188:191], v[28:31]\n"
    "s_cmp_eq_u32 s68, 2\n"
    "s_cbranch_scc1 Lrs_162_%=\n"
    "ds_write_b128 v34, v[16:19] offset:4096\n"
    "ds_write_b128 v34, v[20:23] offset:5120\n"
    "Lrs_162_%=:\n"
    "Lt2_151_%=:\n"
    "s_nop 6\n"
    "s_cmp_eq_u32 s68, 3\n"
    "s_cbranch_scc1 Lrs_163_%=\n"
    "ds_write_b128 v34, v[24:27] offset:6144\n"
    "ds_write_b128 v34, v[28:31] offset:7168\n"
    "Lrs_163_%=:\n"
    "s_swappc_b64 s[98:99], s[82:83]\n"
    "v_pk_add_f32 v[116:117], v[116:117], v[88:89]\n"
    "v_pk_add_f32 v[118:119], v[118:119], v[90:91]\n"
    "v_pk_add_f32 v[120:121], v[120:121], v[92:93]\n"
    "v_pk_add_f32 v[122:123], v[122:123], v[94:95]\n"
    "s_cmp_eq_u32 s71, 0\n"
    "s_cbranch_scc0 Lend_164_%=\n"
    "s_mov_b32 s71, 1\n"
    "s_add_u32 s70, s70, 12582912\n"
    "s_mov_b32 s78, 16\n"
    "s_branch Lloop_8_%=\n"
    "Lend_164_%=:\n"
    "v_cvt_pk_bf16_f32 v0, v116, v117\n"
    "v_cvt_pk_bf16_f32 v1, v118, v119\n"
    "v_cvt_pk_bf16_f32 v2, v120, v121\n"
    "v_cvt_pk_bf16_f32 v3, v122, v123\n"
    "v_and_b32 v5, 1, v115\n"
    "v_lshlrev_b32 v4, 10, v114\n"
    "v_lshl_add_u32 v4, v5, 5, v4\n"
    "v_lshrrev_b32 v5, 1, v115\n"
    "v_lshl_add_u32 v4, v5, 4, v4\n"
    "v_permlane16_swap_b32 v0, v2\n"
    "v_permlane16_swap_b32 v1, v3\n"
    "buffer_store_dwordx4 v[0:3], v4, s[32:35], s74 offen\n"
    "s_cmp_eq_u32 s75, 0\n"
    "s_cbranch_scc1 Lc_165_%=\n"
    "s_mov_b32 s3, 0\n"
    "v_mov_b32 v68, s72\n"
    "v_mov_b32 v69, s73\n"
    "s_mov_b64 exec, 1\n"
    "buffer_store_dwordx2 v[68:69], off, s[4:7], s3 sc1\n"
    "s_mov_b64 exec, -1\n"
    "Lc_165_%=:\n"
    "s_branch Lfin_166_%=\n"
    "Lred_1_%=:\n"
    "s_waitcnt lgkmcnt(0)\n"
    "s_barrier\n"
    "ds_read_b128 v[40:43], v35 offset:16384\n"
    "ds_read_b128 v[44:47], v35 offset:17408\n"
    "ds_read_b128 v[48:51], v35 offset:32768\n"
    "ds_read_b128 v[52:55], v35 offset:33792\n"
    "ds_read_b128 v[56:59], v35 offset:49152\n"
    "ds_read_b128 v[60:63], v35 offset:50176\n"
    "s_waitcnt lgkmcnt(5)\n"
    "v_pk_add_f32 v[88:89], v[0:1], v[40:41]\n"
    "v_pk_add_f32 v[90:91], v[2:3], v[42:43]\n"
    "s_waitcnt lgkmcnt(4)\n"
    "v_pk_add_f32 v[92:93], v[4:5], v[44:45]\n"
    "v_pk_add_f32 v[94:95], v[6:7], v[46:47]\n"
    "s_waitcnt lgkmcnt(3)\n"
    "v_pk_add_f32 v[88:89], v[88:89], v[48:49]\n"
    "v_pk_add_f32 v[90:91], v[90:91], v[50:51]\n"
    "s_waitcnt lgkmcnt(2)\n"
    "v_pk_add_f32 v[92:93], v[92:93], v[52:53]\n"
    "v_pk_add_f32 v[94:95], v[94:95], v[54:55]\n"
    "s_waitcnt lgkmcnt(1)\n"
    "v_pk_add_f32 v[88:89], v[88:89], v[56:57]\n"
    "v_pk_add_f32 v[90:91], v[90:91], v[58:59]\n"
    "s_waitcnt lgkmcnt(0)\n"
    "v_pk_add_f32 v[92:93], v[92:93], v[60:61]\n"
    "v_pk_add_f32 v[94:95], v[94:95], v[62:63]\n"
    "s_setpc_b64 s[98:99]\n"
    "Lred_2_%=:\n"
    "s_waitcnt lgkmcnt(0)\n"
    "s_barrier\n"
    "ds_read_b128 v[40:43], v35 offset:0\n"
    "ds_read_b128 v[44:47], v35 offset:1024\n"
    "ds_read_b128 v[48:51], v35 offset:32768\n"
    "ds_read_b128 v[52:55], v35 offset:33792\n"
    "ds_read_b128 v[56:59], v35 offset:49152\n"
    "ds_read_b128 v[60:63], v35 offset:50176\n"
    "s_waitcnt lgkmcnt(5)\n"
    "v_pk_add_f32 v[88:89], v[8:9], v[40:41]\n"
    "v_pk_add_f32 v[90:91], v[10:11], v[42:43]\n"
    "s_waitcnt lgkmcnt(4)\n"
    "v_pk_add_f32 v[92:93], v[12:13], v[44:45]\n"
    "v_pk_add_f32 v[94:95], v[14:15], v[46:47]\n"
    "s_waitcnt lgkmcnt(3)\n"
    "v_pk_add_f32 v[88:89], v[88:89], v[48:49]\n"
    "v_pk_add_f32 v[90:91], v[90:91], v[50:51]\n"
    "s_waitcnt lgkmcnt(2)\n"
    "v_pk_add_f32 v[92:93], v[92:93], v[52:53]\n"
    "v_pk_add_f32 v[94:95], v[94:95], v[54:55]\n"
    "s_waitcnt lgkmcnt(1)\n"
    "v_pk_add_f32 v[88:89], v[88:89], v[56:57]\n"
    "v_pk_add_f32 v[90:91], v[90:91], v[58:59]\n"
    "s_waitcnt lgkmcnt(0)\n"
    "v_pk_add_f32 v[92:93], v[92:93], v[60:61]\n"
    "v_pk_add_f32 v[94:95], v[94:95], v[62:63]\n"
    "s_setpc_b64 s[98:99]\n"
    "Lred_3_%=:\n"
    "s_waitcnt lgkmcnt(0)\n"
    "s_barrier\n"
    "ds_read_b128 v[40:43], v35 offset:0\n"
    "ds_read_b128 v[44:47], v35 offset:1024\n"
    "ds_read_b128 v[48:51], v35 offset:16384\n"
    "ds_read_b128 v[52:55], v35 offset:17408\n"
    "ds_read_b128 v[56:59], v35 offset:49152\n"
    "ds_read_b128 v[60:63], v35 offset:50176\n"
    "s_waitcnt lgkmcnt(5)\n"
    "v_pk_add_f32 v[88:89], v[16:17], v[40:41]\n"
    "v_pk_add_f32 v[90:91], v[18:19], v[42:43]\n"
    "s_waitcnt lgkmcnt(4)\n"
    "v_pk_add_f32 v[92:93], v[20:21], v[44:45]\n"
    "v_pk_add_f32 v[94:95], v[22:23], v[46:47]\n"
    "s_waitcnt lgkmcnt(3)\n"
    "v_pk_add_f32 v[88:89], v[88:89], v[48:49]\n"
    "v_pk_add_f32 v[90:91], v[90:91], v[50:51]\n"
    "s_waitcnt lgkmcnt(2)\n"
    "v_pk_add_f32 v[92:93], v[92:93], v[52:53]\n"
    "v_pk_add_f32 v[94:95], v[94:95], v[54:55]\n"
    "s_waitcnt lgkmcnt(1)\n"
    "v_pk_add_f32 v[88:89], v[88:89], v[56:57]\n"
    "v_pk_add_f32 v[90:91], v[90:91], v[58:59]\n"
    "s_waitcnt lgkmcnt(0)\n"
    "v_pk_add_f32 v[92:93], v[92:93], v[60:61]\n"
    "v_pk_add_f32 v[94:95], v[94:95], v[62:63]\n"
    "s_setpc_b64 s[98:99]\n"
    "Lred_4_%=:\n"
    "s_waitcnt lgkmcnt(0)\n"
    "s_barrier\n"
    "ds_read_b128 v[40:43], v35 offset:0\n"
    "ds_read_b128 v[44:47], v35 offset:1024\n"
    "ds_read_b128 v[48:51], v35 offset:16384\n"
    "ds_read_b128 v[52:55], v35 offset:17408\n"
    "ds_read_b128 v[56:59], v35 offset:32768\n"
    "ds_read_b128 v[60:63], v35 offset:33792\n"
    "s_waitcnt lgkmcnt(5)\n"
    "v_pk_add_f32 v[88:89], v[24:25], v[40:41]\n"
    "v_pk_add_f32 v[90:91], v[26:27], v[42:43]\n"
    "s_waitcnt lgkmcnt(4)\n"
    "v_pk_add_f32 v[92:93], v[28:29], v[44:45]\n"
    "v_pk_add_f32 v[94:95], v[30:31], v[46:47]\n"
    "s_waitcnt lgkmcnt(3)\n"
    "v_pk_add_f32 v[88:89], v[88:89], v[48:49]\n"
    "v_pk_add_f32 v[90:91], v[90:91], v[50:51]\n"
    "s_waitcnt lgkmcnt(2)\n"
    "v_pk_add_f32 v[92:93], v[92:93], v[52:53]\n"
    "v_pk_add_f32 v[94:95], v[94:95], v[54:55]\n"
    "s_waitcnt lgkmcnt(1)\n"
    "v_pk_add_f32 v[88:89], v[88:89], v[56:57]\n"
    "v_pk_add_f32 v[90:91], v[90:91], v[58:59]\n"
    "s_waitcnt lgkmcnt(0)\n"
    "v_pk_add_f32 v[92:93], v[92:93], v[60:61]\n"
    "v_pk_add_f32 v[94:95], v[94:95], v[62:63]\n"
    "s_setpc_b64 s[98:99]\n"
    "Lred_5_%=:\n"
    "Lstub_11_%=:\n"
    "s_sleep 2\n"
    "buffer_load_dwordx4 v[128:131], v82, s[4:7], s84 offen offset:0 sc1\n"
    "buffer_load_dwordx4 v[132:135], v82, s[4:7], s84 offen offset:1024 sc1\n"
    "buffer_load_dwordx4 v[136:139], v83, s[4:7], s84 offen offset:0 sc1\n"
    "buffer_load_dwordx4 v[140:143], v83, s[4:7], s84 offen offset:1024 sc1\n"
    "s_add_u32 s96, s96, 1\n"
    "s_waitcnt vmcnt(0)\n"
    "v_max_u32 v124, v128, v129\n"
    "v_max3_u32 v124, v124, v130, v131\n"
    "v_max3_u32 v124, v124, v132, v133\n"
    "v_max3_u32 v124, v124, v134, v135\n"
    "v_max3_u32 v124, v124, v136, v137\n"
    "v_max3_u32 v124, v124, v138, v139\n"
    "v_max3_u32 v124, v124, v140, v141\n"
    "v_max3_u32 v124, v124, v142, v143\n"
    "v_cmp_eq_u32 vcc, -1, v124\n"
    "s_cbranch_vccz Lpa_14_%=\n"
    "s_cmp_lt_u32 s96, 65536\n"
    "s_cbranch_scc1 Lstub_11_%=\n"
    "Lpa_14_%=:\n"
    "buffer_load_dwordx4 v[144:147], v82, s[4:7], s85 offen offset:0 sc1\n"
    "buffer_load_dwordx4 v[148:151], v82, s[4:7], s85 offen offset:1024 sc1\n"
    "buffer_load_dwordx4 v[152:155], v83, s[4:7], s85 offen offset:0 sc1\n"
    "buffer_load_dwordx4 v[156:159], v83, s[4:7], s85 offen offset:1024 sc1\n"
    "buffer_load_dwordx4 v[160:163], v82, s[4:7], s86 offen offset:0 sc1\n"
    "buffer_load_dwordx4 v[164:167], v82, s[4:7], s86 offen offset:1024 sc1\n"
    "buffer_load_dwordx4 v[168:171], v83, s[4:7], s86 offen offset:0 sc1\n"
    "buffer_load_dwordx4 v[172:175], v83, s[4:7], s86 offen offset:1024 sc1\n"
    "s_add_u32 s92, s92, 4\n"
    "s_addc_u32 s93, s93, 0\n"
    "s_waitcnt vmcnt(0)\n"
    "s_setpc_b64 s[92:93]\n"
    "Lstub_12_%=:\n"
    "s_sleep 2\n"
    "buffer_load_dwordx4 v[144:147], v82, s[4:7], s85 offen offset:0 sc1\n"
    "buffer_load_dwordx4 v[148:151], v82, s[4:7], s85 offen offset:1024 sc1\n"
    "buffer_load_dwordx4 v[152:155], v83, s[4:7], s85 offen offset:0 sc1\n"
    "buffer_load_dwordx4 v[156:159], v83, s[4:7], s85 offen offset:1024 sc1\n"
    "s_add_u32 s96, s96, 1\n"
    "s_waitcnt vmcnt(0)\n"
    "v_max_u32 v124, v144, v145\n"
    "v_max3_u32 v124, v124, v146, v147\n"
    "v_max3_u32 v124, v124, v148, v149\n"
    "v_max3_u32 v124, v124, v150, v151\n"
    "v_max3_u32 v124, v124, v152, v153\n"
    "v_max3_u32 v124, v124, v154, v155\n"
    "v_max3_u32 v124, v124, v156, v157\n"
    "v_max3_u32 v124, v124, v158, v159\n"
    "v_cmp_eq_u32 vcc, -1, v124\n"
    "s_cbranch_vccz Lpa_15_%=\n"
    "s_cmp_lt_u32 s96, 65536\n"
    "s_cbranch_scc1 Lstub_12_%=\n"
    "Lpa_15_%=:\n"
    "buffer_load_dwordx4 v[128:131], v82, s[4:7], s84 offen offset:0 sc1\n"
    "buffer_load_dwordx4 v[132:135], v82, s[4:7], s84 offen offset:1024 sc1\n"
    "buffer_load_dwordx4 v[136:139], v83, s[4:7], s84 offen offset:0 sc1\n"
    "buffer_load_dwordx4 v[140:143], v83, s[4:7], s84 offen offset:1024 sc1\n"
    "buffer_load_dwordx4 v[160:163], v82, s[4:7], s86 offen offset:0 sc1\n"
    "buffer_load_dwordx4 v[164:167], v82, s[4:7], s86 offen offset:1024 sc1\n"
    "buffer_load_dwordx4 v[168:171], v83, s[4:7], s86 offen offset:0 sc1\n"
    "buffer_load_dwordx4 v[172:175], v83, s[4:7], s86 offen offset:1024 sc1\n"
    "s_add_u32 s92, s92, 4\n"
    "s_addc_u32 s93, s93, 0\n"
    "s_waitcnt vmcnt(0)\n"
    "s_setpc_b64 s[92:93]\n"
    "Lstub_13_%=:\n"
    "s_sleep 2\n"
    "buffer_load_dwordx4 v[160:163], v82, s[4:7], s86 offen offset:0 sc1\n"
    "buffer_load_dwordx4 v[164:167], v82, s[4:7], s86 offen offset:1024 sc1\n"
    "buffer_load_dwordx4 v[168:171], v83, s[4:7], s86 offen offset:0 sc1\n"
    "buffer_load_dwordx4 v[172:175], v83, s[4:7], s86 offen offset:1024 sc1\n"
    "s_add_u32 s96, s96, 1\n"
    "s_waitcnt vmcnt(0)\n"
    "v_max_u32 v124, v160, v161\n"
    "v_max3_u32 v124, v124, v162, v163\n"
    "v_max3_u32 v124, v124, v164, v165\n"
    "v_max3_u32 v124, v124, v166, v167\n"
    "v_max3_u32 v124, v124, v168, v169\n"
    "v_max3_u32 v124, v124, v170, v171\n"
    "v_max3_u32 v124, v124, v172, v173\n"
    "v_max3_u32 v124, v124, v174, v175\n"
    "v_cmp_eq_u32 vcc, -1, v124\n"
    "s_cbranch_vccz Lpa_16_%=\n"
    "s_cmp_lt_u32 s96, 65536\n"
    "s_cbranch_scc1 Lstub_13_%=\n"
    "Lpa_16_%=:\n"
    "buffer_load_dwordx4 v[128:131], v82, s[4:7], s84 offen offset:0 sc1\n"
    "buffer_load_dwordx4 v[132:135], v82, s[4:7], s84 offen offset:1024 sc1\n"
    "buffer_load_dwordx4 v[136:139], v83, s[4:7], s84 offen offset:0 sc1\n"
    "buffer_load_dwordx4 v[140:143], v83, s[4:7], s84 offen offset:1024 sc1\n"
    "buffer_load_dwordx4 v[144:147], v82, s[4:7], s85 offen offset:0 sc1\n"
    "buffer_load_dwordx4 v[148:151], v82, s[4:7], s85 offen offset:1024 sc1\n"
    "buffer_load_dwordx4 v[152:155], v83, s[4:7], s85 offen offset:0 sc1\n"
    "buffer_load_dwordx4 v[156:159], v83, s[4:7], s85 offen offset:1024 sc1\n"
    "s_add_u32 s92, s92, 4\n"
    "s_addc_u32 s93, s93, 0\n"
    "s_waitcnt vmcnt(0)\n"
    "s_setpc_b64 s[92:93]\n"
    "Lno_30_%=:\n"
    "s_getpc_b64 s[92:93]\n"
    "s_branch Lstub_11_%=\n"
    "s_branch Lok_31_%=\n"
    "Lno_32_%=:\n"
    "s_getpc_b64 s[92:93]\n"
    "s_branch Lstub_12_%=\n"
    "s_branch Lok_33_%=\n"
    "Lno_34_%=:\n"
    "s_getpc_b64 s[92:93]\n"
    "s_branch Lstub_13_%=\n"
    "s_branch Lok_35_%=\n"
    "Lno_38_%=:\n"
    "s_getpc_b64 s[92:93]\n"
    "s_branch Lstub_11_%=\n"
    "s_branch Lok_39_%=\n"
    "Lfb_69_%=:\n"
    "s_lshl_b32 s0, s37, 1\n"
    "v_subrev_u32 v89, s0, v89\n"
    "v_mov_b32 v70, 0xff800000\n"
    "s_mov_b32 s0, 2097152\n"
    "Lfa_71_%=:\n"
    "buffer_load_dwordx4 v[52:55], v89, s[4:7], s0 offen sc1\n"
    "s_add_u32 s0, s0, 1024\n"
    "s_waitcnt vmcnt(0)\n"
    "v_mfma_f32_32x32x16_bf16 v[16:31], v[52:55], v[48:51], 0\n"
    "s_nop 11\n"
    "v_max3_f32 v70, v70, v16, v17\n"
    "v_max3_f32 v70, v70, v18, v19\n"
    "v_max3_f32 v70, v70, v20, v21\n"
    "v_max3_f32 v70, v70, v22, v23\n"
    "v_max3_f32 v70, v70, v24, v25\n"
    "v_max3_f32 v70, v70, v26, v27\n"
    "v_max3_f32 v70, v70, v28, v29\n"
    "v_max3_f32 v70, v70, v30, v31\n"
    "s_cmp_lt_u32 s0, 2162688\n"
    "s_cbranch_scc1 Lfa_71_%=\n"
    "v_mov_b32 v71, v70\n"
    "s_nop 1\n"
    "v_permlane32_swap_b32 v70, v71\n"
    "v_max_f32 v70, v70, v71\n"
    "v_mov_b32 v0, 0\n"
    "v_mov_b32 v1, 0\n"
    "v_mov_b32 v2, 0\n"
    "v_mov_b32 v3, 0\n"
    "v_mov_b32 v4, 0\n"
    "v_mov_b32 v5, 0\n"
    "v_mov_b32 v6, 0\n"
    "v_mov_b32 v7, 0\n"
    "v_mov_b32 v8, 0\n"
    "v_mov_b32 v9, 0\n"
    "v_mov_b32 v10, 0\n"
    "v_mov_b32 v11, 0\n"
    "v_mov_b32 v12, 0\n"
    "v_mov_b32 v13, 0\n"
    "v_mov_b32 v14, 0\n"
    "v_mov_b32 v15, 0\n"
    "v_sub_u32 v68, v89, v104\n"
    "v_and_b32 v69, 0x3fff, v113\n"
    "v_add_u32 v68, v68, v69\n"
    "v_and_b32 v69, 27, v102\n"
    "v_cmp_eq_u32 vcc, 16, v69\n"
    "v_mov_b32 v71, 0x3f803f80\n"
    "s_nop 0\n"
    "v_cndmask_b32 v69, 0, v71, vcc\n"
    "v_cmp_lt_u32 s[82:83], 15, v102\n"
    "s_mov_b32 s0, 2097152\n"
    "s_mov_b32 s1, 4194304\n"
    "Lfc_72_%=:\n"
    "buffer_load_dwordx4 v[52:55], v89, s[4:7], s0 offen sc1\n"
    "buffer_load_dwordx4 v[56:59], v68, s[4:7], s1 offen sc1\n"
    "buffer_load_dwordx4 v[60:63], v68, s[4:7], s1 offen offset:512 sc1\n"
    "s_add_u32 s0, s0, 1024\n"
    "s_add_u32 s1, s1, 1024\n"
    "s_waitcnt vmcnt(0)\n"
    "v_mfma_f32_32x32x16_bf16 v[16:31], v[52:55], v[48:51], 0\n"
    "v_cndmask_b32 v56, v56, v69, s[82:83]\n"
    "v_cndmask_b32 v57, v57, v69, s[82:83]\n"
    "v_cndmask_b32 v58, v58, v69, s[82:83]\n"
    "v_cndmask_b32 v59, v59, v69, s[82:83]\n"
    "v_cndmask_b32 v60, v60, v69, s[82:83]\n"
    "v_cndmask_b32 v61, v61, v69, s[82:83]\n"
    "v_cndmask_b32 v62, v62, v69, s[82:83]\n"
    "v_cndmask_b32 v63, v63, v69, s[82:83]\n"
    "s_nop 3\n"
    "v_sub_f32 v16, v16, v70\n"
    "v_sub_f32 v17, v17, v70\n"
    "v_sub_f32 v18, v18, v70\n"
    "v_sub_f32 v19, v19, v70\n"
    "v_sub_f32 v20, v20, v70\n"
    "v_sub_f32 v21, v21, v70\n"
    "v_sub_f32 v22, v22, v70\n"
    "v_sub_f32 v23, v23, v70\n"
    "v_sub_f32 v24, v24, v70\n"
    "v_sub_f32 v25, v25, v70\n"
    "v_sub_f32 v26, v26, v70\n"
    "v_sub_f32 v27, v27, v70\n"
    "v_sub_f32 v28, v28, v70\n"
    "v_sub_f32 v29, v29, v70\n"
    "v_sub_f32 v30, v30, v70\n"
    "v_sub_f32 v31, v31, v70\n"
    "v_exp_f32 v16, v16\n"
    "v_exp_f32 v17, v17\n"
    "v_exp_f32 v18, v18\n"
    "v_exp_f32 v19, v19\n"
    "v_exp_f32 v20, v20\n"
    "v_exp_f32 v21, v21\n"
    "v_exp_f32 v22, v22\n"
    "v_exp_f32 v23, v23\n"
    "v_exp_f32 v24, v24\n"
    "v_exp_f32 v25, v25\n"
    "v_exp_f32 v26, v26\n"
    "v_exp_f32 v27, v27\n"
    "v_exp_f32 v28, v28\n"
    "v_exp_f32 v29, v29\n"
    "v_exp_f32 v30, v30\n"
    "v_exp_f32 v31, v31\n"
    "v_smfmac_f32_32x32x32_bf16 v[0:15], v[56:59], v[16:23], v64\n"
    "s_nop 0\n"
    "v_smfmac_f32_32x32x32_bf16 v[0:15], v[60:63], v[24:31], v64\n"
    "s_cmp_lt_u32 s0, 2162688\n"
    "s_nop 7\n"
    "s_cbranch_scc1 Lfc_72_%=\n"
    "s_nop 7\n"
    "s_branch Lnorm_70_%=\n"
    "Lstub_73_%=:\n"
    "s_sleep 2\n"
    "buffer_load_dwordx4 v[40:43], v32, s[4:7], s84 offen offset:0 sc1\n"
    "buffer_load_dwordx4 v[44:47], v32, s[4:7], s84 offen offset:1024 sc1\n"
    "buffer_load_dwordx4 v[48:51], v33, s[4:7], s84 offen offset:0 sc1\n"
    "buffer_load_dwordx4 v[52:55], v33, s[4:7], s84 offen offset:1024 sc1\n"
    "s_add_u32 s96, s96, 1\n"
    "s_waitcnt vmcnt(0)\n"
    "v_max_u32 v124, v40, v41\n"
    "v_max3_u32 v124, v124, v42, v43\n"
    "v_max3_u32 v124, v124, v44, v45\n"
    "v_max3_u32 v124, v124, v46, v47\n"
    "v_max3_u32 v124, v124, v48, v49\n"
    "v_max3_u32 v124, v124, v50, v51\n"
    "v_max3_u32 v124, v124, v52, v53\n"
    "v_max3_u32 v124, v124, v54, v55\n"
    "v_cmp_eq_u32 vcc, -1, v124\n"
    "s_cbranch_vccz Lpa_75_%=\n"
    "s_cmp_lt_u32 s96, 65536\n"
    "s_cbranch_scc1 Lstub_73_%=\n"
    "Lpa_75_%=:\n"
    "buffer_load_dwordx4 v[56:59], v32, s[4:7], s85 offen offset:0 sc1\n"
    "buffer_load_dwordx4 v[60:63], v32, s[4:7], s85 offen offset:1024 sc1\n"
    "buffer_load_dwordx4 v[64:67], v33, s[4:7], s85 offen offset:0 sc1\n"
    "buffer_load_dwordx4 v[68:71], v33, s[4:7], s85 offen offset:1024 sc1\n"
    "s_add_u32 s92, s92, 4\n"
    "s_addc_u32 s93, s93, 0\n"
    "s_waitcnt vmcnt(0)\n"
    "s_setpc_b64 s[92:93]\n"
    "Lstub_74_%=:\n"
    "s_sleep 2\n"
    "buffer_load_dwordx4 v[56:59], v32, s[4:7], s85 offen offset:0 sc1\n"
    "buffer_load_dwordx4 v[60:63], v32, s[4:7], s85 offen offset:1024 sc1\n"
    "buffer_load_dwordx4 v[64:67], v33, s[4:7], s85 offen offset:0 sc1\n"
    "buffer_load_dwordx4 v[68:71], v33, s[4:7], s85 offen offset:1024 sc1\n"
    "s_add_u32 s96, s96, 1\n"
    "s_waitcnt vmcnt(0)\n"
    "v_max_u32 v124, v56, v57\n"
    "v_max3_u32 v124, v124, v58, v59\n"
    "v_max3_u32 v124, v124, v60, v61\n"
    "v_max3_u32 v124, v124, v62, v63\n"
    "v_max3_u32 v124, v124, v64, v65\n"
    "v_max3_u32 v124, v124, v66, v67\n"
    "v_max3_u32 v124, v124, v68, v69\n"
    "v_max3_u32 v124, v124, v70, v71\n"
    "v_cmp_eq_u32 vcc, -1, v124\n"
    "s_cbranch_vccz Lpa_76_%=\n"
    "s_cmp_lt_u32 s96, 65536\n"
    "s_cbranch_scc1 Lstub_74_%=\n"
    "Lpa_76_%=:\n"
    "buffer_load_dwordx4 v[40:43], v32, s[4:7], s84 offen offset:0 sc1\n"
    "buffer_load_dwordx4 v[44:47], v32, s[4:7], s84 offen offset:1024 sc1\n"
    "buffer_load_dwordx4 v[48:51], v33, s[4:7], s84 offen offset:0 sc1\n"
    "buffer_load_dwordx4 v[52:55], v33, s[4:7], s84 offen offset:1024 sc1\n"
    "s_add_u32 s92, s92, 4\n"
    "s_addc_u32 s93, s93, 0\n"
    "s_waitcnt vmcnt(0)\n"
    "s_setpc_b64 s[92:93]\n"
    "Lno_78_%=:\n"
    "s_getpc_b64 s[92:93]\n"
    "s_branch Lstub_73_%=\n"
    "s_branch Lok_79_%=\n"
    "Lno_80_%=:\n"
    "s_getpc_b64 s[92:93]\n"
    "s_branch Lstub_74_%=\n"
    "s_branch Lok_81_%=\n"
    "Lstub_96_%=:\n"
    "s_sleep 2\n"
    "buffer_load_dwordx4 v[40:43], v32, s[4:7], s84 offen offset:0 sc1\n"
    "buffer_load_dwordx4 v[44:47], v32, s[4:7], s84 offen offset:1024 sc1\n"
    "buffer_load_dwordx4 v[48:51], v33, s[4:7], s84 offen offset:0 sc1\n"
    "buffer_load_dwordx4 v[52:55], v33, s[4:7], s84 offen offset:1024 sc1\n"
    "s_add_u32 s96, s96, 1\n"
    "s_waitcnt vmcnt(0)\n"
    "v_max_u32 v124, v40, v41\n"
    "v_max3_u32 v124, v124, v42, v43\n"
    "v_max3_u32 v124, v124, v44, v45\n"
    "v_max3_u32 v124, v124, v46, v47\n"
    "v_max3_u32 v124, v124, v48, v49\n"
    "v_max3_u32 v124, v124, v50, v51\n"
    "v_max3_u32 v124, v124, v52, v53\n"
    "v_max3_u32 v124, v124, v54, v55\n"
    "v_cmp_eq_u32 vcc, -1, v124\n"
    "s_cbranch_vccz Lpa_99_%=\n"
    "s_cmp_lt_u32 s96, 65536\n"
    "s_cbranch_scc1 Lstub_96_%=\n"
    "Lpa_99_%=:\n"
    "buffer_load_dwordx4 v[56:59], v32, s[4:7], s85 offen offset:0 sc1\n"
    "buffer_load_dwordx4 v[60:63], v32, s[4:7], s85 offen offset:1024 sc1\n"
    "buffer_load_dwordx4 v[64:67], v33, s[4:7], s85 offen offset:0 sc1\n"
    "buffer_load_dwordx4 v[68:71], v33, s[4:7], s85 offen offset:1024 sc1\n"
    "buffer_load_dwordx4 v[72:75], v32, s[4:7], s86 offen offset:0 sc1\n"
    "buffer_load_dwordx4 v[76:79], v32, s[4:7], s86 offen offset:1024 sc1\n"
    "buffer_load_dwordx4 v[80:83], v33, s[4:7], s86 offen offset:0 sc1\n"
    "buffer_load_dwordx4 v[84:87], v33, s[4:7], s86 offen offset:1024 sc1\n"
    "s_add_u32 s92, s92, 4\n"
    "s_addc_u32 s93, s93, 0\n"
    "s_waitcnt vmcnt(0)\n"
    "s_setpc_b64 s[92:93]\n"
    "Lstub_97_%=:\n"
    "s_sleep 2\n"
    "buffer_load_dwordx4 v[56:59], v32, s[4:7], s85 offen offset:0 sc1\n"
    "buffer_load_dwordx4 v[60:63], v32, s[4:7], s85 offen offset:1024 sc1\n"
    "buffer_load_dwordx4 v[64:67], v33, s[4:7], s85 offen offset:0 sc1\n"
    "buffer_load_dwordx4 v[68:71], v33, s[4:7], s85 offen offset:1024 sc1\n"
    "s_add_u32 s96, s96, 1\n"
    "s_waitcnt vmcnt(0)\n"
    "v_max_u32 v124, v56, v57\n"
    "v_max3_u32 v124, v124, v58, v59\n"
    "v_max3_u32 v124, v124, v60, v61\n"
    "v_max3_u32 v124, v124, v62, v63\n"
    "v_max3_u32 v124, v124, v64, v65\n"
    "v_max3_u32 v124, v124, v66, v67\n"
    "v_max3_u32 v124, v124, v68, v69\n"
    "v_max3_u32 v124, v124, v70, v71\n"
    "v_cmp_eq_u32 vcc, -1, v124\n"
    "s_cbranch_vccz Lpa_100_%=\n"
    "s_cmp_lt_u32 s96, 65536\n"
    "s_cbranch_scc1 Lstub_97_%=\n"
    "Lpa_100_%=:\n"
    "buffer_load_dwordx4 v[40:43], v32, s[4:7], s84 offen offset:0 sc1\n"
    "buffer_load_dwordx4 v[44:47], v32, s[4:7], s84 offen offset:1024 sc1\n"
    "buffer_load_dwordx4 v[48:51], v33, s[4:7], s84 offen offset:0 sc1\n"
    "buffer_load_dwordx4 v[52:55], v33, s[4:7], s84 offen offset:1024 sc1\n"
    "buffer_load_dwordx4 v[72:75], v32, s[4:7], s86 offen offset:0 sc1\n"
    "buffer_load_dwordx4 v[76:79], v32, s[4:7], s86 offen offset:1024 sc1\n"
    "buffer_load_dwordx4 v[80:83], v33, s[4:7], s86 offen offset:0 sc1\n"
    "buffer_load_dwordx4 v[84:87], v33, s[4:7], s86 offen offset:1024 sc1\n"
    "s_add_u32 s92, s92, 4\n"
    "s_addc_u32 s93, s93, 0\n"
    "s_waitcnt vmcnt(0)\n"
    "s_setpc_b64 s[92:93]\n"
    "Lstub_98_%=:\n"
    "s_sleep 2\n"
    "buffer_load_dwordx4 v[72:75], v32, s[4:7], s86 offen offset:0 sc1\n"
    "buffer_load_dwordx4 v[76:79], v32, s[4:7], s86 offen offset:1024 sc1\n"
    "buffer_load_dwordx4 v[80:83], v33, s[4:7], s86 offen offset:0 sc1\n"
    "buffer_load_dwordx4 v[84:87], v33, s[4:7], s86 offen offset:1024 sc1\n"
    "s_add_u32 s96, s96, 1\n"
    "s_waitcnt vmcnt(0)\n"
    "v_max_u32 v124, v72, v73\n"
    "v_max3_u32 v124, v124, v74, v75\n"
    "v_max3_u32 v124, v124, v76, v77\n"
    "v_max3_u32 v124, v124, v78, v79\n"
    "v_max3_u32 v124, v124, v80, v81\n"
    "v_max3_u32 v124, v124, v82, v83\n"
    "v_max3_u32 v124, v124, v84, v85\n"
    "v_max3_u32 v124, v124, v86, v87\n"
    "v_cmp_eq_u32 vcc, -1, v124\n"
    "s_cbranch_vccz Lpa_101_%=\n"
    "s_cmp_lt_u32 s96, 65536\n"
    "s_cbranch_scc1 Lstub_98_%=\n"
    "Lpa_101_%=:\n"
    "buffer_load_dwordx4 v[40:43], v32, s[4:7], s84 offen offset:0 sc1\n"
    "buffer_load_dwordx4 v[44:47], v32, s[4:7], s84 offen offset:1024 sc1\n"
    "buffer_load_dwordx4 v[48:51], v33, s[4:7], s84 offen offset:0 sc1\n"
    "buffer_load_dwordx4 v[52:55], v33, s[4:7], s84 offen offset:1024 sc1\n"
    "buffer_load_dwordx4 v[56:59], v32, s[4:7], s85 offen offset:0 sc1\n"
    "buffer_load_dwordx4 v[60:63], v32, s[4:7], s85 offen offset:1024 sc1\n"
    "buffer_load_dwordx4 v[64:67], v33, s[4:7], s85 offen offset:0 sc1\n"
    "buffer_load_dwordx4 v[68:71], v33, s[4:7], s85 offen offset:1024 sc1\n"
    "s_add_u32 s92, s92, 4\n"
    "s_addc_u32 s93, s93, 0\n"
    "s_waitcnt vmcnt(0)\n"
    "s_setpc_b64 s[92:93]\n"
    "Lno_106_%=:\n"
    "s_getpc_b64 s[92:93]\n"
    "s_branch Lstub_96_%=\n"
    "s_branch Lok_107_%=\n"
    "Lno_108_%=:\n"
    "s_getpc_b64 s[92:93]\n"
    "s_branch Lstub_97_%=\n"
    "s_branch Lok_109_%=\n"
    "Lno_110_%=:\n"
    "s_getpc_b64 s[92:93]\n"
    "s_branch Lstub_98_%=\n"
    "s_branch Lok_111_%=\n"
    "Lno_112_%=:\n"
    "s_getpc_b64 s[92:93]\n"
    "s_branch Lstub_96_%=\n"
    "s_branch Lok_113_%=\n"
    "Lno_114_%=:\n"
    "s_getpc_b64 s[92:93]\n"
    "s_branch Lstub_97_%=\n"
    "s_branch Lok_115_%=\n"
    "Lno_116_%=:\n"
    "s_getpc_b64 s[92:93]\n"
    "s_branch Lstub_98_%=\n"
    "s_branch Lok_117_%=\n"
    "Lno_118_%=:\n"
    "s_getpc_b64 s[92:93]\n"
    "s_branch Lstub_96_%=\n"
    "s_branch Lok_119_%=\n"
    "Lno_120_%=:\n"
    "s_getpc_b64 s[92:93]\n"
    "s_branch Lstub_97_%=\n"
    "s_branch Lok_121_%=\n"
    "Lstub_126_%=:\n"
    "s_sleep 2\n"
    "buffer_load_dwordx4 v[40:43], v32, s[4:7], s84 offen offset:0 sc1\n"
    "buffer_load_dwordx4 v[44:47], v32, s[4:7], s84 offen offset:1024 sc1\n"
    "buffer_load_dwordx4 v[48:51], v33, s[4:7], s84 offen offset:0 sc1\n"
    "buffer_load_dwordx4 v[52:55], v33, s[4:7], s84 offen offset:1024 sc1\n"
    "s_add_u32 s96, s96, 1\n"
    "s_waitcnt vmcnt(0)\n"
    "v_max_u32 v124, v40, v41\n"
    "v_max3_u32 v124, v124, v42, v43\n"
    "v_max3_u32 v124, v124, v44, v45\n"
    "v_max3_u32 v124, v124, v46, v47\n"
    "v_max3_u32 v124, v124, v48, v49\n"
    "v_max3_u32 v124, v124, v50, v51\n"
    "v_max3_u32 v124, v124, v52, v53\n"
    "v_max3_u32 v124, v124, v54, v55\n"
    "v_cmp_eq_u32 vcc, -1, v124\n"
    "s_cbranch_vccz Lpa_129_%=\n"
    "s_cmp_lt_u32 s96, 65536\n"
    "s_cbranch_scc1 Lstub_126_%=\n"
    "Lpa_129_%=:\n"
    "buffer_load_dwordx4 v[56:59], v32, s[4:7], s85 offen offset:0 sc1\n"
    "buffer_load_dwordx4 v[60:63], v32, s[4:7], s85 offen offset:1024 sc1\n"
    "buffer_load_dwordx4 v[64:67], v33, s[4:7], s85 offen offset:0 sc1\n"
    "buffer_load_dwordx4 v[68:71], v33, s[4:7], s85 offen offset:1024 sc1\n"
    "buffer_load_dwordx4 v[72:75], v32, s[4:7], s86 offen offset:0 sc1\n"
    "buffer_load_dwordx4 v[76:79], v32, s[4:7], s86 offen offset:1024 sc1\n"
    "buffer_load_dwordx4 v[80:83], v33, s[4:7], s86 offen offset:0 sc1\n"
    "buffer_load_dwordx4 v[84:87], v33, s[4:7], s86 offen offset:1024 sc1\n"
    "s_add_u32 s92, s92, 4\n"
    "s_addc_u32 s93, s93, 0\n"
    "s_waitcnt vmcnt(0)\n"
    "s_setpc_b64 s[92:93]\n"
    "Lstub_127_%=:\n"
    "s_sleep 2\n"
    "buffer_load_dwordx4 v[56:59], v32, s[4:7], s85 offen offset:0 sc1\n"
    "buffer_load_dwordx4 v[60:63], v32, s[4:7], s85 offen offset:1024 sc1\n"
    "buffer_load_dwordx4 v[64:67], v33, s[4:7], s85 offen offset:0 sc1\n"
    "buffer_load_dwordx4 v[68:71], v33, s[4:7], s85 offen offset:1024 sc1\n"
    "s_add_u32 s96, s96, 1\n"
    "s_waitcnt vmcnt(0)\n"
    "v_max_u32 v124, v56, v57\n"
    "v_max3_u32 v124, v124, v58, v59\n"
    "v_max3_u32 v124, v124, v60, v61\n"
    "v_max3_u32 v124, v124, v62, v63\n"
    "v_max3_u32 v124, v124, v64, v65\n"
    "v_max3_u32 v124, v124, v66, v67\n"
    "v_max3_u32 v124, v124, v68, v69\n"
    "v_max3_u32 v124, v124, v70, v71\n"
    "v_cmp_eq_u32 vcc, -1, v124\n"
    "s_cbranch_vccz Lpa_130_%=\n"
    "s_cmp_lt_u32 s96, 65536\n"
    "s_cbranch_scc1 Lstub_127_%=\n"
    "Lpa_130_%=:\n"
    "buffer_load_dwordx4 v[40:43], v32, s[4:7], s84 offen offset:0 sc1\n"
    "buffer_load_dwordx4 v[44:47], v32, s[4:7], s84 offen offset:1024 sc1\n"
    "buffer_load_dwordx4 v[48:51], v33, s[4:7], s84 offen offset:0 sc1\n"
    "buffer_load_dwordx4 v[52:55], v33, s[4:7], s84 offen offset:1024 sc1\n"
    "buffer_load_dwordx4 v[72:75], v32, s[4:7], s86 offen offset:0 sc1\n"
    "buffer_load_dwordx4 v[76:79], v32, s[4:7], s86 offen offset:1024 sc1\n"
    "buffer_load_dwordx4 v[80:83], v33, s[4:7], s86 offen offset:0 sc1\n"
    "buffer_load_dwordx4 v[84:87], v33, s[4:7], s86 offen offset:1024 sc1\n"
    "s_add_u32 s92, s92, 4\n"
    "s_addc_u32 s93, s93, 0\n"
    "s_waitcnt vmcnt(0)\n"
    "s_setpc_b64 s[92:93]\n"
    "Lstub_128_%=:\n"
    "s_sleep 2\n"
    "buffer_load_dwordx4 v[72:75], v32, s[4:7], s86 offen offset:0 sc1\n"
    "buffer_load_dwordx4 v[76:79], v32, s[4:7], s86 offen offset:1024 sc1\n"
    "buffer_load_dwordx4 v[80:83], v33, s[4:7], s86 offen offset:0 sc1\n"
    "buffer_load_dwordx4 v[84:87], v33, s[4:7], s86 offen offset:1024 sc1\n"
    "s_add_u32 s96, s96, 1\n"
    "s_waitcnt vmcnt(0)\n"
    "v_max_u32 v124, v72, v73\n"
    "v_max3_u32 v124, v124, v74, v75\n"
    "v_max3_u32 v124, v124, v76, v77\n"
    "v_max3_u32 v124, v124, v78, v79\n"
    "v_max3_u32 v124, v124, v80, v81\n"
    "v_max3_u32 v124, v124, v82, v83\n"
    "v_max3_u32 v124, v124, v84, v85\n"
    "v_max3_u32 v124, v124, v86, v87\n"
    "v_cmp_eq_u32 vcc, -1, v124\n"
    "s_cbranch_vccz Lpa_131_%=\n"
    "s_cmp_lt_u32 s96, 65536\n"
    "s_cbranch_scc1 Lstub_128_%=\n"
    "Lpa_131_%=:\n"
    "buffer_load_dwordx4 v[40:43], v32, s[4:7], s84 offen offset:0 sc1\n"
    "buffer_load_dwordx4 v[44:47], v32, s[4:7], s84 offen offset:1024 sc1\n"
    "buffer_load_dwordx4 v[48:51], v33, s[4:7], s84 offen offset:0 sc1\n"
    "buffer_load_dwordx4 v[52:55], v33, s[4:7], s84 offen offset:1024 sc1\n"
    "buffer_load_dwordx4 v[56:59], v32, s[4:7], s85 offen offset:0 sc1\n"
    "buffer_load_dwordx4 v[60:63], v32, s[4:7], s85 offen offset:1024 sc1\n"
    "buffer_load_dwordx4 v[64:67], v33, s[4:7], s85 offen offset:0 sc1\n"
    "buffer_load_dwordx4 v[68:71], v33, s[4:7], s85 offen offset:1024 sc1\n"
    "s_add_u32 s92, s92, 4\n"
    "s_addc_u32 s93, s93, 0\n"
    "s_waitcnt vmcnt(0)\n"
    "s_setpc_b64 s[92:93]\n"
    "Lno_138_%=:\n"
    "s_getpc_b64 s[92:93]\n"
    "s_branch Lstub_126_%=\n"
    "s_branch Lok_139_%=\n"
    "Lno_140_%=:\n"
    "s_getpc_b64 s[92:93]\n"
    "s_branch Lstub_127_%=\n"
    "s_branch Lok_141_%=\n"
    "Lno_142_%=:\n"
    "s_getpc_b64 s[92:93]\n"
    "s_branch Lstub_128_%=\n"
    "s_branch Lok_143_%=\n"
    "Lno_144_%=:\n"
    "s_getpc_b64 s[92:93]\n"
    "s_branch Lstub_126_%=\n"
    "s_branch Lok_145_%=\n"
    "Lno_146_%=:\n"
    "s_getpc_b64 s[92:93]\n"
    "s_branch Lstub_127_%=\n"
    "s_branch Lok_147_%=\n"
    "Lno_148_%=:\n"
    "s_getpc_b64 s[92:93]\n"
    "s_branch Lstub_128_%=\n"
    "s_branch Lok_149_%=\n"
    "Lno_152_%=:\n"
    "s_getpc_b64 s[92:93]\n"
    "s_branch Lstub_126_%=\n"
    "s_branch Lok_153_%=\n"
    "Lno_158_%=:\n"
    "s_getpc_b64 s[92:93]\n"
    "s_branch Lstub_126_%=\n"
    "s_branch Lok_159_%=\n"
    "Lcend_7_%=:\n"
    "Lfin_166_%=:\n"
        : "+{v[0:3]}"(xin), "+{v[4:5]}"(epoch), "+{v[26:27]}"(sink), "+{s[68:71]}"(sE), "+{s[20:35]}"(sB), "+{s78}"(sb16)
        : "{s[4:19]}"(sA), "{s[36:51]}"(sC), "{s[52:67]}"(sD), "{s[74:75]}"(sF), "{s81}"(w1 * 1024), "{s79}"(b1so), "{s80}"(b2so), "{s76}"((ti * 16 + (w & 1) * 8) * D * 2),
          "{s77}"((2 + (qt >> 2) * 2) * MB + (head * 64 + (qt & 3) * 4 + kq1) * 1024),
          "{v[104:107]}"(vA), "{v[108:111]}"(vB), "{v[112:115]}"(vC), "{v125}"(vD)
        : "v6", "v7", "v8", "v9", "v10", "v11", "v12", "v13", "v14", "v15", "v16", "v17", "v18", "v19", "v20", "v21", "v22", "v23", "v24", "v25", "v28", "v29", "v30", "v31", "v32", "v33", "v34", "v35", "v36", "v37", "v38", "v39", "v40", "v41", "v42", "v43", "v44", "v45", "v46", "v47", "v48", "v49", "v50", "v51", "v52", "v53", "v54", "v55", "v56", "v57", "v58", "v59", "v60", "v61", "v62", "v63", "v64", "v65", "v66", "v67", "v68", "v69", "v70", "v71", "v72", "v73", "v74", "v75", "v76", "v77", "v78", "v79", "v80", "v81", "v82", "v83", "v84", "v85", "v86", "v87", "v88", "v89", "v90", "v91", "v92", "v93", "v94", "v95", "v96", "v97", "v98", "v99", "v100", "v101", "v102", "v103", "v116", "v117", "v118", "v119", "v120", "v121", "v122", "v123", "v124", "v128", "v129", "v130", "v131", "v132", "v133", "v134", "v135", "v136", "v137", "v138", "v139", "v140", "v141", "v142", "v143", "v144", "v145", "v146", "v147", "v148", "v149", "v150", "v151", "v152", "v153", "v154", "v155", "v156", "v157", "v158", "v159", "v160", "v161", "v162", "v163", "v164", "v165", "v166", "v167", "v168", "v169", "v170", "v171", "v172", "v173", "v174", "v175", "v176", "v177", "v178", "v179", "v180", "v181", "v182", "v183", "v184", "v185", "v186", "v187", "v188", "v189", "v190", "v191", "v192", "v193", "v194", "v195", "v196", "v197", "v198", "v199", "v200", "v201", "v202", "v203", "v204", "v205", "v206", "v207", "v208", "v209", "v210", "v211", "v212", "v213", "v214", "v215", "v216", "v217", "v218", "v219", "v220", "v221", "v222", "v223", "v224", "v225", "v226", "v227", "v228", "v229", "v230", "v231", "v232", "v233", "v234", "v235", "v236", "v237", "v238", "v239", "v240", "v241", "v242", "v243", "v244", "v245", "v246", "v247", "v248", "v249", "v250", "v251", "v252", "v253", "v254", "v255", "s0", "s1", "s2", "s3", "s72", "s73", "s82", "s83", "s84", "s85", "s86", "s87", "s88", "s89", "s90", "s91", "s92", "s93", "s94", "s95", "s96", "s97", "s98", "s99"
          , "vcc", "scc", "memory");
}

extern "C" void kernel_launch(void* const* d_in, const int* in_sizes, int n_in, void* d_out, int out_size, void* d_ws, size_t ws_size, hipStream_t stream) {
    Params P;
    P.x = (const u16*)d_in[0]; P.wqkv = (const u16*)d_in[1]; P.wo = (const u16*)d_in[2];
    P.w1 = (const u16*)d_in[4]; P.b1 = (const u16*)d_in[5]; P.w2 = (const u16*)d_in[6]; P.b2 = (const u16*)d_in[7];
    P.out = (u16*)d_out; P.ws = (char*)d_ws;
    (void)hipFuncSetAttribute((const void*)fused_kernel, hipFuncAttributeMaxDynamicSharedMemorySize, LDS_BYTES);
    fused_kernel<<<256, 512, LDS_BYTES, stream>>>(P);
}
```
